```python
import torch

def _softmax(x, axis=-1):
    return torch.softmax(x, dim=axis)
def _gelu(x):
    return torch.nn.functional.gelu(x, approximate="tanh")
def _silu(x):
    return torch.nn.functional.silu(x)
def _sigmoid(x):
    return torch.sigmoid(x)
def _layernorm(x, axis=-1, eps=1e-5):
    m = x.mean(dim=axis, keepdim=True)
    v = x.var(dim=axis, keepdim=True, unbiased=False)
    return (x - m) / torch.sqrt(v + eps)
def _scatter_add(tgt, idx, src, axis=0):
    return tgt.clone().index_add_(axis, idx, src)
def _segment_sum(src, seg, n):
    out = torch.zeros((n,) + src.shape[1:], dtype=src.dtype)
    return out.index_add_(0, seg, src)
def _pad(x, pw):
    flat = tuple(v for pair in reversed(pw) for v in pair)
    return torch.nn.functional.pad(x, flat)

kernel_name = 'gated_mix__ssm_conv_s4__grouped_kv_mqa__prompt'

def setup_inputs():

    _w = (lambda a: (lambda t: t.to(torch.bfloat16) if t.is_floating_point() else t)(torch.as_tensor(a)))
    def _A_setup():
        B, L, D, N = 1, 2048, 640, 128
        A_re = -(lambda _x: torch.exp(torch.as_tensor(_x)))((lambda *s: torch.randn(*s, dtype=torch.bfloat16) * 3.2)(D, N))
        A_im = torch.pi * (lambda *s: torch.randn(*s, dtype=torch.bfloat16) * 0.05)(D, N)
        B_re = (lambda *s: torch.randn(*s, dtype=torch.bfloat16) * 0.1)(D, N)
        B_im = (lambda *s: torch.randn(*s, dtype=torch.bfloat16) * 0.1)(D, N)
        C_re = (lambda *s: torch.randn(*s, dtype=torch.bfloat16) * 0.1)(D, N)
        C_im = (lambda *s: torch.randn(*s, dtype=torch.bfloat16) * 0.1)(D, N)
        log_dt = (lambda *s: torch.randn(*s, dtype=torch.bfloat16) * 0.05)(D) * 0.5 - 4.0
        Dskip = (lambda *s: torch.randn(*s, dtype=torch.bfloat16) * 0.05)(D)
        return {
            "u": _w((lambda *s: torch.randn(*s, dtype=torch.bfloat16) * 0.05)(B, L, D)),
            "A_re": _w(A_re),
            "A_im": _w(A_im),
            "B_re": _w(B_re),
            "B_im": _w(B_im),
            "C_re": _w(C_re),
            "C_im": _w(C_im),
            "log_dt": _w(log_dt),
            "Dskip": _w(Dskip),
        }
    _A_in = _A_setup()

    def _B_setup():
        B, S, D, H = 1, 2048, 640, 32
        Dh = D // H
        return {
            "x": _w((lambda *s: torch.randn(*s, dtype=torch.bfloat16) * 0.05)(B, S, D)),
            "wq": _w((lambda *s: torch.randn(*s, dtype=torch.bfloat16) * 0.8)(D, H * Dh)),
            "wk": _w((lambda *s: torch.randn(*s, dtype=torch.bfloat16) * 0.8)(D, Dh)),
            "wv": _w((lambda *s: torch.randn(*s, dtype=torch.bfloat16) * 0.05)(D, Dh)),
            "wo": _w((lambda *s: torch.randn(*s, dtype=torch.bfloat16) * 0.05)(H * Dh, D)),
        }
    _B_in = _B_setup()

    return {
        "A_u": _A_in["u"],
        "A_A_re": _A_in["A_re"],
        "A_A_im": _A_in["A_im"],
        "A_B_re": _A_in["B_re"],
        "A_B_im": _A_in["B_im"],
        "A_C_re": _A_in["C_re"],
        "A_C_im": _A_in["C_im"],
        "A_log_dt": _A_in["log_dt"],
        "A_Dskip": _A_in["Dskip"],
        "B_x": _B_in["x"],
        "B_wq": _B_in["wq"],
        "B_wk": _B_in["wk"],
        "B_wv": _B_in["wv"],
        "B_wo": _B_in["wo"],
        "gate_w": _w((lambda *s: torch.randn(*s, dtype=torch.bfloat16) * 0.05)(640) * 16.0),
    }

def reference(A_u, A_A_re, A_A_im, A_B_re, A_B_im, A_C_re, A_C_im, A_log_dt, A_Dskip, B_x, B_wq, B_wk, B_wv, B_wo, gate_w):
    def _A_ref(u, A_re, A_im, B_re, B_im, C_re, C_im, log_dt, Dskip):
        Bsz, L, D = u.shape
        dt = torch.exp(log_dt)[:, None]
        A = A_re + 1j * A_im
        Bc = B_re + 1j * B_im
        Cc = C_re + 1j * C_im
        logdA = dt * A
        dB = dt * Bc
        l = (lambda x, dt: x.to(dt))(torch.arange(L), A_re.dtype)
        P = torch.exp(logdA[:, :, None] * l[None, None, :])
        K = torch.einsum("dn,dnl,dn->dl", Cc, P, dB).real
        L2 = 2 * L
        Kp = _pad(K, ((0, 0), (0, L)))
        up = (lambda x, *d: x.permute(*d))(u, 0, 2, 1)
        up = _pad(up, ((0, 0), (0, 0), (0, L)))
        Kf = (lambda x, axes=(-2,-1): torch.fft.rfft2(x.float(), dim=axes))(Kp[:, None, :], axes=(-2, -1))
        Uf = (lambda x, axes=(-2,-1): torch.fft.rfft2(x.float(), dim=axes))(up[:, :, None, :], axes=(-2, -1))
        Yf = Uf * Kf[None, :, :, :]
        y = (lambda x, s, axes=(-2,-1): torch.fft.irfft2(x, s=s, dim=axes).to(torch.bfloat16))(Yf, (1, L2), axes=(-2, -1))[:, :, 0, :L]
        y = (lambda x, *d: x.permute(*d))(y, 0, 2, 1) + u * Dskip[None, None, :]
        return y

    def _B_ref(x, wq, wk, wv, wo):
        B, S, D = x.shape
        H = 32
        Dh = D // H
        q = (lambda x, *d: x.permute(*d))((x @ wq).reshape(B, S, H, Dh), 0, 2, 1, 3)
        k = x @ wk
        v = x @ wv
        scores = torch.einsum("bhqd,bkd->bhqk", q, k) * (Dh ** -0.5)
        p = _softmax(scores, axis=-1)
        o = torch.einsum("bhqk,bkd->bhqd", p, v)
        return (lambda x, *d: x.permute(*d))(o, 0, 2, 1, 3).reshape(B, S, H * Dh) @ wo

    x = A_u
    g = _sigmoid((x * gate_w).sum(-1, keepdims=True))
    a = _A_ref(x, A_A_re, A_A_im, A_B_re, A_B_im, A_C_re, A_C_im, A_log_dt, A_Dskip)
    b = _B_ref(x, B_wq, B_wk, B_wv, B_wo)
    return g * a + (1 - g) * b
```

```cpp
#include <hip/hip_runtime.h>
#include <hip/hip_bf16.h>
typedef unsigned short u16;
__device__ inline float b2f(u16 v){ return __uint_as_float(((unsigned)v)<<16); }
__device__ inline u16 f2b(float f){ unsigned u=__float_as_uint(f); return (u16)((u+0x7fff+((u>>16)&1))>>16); }
constexpr int L=2048,D=640,N=128,H=32,DH=20;
__global__ void kgen(const u16*Are,const u16*Aim,const u16*Bre,const u16*Bim,const u16*Cre,const u16*Cim,const u16*logdt,float*K){
  int d=blockIdx.x;
  float dt=b2f(f2b(expf(b2f(logdt[d]))));
  for(int l=threadIdx.x;l<L;l+=blockDim.x){
    float lf=b2f(f2b((float)l)); float s=0;
    for(int n=0;n<N;n++){
      float zr=dt*b2f(Are[d*N+n]), zi=dt*b2f(Aim[d*N+n]);
      float br=dt*b2f(Bre[d*N+n]), bi=dt*b2f(Bim[d*N+n]);
      float cr=b2f(Cre[d*N+n]), ci=b2f(Cim[d*N+n]);
      float wr=cr*br-ci*bi, wi=cr*bi+ci*br;
      float e=expf(zr*lf), c=cosf(zi*lf), sn=sinf(zi*lf);
      s+=e*(wr*c-wi*sn);
    }
    K[d*L+l]=s;
  }
}
__global__ void conv(const float*K,const u16*u,float*y){
  int l=blockIdx.x, d=threadIdx.x; float s=0;
  for(int j=0;j<=l;j++) s+=K[d*L+l-j]*b2f(u[j*D+d]);
  y[l*D+d]=s;
}
__global__ void proj(const u16*x,const u16*wq,const u16*wk,const u16*wv,float*q,float*k,float*v){
  int l=blockIdx.x, c=threadIdx.x;
  if(c>=680) return;
  float s=0;
  for(int j=0;j<D;j++){ float xv=b2f(x[l*D+j]); float w = c<640? b2f(wq[j*D+c]) : (c<660? b2f(wk[j*DH+c-640]) : b2f(wv[j*DH+c-660])); s+=xv*w; }
  s=b2f(f2b(s));
  if(c<640) q[l*D+c]=s; else if(c<660) k[l*DH+c-640]=s; else v[l*DH+c-660]=s;
}
__global__ void attn(const float*q,const float*k,const float*v,float*o){
  int idx=blockIdx.x*blockDim.x+threadIdx.x; int l=idx/H,h=idx%H;
  float qq[DH],acc[DH]; for(int i=0;i<DH;i++){qq[i]=q[l*D+h*DH+i]*0.22360679775f; acc[i]=0;}
  float m=-1e30f,sum=0;
  for(int j=0;j<L;j++){ float s=0; for(int i=0;i<DH;i++) s+=qq[i]*k[j*DH+i];
    float mn=fmaxf(m,s); float al=expf(m-mn), p=expf(s-mn); sum=sum*al+p; for(int i=0;i<DH;i++) acc[i]=acc[i]*al+p*v[j*DH+i]; m=mn; }
  for(int i=0;i<DH;i++) o[l*D+h*DH+i]=b2f(f2b(acc[i]/sum));
}
__global__ void fin(const float*o,const u16*wo,const float*y,const u16*x,const u16*Dsk,const u16*gw,u16*out){
  int l=blockIdx.x,d=threadIdx.x;
  __shared__ float red[640];
  red[d]=b2f(x[l*D+d])*b2f(gw[d]); __syncthreads();
  float gs=0; for(int i=0;i<D;i++) gs+=red[i];
  float g=1.f/(1.f+expf(-gs));
  float b=0; for(int j=0;j<D;j++) b+=o[l*D+j]*b2f(wo[j*D+d]);
  float a=y[l*D+d]+b2f(x[l*D+d])*b2f(Dsk[d]);
  out[l*D+d]=f2b(g*a+(1-g)*b);
}
extern "C" void kernel_launch(void* const* d_in, const int* in_sizes, int n_in, void* d_out, int out_size, void* d_ws, size_t ws_size, hipStream_t stream){
  const u16* const* in=(const u16* const*)d_in;
  float* ws=(float*)d_ws; float*K=ws; float*y=K+D*L; float*q=y+L*D; float*k=q+L*D; float*v=k+L*DH; float*o=v+L*DH;
  kgen<<<D,256,0,stream>>>(in[1],in[2],in[3],in[4],in[5],in[6],in[7],K);
  conv<<<L,D,0,stream>>>(K,in[0],y);
  proj<<<L,704,0,stream>>>(in[0],in[10],in[11],in[12],q,k,v);
  attn<<<L*H/256,256,0,stream>>>(q,k,v,o);
  fin<<<L,D,0,stream>>>(o,in[13],y,in[0],in[8],in[14],(u16*)d_out);
}
```

```cpp
#include <hip/hip_runtime.h>

struct Params {
  const void *x, *are, *aim, *bre, *bim, *cre, *cim, *logdt, *dskip, *wq, *wk, *wv, *wo, *gw;
  void *out, *ws;
};
constexpr unsigned WS_END = 12648448u, WS_DBG = 16777216u;

__global__ __launch_bounds__(512, 2) void fused_kernel(Params P) {
  asm volatile(
    "s_getpc_b64 s[96:97]\n"
    ".Lkbeg_1_%=:\n"
    "s_load_dwordx16 s[48:63], s[0:1], 0x0\n"
    "s_load_dwordx16 s[64:79], s[0:1], 0x40\n"
    "v_lshrrev_b32 v237, 6, v0\n"
    "v_and_b32 v230, 63, v0\n"
    "v_mov_b32 v245, 0\n"
    "v_readfirstlane_b32 s24, v237\n"
    "s_mov_b32 s84, s96\n"
    "s_and_b32 s85, s97, 0xffff\n"
    "s_mov_b32 s86, .Lkend_2_%=-.Lkbeg_1_%=\n"
    "s_mov_b32 s87, 0x20000\n"
    "s_and_b32 s38, s2, 0xf8\n"
    "s_lshl_b32 s38, s38, 8\n"
    "v_lshlrev_b32 v238, 2, v0\n"
    "buffer_load_dword v219, v238, s[84:87], s38 offen\n"
    "v_mov_b32 v235, 1\n"
    "v_and_b32 v231, 15, v0\n"
    "v_bfe_u32 v232, v0, 4, 2\n"
    "v_and_b32 v233, 31, v0\n"
    "v_bfe_u32 v234, v0, 5, 1\n"
    "v_lshlrev_b32 v247, 4, v230\n"
    "v_lshlrev_b32 v237, 2, v231\n"
    "v_mov_b32 v255, 0\n"
    "v_mov_b32 v254, 2147483392\n"
    "v_cmp_eq_u32 vcc, 0, v230\n"
    "s_mov_b32 s80, 4194304\n"
    "ds_write_b32 v237, v245\n"
    "v_cndmask_b32 v254, v254, v245, vcc\n"
    "s_and_b32 s27, s2, 7\n"
    "s_lshr_b32 s28, s2, 3\n"
    "s_lshr_b32 s38, s2, 6\n"
    "s_lshl_b32 s25, s27, 2\n"
    "s_add_u32 s25, s25, s38\n"
    "s_and_b32 s26, s28, 7\n"
    "s_lshl_b32 s29, s25, 6\n"
    "s_lshr_b32 s30, s26, 1\n"
    "s_and_b32 s31, s28, 1\n"
    "s_lshr_b32 s38, s28, 1\n"
    "s_mul_i32 s38, s38, 5\n"
    "s_mul_i32 s83, s27, 80\n"
    "s_add_u32 s83, s83, s38\n"
    "s_sub_u32 s33, s24, s30\n"
    "s_and_b32 s33, s33, 3\n"
    "v_xor_b32 v237, v231, v232\n"
    "v_mul_u32_u24 v236, 1280, v232\n"
    "v_lshl_add_u32 v236, v237, 4, v236\n"
    "v_add_u32 v236, 1024, v236\n"
    "v_mul_u32_u24 v237, 26, v230\n"
    "v_lshrrev_b32 v237, 8, v237\n"
    "v_mul_u32_u24 v237, 1120, v237\n"
    "v_add_u32 v248, v237, v247\n"
    "s_mul_i32 s38, s29, 1280\n"
    "s_mul_i32 s39, s26, 160\n"
    "s_lshl_b32 s40, s30, 4\n"
    "s_lshl_b32 s41, s24, 1\n"
    "s_add_u32 s40, s40, s41\n"
    "s_mul_i32 s41, s40, 1280\n"
    "s_mul_i32 s40, s40, 1040\n"
    "s_add_u32 s40, s40, 79872\n"
    "s_mul_i32 s42, s24, 3072\n"
    "s_cmp_lt_u32 s24, 4\n"
    "s_cbranch_scc1 .LkL_3_%=\n"
    "s_setprio 3\n"
    ".LkL_3_%=:\n"
    "s_waitcnt lgkmcnt(0)\n"
    "s_add_u32 s4, s48, s38\n"
    "s_addc_u32 s5, s49, 0\n"
    "s_and_b32 s5, s5, 0xffff\n"
    "s_mov_b32 s6, 81920\n"
    "s_mov_b32 s7, 0x20000\n"
    "s_mov_b32 m0, s40\n"
    "s_bitcmp1_b32 s26, 0\n"
    "s_cselect_b32 s16, s70, s68\n"
    "s_cselect_b32 s17, s71, s69\n"
    "buffer_load_dwordx4 v247, s[4:7], s41 offen lds\n"
    "s_add_u32 m0, m0, 1040\n"
    "s_add_u32 s41, s41, 1280\n"
    "s_and_b32 s17, s17, 0xffff\n"
    "s_mov_b32 s18, 25600\n"
    "s_mov_b32 s19, 0x20000\n"
    "buffer_load_dwordx4 v247, s[4:7], s41 offen lds\n"
    "s_add_u32 m0, s42, 46592\n"
    "s_nop 0\n"
    "buffer_load_dwordx4 v247, s[16:19], s42 offen lds\n"
    "buffer_load_dwordx4 v247, s[16:19], s42 offen offset:1024 lds\n"
    "buffer_load_dwordx4 v247, s[16:19], s42 offen offset:2048 lds\n"
    "s_cmp_lt_u32 s24, 4\n"
    "s_cbranch_scc0 .LkL_4_%=\n"
    "s_lshl_b32 s40, s30, 2\n"
    "s_add_u32 s40, s40, s24\n"
    "s_mul_i32 s41, s40, 5120\n"
    "s_mul_i32 s40, s40, 1088\n"
    "s_add_u32 m0, s40, 146432\n"
    "s_nop 0\n"
    "buffer_load_dwordx4 v236, s[4:7], s41 offen lds\n"
    "s_branch .LkL_6_%=\n"
    ".LkL_4_%=:\n"
    "s_cmp_eq_u32 s24, 4\n"
    "s_cbranch_scc0 .LkL_5_%=\n"
    "s_mov_b32 m0, 71168\n"
    "s_movk_i32 s41, 24576\n"
    "buffer_load_dwordx4 v247, s[16:19], s41 offen lds\n"
    "s_branch .LkL_6_%=\n"
    ".LkL_5_%=:\n"
    "s_mov_b32 s20, s74\n"
    "s_and_b32 s21, s75, 0xffff\n"
    "s_mov_b32 s22, 1280\n"
    "s_mov_b32 s23, 0x20000\n"
    "s_and_b32 s41, s24, 1\n"
    "s_lshl_b32 s41, s41, 8\n"
    "s_add_u32 m0, s41, 78592\n"
    "s_nop 0\n"
    "buffer_load_dwordx4 v247, s[20:23], s41 offen lds\n"
    ".LkL_6_%=:\n"
    "s_barrier\n"
    "s_mov_b32 s12, s78\n"
    "s_and_b32 s13, s79, 0xffff\n"
    "s_mov_b32 s14, 12648448\n"
    "s_mov_b32 s15, 0x20000\n"
    "buffer_load_dwordx2 v[252:253], v245, s[12:15], 0 offen sc1\n"
    "s_sub_u32 s40, 819200, s39\n"
    "s_add_u32 s8, s66, s39\n"
    "s_addc_u32 s9, s67, 0\n"
    "s_and_b32 s9, s9, 0xffff\n"
    "s_mov_b32 s10, s40\n"
    "s_mov_b32 s11, 0x20000\n"
    "s_sub_u32 s38, s24, 4\n"
    "s_cmp_lt_u32 s38, 3\n"
    "s_cbranch_scc1 .Lkkgen_7_%=\n"
    "s_mov_b32 s46, 79872\n"
    "s_mov_b32 s45, 146432\n"
    "s_cmp_eq_u32 s24, 7\n"
    "s_cbranch_scc1 .Lkw7_11_%=\n"
    "s_cmp_eq_u32 s33, 0\n"
    "s_cbranch_scc1 .Lkkv_9_%=\n"
    "s_mul_i32 s39, s24, 20480\n"
    "s_mul_i32 s40, s24, 16640\n"
    "s_add_u32 m0, s40, 79872\n"
    "s_mov_b32 s41, s39\n"
    "buffer_load_dwordx4 v247, s[4:7], s39 offen lds\n"
    "s_add_u32 m0, m0, 1040\n"
    "s_add_u32 s39, s39, 1280\n"
    "buffer_load_dwordx4 v247, s[4:7], s39 offen lds\n"
    "s_add_u32 m0, m0, 1040\n"
    "s_add_u32 s39, s39, 1280\n"
    "buffer_load_dwordx4 v247, s[4:7], s39 offen lds\n"
    "s_add_u32 m0, m0, 1040\n"
    "s_add_u32 s39, s39, 1280\n"
    "buffer_load_dwordx4 v247, s[4:7], s39 offen lds\n"
    "s_add_u32 m0, m0, 1040\n"
    "s_add_u32 s39, s39, 1280\n"
    "buffer_load_dwordx4 v247, s[4:7], s39 offen lds\n"
    "s_add_u32 m0, m0, 1040\n"
    "s_add_u32 s39, s39, 1280\n"
    "buffer_load_dwordx4 v247, s[4:7], s39 offen lds\n"
    "s_add_u32 m0, m0, 1040\n"
    "s_add_u32 s39, s39, 1280\n"
    "buffer_load_dwordx4 v247, s[4:7], s39 offen lds\n"
    "s_add_u32 m0, m0, 1040\n"
    "s_add_u32 s39, s39, 1280\n"
    "buffer_load_dwordx4 v247, s[4:7], s39 offen lds\n"
    "s_waitcnt vmcnt(9)\n"
    "ds_add_u32 v254, v235 offset:0\n"
    "s_add_u32 m0, m0, 1040\n"
    "s_add_u32 s39, s39, 1280\n"
    "buffer_load_dwordx4 v247, s[4:7], s39 offen lds\n"
    "s_add_u32 m0, m0, 1040\n"
    "s_add_u32 s39, s39, 1280\n"
    "buffer_load_dwordx4 v247, s[4:7], s39 offen lds\n"
    "s_add_u32 m0, m0, 1040\n"
    "s_add_u32 s39, s39, 1280\n"
    "buffer_load_dwordx4 v247, s[4:7], s39 offen lds\n"
    "s_add_u32 m0, m0, 1040\n"
    "s_add_u32 s39, s39, 1280\n"
    "buffer_load_dwordx4 v247, s[4:7], s39 offen lds\n"
    "s_add_u32 m0, m0, 1040\n"
    "s_add_u32 s39, s39, 1280\n"
    "buffer_load_dwordx4 v247, s[4:7], s39 offen lds\n"
    "s_add_u32 m0, m0, 1040\n"
    "s_add_u32 s39, s39, 1280\n"
    "buffer_load_dwordx4 v247, s[4:7], s39 offen lds\n"
    "s_add_u32 m0, m0, 1040\n"
    "s_add_u32 s39, s39, 1280\n"
    "buffer_load_dwordx4 v247, s[4:7], s39 offen lds\n"
    "s_add_u32 m0, m0, 1040\n"
    "s_add_u32 s39, s39, 1280\n"
    "buffer_load_dwordx4 v247, s[4:7], s39 offen lds\n"
    "s_mul_i32 s40, s24, 4352\n"
    "s_add_u32 m0, s40, 146432\n"
    "s_nop 0\n"
    "buffer_load_dwordx4 v236, s[4:7], s41 offen lds\n"
    "s_add_u32 m0, m0, 1088\n"
    "s_add_u32 s41, s41, 5120\n"
    "buffer_load_dwordx4 v236, s[4:7], s41 offen lds\n"
    "s_add_u32 m0, m0, 1088\n"
    "s_add_u32 s41, s41, 5120\n"
    "buffer_load_dwordx4 v236, s[4:7], s41 offen lds\n"
    "s_add_u32 m0, m0, 1088\n"
    "s_add_u32 s41, s41, 5120\n"
    "buffer_load_dwordx4 v236, s[4:7], s41 offen lds\n"
    "s_lshl_b32 s40, s33, 2\n"
    "s_sub_u32 s40, s40, 4\n"
    "s_mov_b32 s41, 512\n"
    "s_mul_i32 s38, s40, 7680\n"
    "s_mul_i32 s39, s40, 960\n"
    "s_add_u32 m0, s39, s41\n"
    "s_mov_b32 exec_hi, 0x0fffffff\n"
    "buffer_load_dwordx4 v248, s[8:11], s38 offen lds\n"
    "s_add_u32 m0, m0, 960\n"
    "s_add_u32 s38, s38, 7680\n"
    "buffer_load_dwordx4 v248, s[8:11], s38 offen lds\n"
    "s_add_u32 m0, m0, 960\n"
    "s_add_u32 s38, s38, 7680\n"
    "buffer_load_dwordx4 v248, s[8:11], s38 offen lds\n"
    "s_add_u32 m0, m0, 960\n"
    "s_add_u32 s38, s38, 7680\n"
    "buffer_load_dwordx4 v248, s[8:11], s38 offen lds\n"
    "s_mov_b64 exec, -1\n"
    "s_lshl_b32 s40, s33, 1\n"
    "s_add_u32 s40, s40, 73\n"
    "s_mov_b32 s41, 512\n"
    "s_mul_i32 s38, s40, 7680\n"
    "s_mul_i32 s39, s40, 960\n"
    "s_add_u32 m0, s39, s41\n"
    "s_mov_b32 exec_hi, 0x0fffffff\n"
    "buffer_load_dwordx4 v248, s[8:11], s38 offen lds\n"
    "s_add_u32 m0, m0, 960\n"
    "s_add_u32 s38, s38, 7680\n"
    "buffer_load_dwordx4 v248, s[8:11], s38 offen lds\n"
    "s_mov_b64 exec, -1\n"
    "s_waitcnt vmcnt(26)\n"
    "v_readfirstlane_b32 s36, v252\n"
    "v_readfirstlane_b32 s37, v253\n"
    "s_waitcnt lgkmcnt(0)\n"
    "s_add_u32 s36, s36, 1\n"
    "s_addc_u32 s37, s37, 0\n"
    "s_lshl_b64 s[34:35], s[36:37], 16\n"
    "s_lshl_b32 s38, s24, 4\n"
    "v_add_u32 v185, s38, v231\n"
    "v_lshrrev_b32 v237, 1, v232\n"
    "v_and_b32 v238, 1, v232\n"
    "v_lshlrev_b32 v238, 3, v238\n"
    "v_mul_u32_u24 v182, 1040, v185\n"
    "v_lshl_add_u32 v182, v237, 4, v182\n"
    "v_add3_u32 v182, v182, v238, s46\n"
    "v_lshrrev_b32 v239, 2, v185\n"
    "v_mul_u32_u24 v239, 1088, v239\n"
    "v_and_b32 v1, 3, v185\n"
    "v_lshl_add_u32 v239, v1, 8, v239\n"
    "v_add3_u32 v239, v239, v238, s45\n"
    "v_xor_b32 v237, v237, v1\n"
    "v_lshl_add_u32 v183, v237, 4, v239\n"
    "v_xor_b32 v237, 2, v237\n"
    "v_lshl_add_u32 v184, v237, 4, v239\n"
    "s_mul_i32 s38, s24, 3072\n"
    "s_add_u32 s38, s38, 49152\n"
    "v_mul_u32_u24 v237, 192, v231\n"
    "v_add_u32 v237, s38, v237\n"
    "v_lshl_add_u32 v186, v232, 3, v237\n"
    "v_min_u32 v238, 1, v232\n"
    "v_lshl_add_u32 v187, v238, 3, v186\n"
    "v_lshrrev_b32 v239, 1, v232\n"
    "v_lshl_add_u32 v188, v239, 3, v186\n"
    "v_and_b32 v239, v239, v232\n"
    "v_lshl_add_u32 v189, v239, 3, v186\n"
    "v_mul_u32_u24 v238, 48, v232\n"
    "v_add_u32 v190, v237, v238\n"
    "v_lshrrev_b32 v237, 2, v231\n"
    "v_lshl_add_u32 v237, v232, 2, v237\n"
    "v_mul_u32_u24 v192, 160, v237\n"
    "v_and_b32 v237, 3, v231\n"
    "v_lshl_add_u32 v192, v237, 3, v192\n"
    "v_add_u32 v193, 46592, v192\n"
    "v_add_u32 v192, 512, v192\n"
    "v_mul_u32_u24 v243, 192, v233\n"
    "v_add_u32 v243, 49152, v243\n"
    "v_lshl_add_u32 v242, v234, 4, v243\n"
    "v_lshl_add_u32 v243, v234, 3, v243\n"
    "v_lshlrev_b32 v237, 3, v233\n"
    "v_add_u32 v237, 1024, v237\n"
    "v_mov_b32 v238, 1280\n"
    "v_cmp_eq_u32 vcc, 0, v234\n"
    "v_mul_u32_u24 v157, 336, v234\n"
    "v_lshl_add_u32 v157, v233, 4, v157\n"
    "v_cndmask_b32 v156, v238, v237, vcc\n"
    "v_add_u32 v157, 1296, v157\n"
    "v_cmp_gt_u32 vcc, 21, v233\n"
    "v_mov_b32 v237, 2147483392\n"
    "v_lshlrev_b32 v158, 3, v230\n"
    "v_cndmask_b32 v157, v237, v157, vcc\n"
    "s_mul_i32 s90, s24, 21504\n"
    "s_add_u32 s90, s90, 4096\n"
    "s_lshl_b32 s39, s24, 9\n"
    "s_add_u32 s39, s39, 180224\n"
    "v_add_u32 v158, s39, v158\n"
    "s_waitcnt vmcnt(6)\n"
    "ds_add_u32 v254, v235 offset:36\n"
    "ds_read_b64 v[2:3], v182 offset:0\n"
    "ds_read_b64 v[4:5], v182 offset:32\n"
    "ds_read_b64 v[6:7], v182 offset:64\n"
    "ds_read_b64 v[8:9], v182 offset:96\n"
    "ds_read_b64 v[10:11], v182 offset:128\n"
    "ds_read_b64 v[12:13], v182 offset:160\n"
    "ds_read_b64 v[14:15], v182 offset:192\n"
    "ds_read_b64 v[16:17], v182 offset:224\n"
    "ds_read_b64 v[18:19], v182 offset:256\n"
    "ds_read_b64 v[20:21], v182 offset:288\n"
    "ds_read_b64 v[22:23], v182 offset:320\n"
    "ds_read_b64 v[24:25], v182 offset:352\n"
    "ds_read_b64 v[26:27], v182 offset:384\n"
    "ds_read_b64 v[28:29], v182 offset:416\n"
    "ds_read_b64 v[30:31], v182 offset:448\n"
    "ds_read_b64 v[32:33], v182 offset:480\n"
    "ds_read_b64 v[34:35], v182 offset:512\n"
    "ds_read_b64 v[36:37], v182 offset:544\n"
    "ds_read_b64 v[38:39], v182 offset:576\n"
    "ds_read_b64 v[40:41], v182 offset:608\n"
    "ds_read_b64 v[42:43], v182 offset:640\n"
    "ds_read_b64 v[44:45], v182 offset:672\n"
    "ds_read_b64 v[46:47], v182 offset:704\n"
    "ds_read_b64 v[48:49], v182 offset:736\n"
    "ds_read_b64 v[50:51], v182 offset:768\n"
    "ds_read_b64 v[52:53], v182 offset:800\n"
    "ds_read_b64 v[54:55], v182 offset:832\n"
    "ds_read_b64 v[56:57], v182 offset:864\n"
    "ds_read_b64 v[58:59], v182 offset:896\n"
    "ds_read_b64 v[60:61], v182 offset:928\n"
    "ds_read_b64 v[62:63], v182 offset:960\n"
    "ds_read_b64 v[64:65], v182 offset:992\n"
    "ds_read_b64 v[66:67], v183 offset:0\n"
    "ds_read_b64 v[68:69], v184 offset:0\n"
    "ds_read_b64 v[70:71], v183 offset:64\n"
    "ds_read_b64 v[72:73], v184 offset:64\n"
    "ds_read_b64 v[74:75], v183 offset:128\n"
    "ds_read_b64 v[76:77], v184 offset:128\n"
    "ds_read_b64 v[78:79], v183 offset:192\n"
    "ds_read_b64 v[80:81], v184 offset:192\n"
    "s_waitcnt vmcnt(0)\n"
    "ds_add_u32 v254, v235 offset:12\n"
    "s_waitcnt lgkmcnt(0)\n"
    "ds_add_u32 v254, v235 offset:4\n"
    "s_branch .Lkq_10_%=\n"
    ".Lkw7_11_%=:\n"
    "s_setprio 2\n"
    "s_lshl_b32 s38, s30, 4\n"
    "v_add_u32 v185, s38, v231\n"
    "v_lshrrev_b32 v237, 1, v232\n"
    "v_and_b32 v238, 1, v232\n"
    "v_lshlrev_b32 v238, 3, v238\n"
    "v_mul_u32_u24 v226, 1040, v185\n"
    "v_lshl_add_u32 v226, v237, 4, v226\n"
    "v_add3_u32 v226, v226, v238, s46\n"
    "v_lshrrev_b32 v239, 2, v185\n"
    "v_mul_u32_u24 v239, 1088, v239\n"
    "v_and_b32 v1, 3, v185\n"
    "v_lshl_add_u32 v239, v1, 8, v239\n"
    "v_add3_u32 v239, v239, v238, s45\n"
    "v_xor_b32 v237, v237, v1\n"
    "v_lshl_add_u32 v227, v237, 4, v239\n"
    "v_xor_b32 v237, 2, v237\n"
    "v_lshl_add_u32 v228, v237, 4, v239\n"
    "v_mul_u32_u24 v243, 192, v233\n"
    "v_add_u32 v243, 49152, v243\n"
    "v_lshl_add_u32 v242, v234, 4, v243\n"
    "v_lshl_add_u32 v243, v234, 3, v243\n"
    "v_lshlrev_b32 v237, 3, v233\n"
    "v_add_u32 v237, 1024, v237\n"
    "v_mov_b32 v238, 1280\n"
    "v_cmp_eq_u32 vcc, 0, v234\n"
    "v_mul_u32_u24 v157, 336, v234\n"
    "v_lshl_add_u32 v157, v233, 4, v157\n"
    "v_cndmask_b32 v156, v238, v237, vcc\n"
    "v_add_u32 v157, 1296, v157\n"
    "v_cmp_gt_u32 vcc, 21, v233\n"
    "v_mov_b32 v237, 2147483392\n"
    "v_lshlrev_b32 v158, 3, v230\n"
    "v_cndmask_b32 v157, v237, v157, vcc\n"
    "s_mul_i32 s90, s24, 21504\n"
    "s_add_u32 s90, s90, 4096\n"
    "s_lshl_b32 s39, s24, 9\n"
    "s_add_u32 s39, s39, 180224\n"
    "v_add_u32 v158, s39, v158\n"
    "v_lshrrev_b32 v237, 2, v231\n"
    "v_lshl_add_u32 v237, v232, 2, v237\n"
    "v_mul_u32_u24 v196, 40, v237\n"
    "v_and_b32 v237, 3, v231\n"
    "v_lshl_add_u32 v196, v237, 3, v196\n"
    "v_add_u32 v196, 46624, v196\n"
    "s_and_b32 s39, s30, 1\n"
    "v_lshrrev_b32 v237, 1, v232\n"
    "v_and_b32 v238, 1, v232\n"
    "v_mov_b32 v198, 0x3f803f80\n"
    "v_mov_b32 v1, 2147483392\n"
    "s_bitcmp1_b32 s26, 0\n"
    "s_cbranch_scc1 .LkL_12_%=\n"
    "s_lshl_b32 s40, s39, 4\n"
    "v_add_u32 v239, s40, v231\n"
    "v_mov_b32 v199, 0x3f80\n"
    "v_lshlrev_b32 v197, 3, v239\n"
    "v_add_u32 v197, 1024, v197\n"
    "v_cmp_gt_u32 vcc, 16, v230\n"
    "v_cmp_eq_u32 s[98:99], 32, v230\n"
    "v_mov_b32 v239, 1280\n"
    "v_cndmask_b32 v197, v1, v197, vcc\n"
    "v_cndmask_b32 v197, v197, v239, s[98:99]\n"
    "s_branch .LkL_13_%=\n"
    ".LkL_12_%=:\n"
    "s_lshl_b32 s40, s39, 1\n"
    "v_add_u32 v238, s40, v238\n"
    "v_mul_u32_u24 v197, 336, v238\n"
    "v_mov_b32 v199, 0x3f803f80\n"
    "v_lshl_add_u32 v197, v231, 4, v197\n"
    "v_lshl_add_u32 v197, v237, 3, v197\n"
    "v_add_u32 v197, 1552, v197\n"
    "v_cmp_gt_u32 vcc, 5, v231\n"
    "v_cmp_eq_u32 s[98:99], 4, v231\n"
    "s_nop 0\n"
    "v_cndmask_b32 v197, v1, v197, vcc\n"
    ".LkL_13_%=:\n"
    "s_lshl_b32 s43, s25, 1\n"
    "s_lshr_b32 s40, s26, 2\n"
    "s_add_u32 s43, s43, s40\n"
    "s_mul_i32 s43, s43, 2688\n"
    "s_add_u32 s43, s43, 4096\n"
    "s_lshl_b32 s44, s25, 3\n"
    "s_add_u32 s44, s44, s26\n"
    "s_lshl_b32 s44, s44, 4\n"
    "s_add_u32 s44, s44, 180232\n"
    "s_bitcmp1_b32 s26, 0\n"
    "s_cselect_b32 s42, 1.0, 1051011931\n"
    "v_lshlrev_b32 v200, 3, v232\n"
    "v_add_u32 v200, 78592, v200\n"
    "s_mov_b32 s88, 0x00100010\n"
    "s_mov_b32 s89, 0x00100010\n"
    "s_waitcnt vmcnt(1)\n"
    "ds_add_u32 v254, v235 offset:0\n"
    "s_mov_b32 s41, 512\n"
    "s_mov_b32 s38, 130560\n"
    "s_add_u32 m0, s41, 16320\n"
    "s_mov_b32 exec_hi, 0x0fffffff\n"
    "buffer_load_dwordx4 v248, s[8:11], s38 offen lds\n"
    "s_add_u32 m0, m0, 960\n"
    "s_add_u32 s38, s38, 7680\n"
    "buffer_load_dwordx4 v248, s[8:11], s38 offen lds\n"
    "s_add_u32 m0, m0, 960\n"
    "s_add_u32 s38, s38, 7680\n"
    "buffer_load_dwordx4 v248, s[8:11], s38 offen lds\n"
    "s_add_u32 m0, m0, 960\n"
    "s_add_u32 s38, s38, 7680\n"
    "buffer_load_dwordx4 v248, s[8:11], s38 offen lds\n"
    "s_add_u32 m0, m0, 960\n"
    "s_add_u32 s38, s38, 7680\n"
    "buffer_load_dwordx4 v248, s[8:11], s38 offen lds\n"
    "s_mov_b64 exec, -1\n"
    ".Lkw_14_%=:\n"
    "ds_read_b32 v1, v245 offset:0\n"
    "s_waitcnt lgkmcnt(0)\n"
    "v_readfirstlane_b32 s47, v1\n"
    "s_cmp_ge_u32 s47, 8\n"
    "s_cbranch_scc1 .Lkd_15_%=\n"
    "s_sleep 1\n"
    "s_sub_u32 s80, s80, 1\n"
    "s_cbranch_scc0 .Lkw_14_%=\n"
    "s_mov_b32 s80, 0\n"
    ".Lkd_15_%=:\n"
    "ds_read_b64 v[2:3], v226 offset:0\n"
    "ds_read_b64 v[4:5], v226 offset:32\n"
    "ds_read_b64 v[6:7], v226 offset:64\n"
    "ds_read_b64 v[8:9], v226 offset:96\n"
    "ds_read_b64 v[10:11], v226 offset:128\n"
    "ds_read_b64 v[12:13], v226 offset:160\n"
    "ds_read_b64 v[14:15], v226 offset:192\n"
    "ds_read_b64 v[16:17], v226 offset:224\n"
    "ds_read_b64 v[18:19], v226 offset:256\n"
    "ds_read_b64 v[20:21], v226 offset:288\n"
    "ds_read_b64 v[22:23], v226 offset:320\n"
    "ds_read_b64 v[24:25], v226 offset:352\n"
    "ds_read_b64 v[26:27], v226 offset:384\n"
    "ds_read_b64 v[28:29], v226 offset:416\n"
    "ds_read_b64 v[30:31], v226 offset:448\n"
    "ds_read_b64 v[32:33], v226 offset:480\n"
    "ds_read_b64 v[34:35], v226 offset:512\n"
    "ds_read_b64 v[36:37], v226 offset:544\n"
    "ds_read_b64 v[38:39], v226 offset:576\n"
    "ds_read_b64 v[40:41], v226 offset:608\n"
    "ds_read_b64 v[42:43], v226 offset:640\n"
    "ds_read_b64 v[44:45], v226 offset:672\n"
    "ds_read_b64 v[46:47], v226 offset:704\n"
    "ds_read_b64 v[48:49], v226 offset:736\n"
    "ds_read_b64 v[50:51], v226 offset:768\n"
    "ds_read_b64 v[52:53], v226 offset:800\n"
    "ds_read_b64 v[54:55], v226 offset:832\n"
    "ds_read_b64 v[56:57], v226 offset:864\n"
    "ds_read_b64 v[58:59], v226 offset:896\n"
    "ds_read_b64 v[60:61], v226 offset:928\n"
    "ds_read_b64 v[62:63], v226 offset:960\n"
    "ds_read_b64 v[64:65], v226 offset:992\n"
    "ds_read_b64 v[66:67], v227 offset:0\n"
    "ds_read_b64 v[68:69], v228 offset:0\n"
    "ds_read_b64 v[70:71], v227 offset:64\n"
    "ds_read_b64 v[72:73], v228 offset:64\n"
    "ds_read_b64 v[74:75], v227 offset:128\n"
    "ds_read_b64 v[76:77], v228 offset:128\n"
    "ds_read_b64 v[78:79], v227 offset:192\n"
    "ds_read_b64 v[80:81], v228 offset:192\n"
    "s_bitcmp1_b32 s26, 0\n"
    "s_cbranch_scc1 .LkL_16_%=\n"
    "ds_read_b64_tr_b16 v[82:83], v196 offset:0\n"
    "ds_read_b64_tr_b16 v[84:85], v196 offset:640\n"
    "s_mov_b64 exec, s[88:89]\n"
    "ds_read_b64 v[82:83], v200 offset:0\n"
    "ds_read_b64 v[84:85], v200 offset:32\n"
    "s_mov_b64 exec, -1\n"
    "ds_read_b64_tr_b16 v[86:87], v196 offset:1280\n"
    "ds_read_b64_tr_b16 v[88:89], v196 offset:1920\n"
    "s_mov_b64 exec, s[88:89]\n"
    "ds_read_b64 v[86:87], v200 offset:64\n"
    "ds_read_b64 v[88:89], v200 offset:96\n"
    "s_mov_b64 exec, -1\n"
    "ds_read_b64_tr_b16 v[90:91], v196 offset:2560\n"
    "ds_read_b64_tr_b16 v[92:93], v196 offset:3200\n"
    "s_mov_b64 exec, s[88:89]\n"
    "ds_read_b64 v[90:91], v200 offset:128\n"
    "ds_read_b64 v[92:93], v200 offset:160\n"
    "s_mov_b64 exec, -1\n"
    "ds_read_b64_tr_b16 v[94:95], v196 offset:3840\n"
    "ds_read_b64_tr_b16 v[96:97], v196 offset:4480\n"
    "s_mov_b64 exec, s[88:89]\n"
    "ds_read_b64 v[94:95], v200 offset:192\n"
    "ds_read_b64 v[96:97], v200 offset:224\n"
    "s_mov_b64 exec, -1\n"
    "s_waitcnt lgkmcnt(12)\n"
    "ds_add_u32 v254, v235 offset:4\n"
    "v_mfma_f32_16x16x32_bf16 v[162:165], v[82:85], v[2:5], 0\n"
    "ds_read_b64_tr_b16 v[98:99], v196 offset:5120\n"
    "ds_read_b64_tr_b16 v[100:101], v196 offset:5760\n"
    "s_mov_b64 exec, s[88:89]\n"
    "ds_read_b64 v[98:99], v200 offset:256\n"
    "ds_read_b64 v[100:101], v200 offset:288\n"
    "s_mov_b64 exec, -1\n"
    "s_waitcnt lgkmcnt(13)\n"
    "v_mfma_f32_16x16x32_bf16 v[162:165], v[86:89], v[6:9], v[162:165]\n"
    "ds_read_b64_tr_b16 v[102:103], v196 offset:6400\n"
    "ds_read_b64_tr_b16 v[104:105], v196 offset:7040\n"
    "s_mov_b64 exec, s[88:89]\n"
    "ds_read_b64 v[102:103], v200 offset:320\n"
    "ds_read_b64 v[104:105], v200 offset:352\n"
    "s_mov_b64 exec, -1\n"
    "s_waitcnt lgkmcnt(13)\n"
    "v_mfma_f32_16x16x32_bf16 v[162:165], v[90:93], v[10:13], v[162:165]\n"
    "ds_read_b64_tr_b16 v[106:107], v196 offset:7680\n"
    "ds_read_b64_tr_b16 v[108:109], v196 offset:8320\n"
    "s_mov_b64 exec, s[88:89]\n"
    "ds_read_b64 v[106:107], v200 offset:384\n"
    "ds_read_b64 v[108:109], v200 offset:416\n"
    "s_mov_b64 exec, -1\n"
    "s_waitcnt lgkmcnt(13)\n"
    "v_mfma_f32_16x16x32_bf16 v[162:165], v[94:97], v[14:17], v[162:165]\n"
    "ds_read_b64_tr_b16 v[110:111], v196 offset:8960\n"
    "ds_read_b64_tr_b16 v[112:113], v196 offset:9600\n"
    "s_mov_b64 exec, s[88:89]\n"
    "ds_read_b64 v[110:111], v200 offset:448\n"
    "ds_read_b64 v[112:113], v200 offset:480\n"
    "s_mov_b64 exec, -1\n"
    "s_waitcnt lgkmcnt(12)\n"
    "v_mfma_f32_16x16x32_bf16 v[162:165], v[98:101], v[18:21], v[162:165]\n"
    "ds_read_b64_tr_b16 v[82:83], v196 offset:10240\n"
    "ds_read_b64_tr_b16 v[84:85], v196 offset:10880\n"
    "s_mov_b64 exec, s[88:89]\n"
    "ds_read_b64 v[82:83], v200 offset:512\n"
    "ds_read_b64 v[84:85], v200 offset:544\n"
    "s_mov_b64 exec, -1\n"
    "s_waitcnt lgkmcnt(12)\n"
    "v_mfma_f32_16x16x32_bf16 v[162:165], v[102:105], v[22:25], v[162:165]\n"
    "ds_read_b64_tr_b16 v[86:87], v196 offset:11520\n"
    "ds_read_b64_tr_b16 v[88:89], v196 offset:12160\n"
    "s_mov_b64 exec, s[88:89]\n"
    "ds_read_b64 v[86:87], v200 offset:576\n"
    "ds_read_b64 v[88:89], v200 offset:608\n"
    "s_mov_b64 exec, -1\n"
    "s_waitcnt lgkmcnt(12)\n"
    "v_mfma_f32_16x16x32_bf16 v[162:165], v[106:109], v[26:29], v[162:165]\n"
    "ds_read_b64_tr_b16 v[90:91], v196 offset:12800\n"
    "ds_read_b64_tr_b16 v[92:93], v196 offset:13440\n"
    "s_mov_b64 exec, s[88:89]\n"
    "ds_read_b64 v[90:91], v200 offset:640\n"
    "ds_read_b64 v[92:93], v200 offset:672\n"
    "s_mov_b64 exec, -1\n"
    "s_waitcnt lgkmcnt(12)\n"
    "v_mfma_f32_16x16x32_bf16 v[162:165], v[110:113], v[30:33], v[162:165]\n"
    "ds_read_b64_tr_b16 v[94:95], v196 offset:14080\n"
    "ds_read_b64_tr_b16 v[96:97], v196 offset:14720\n"
    "s_mov_b64 exec, s[88:89]\n"
    "ds_read_b64 v[94:95], v200 offset:704\n"
    "ds_read_b64 v[96:97], v200 offset:736\n"
    "s_mov_b64 exec, -1\n"
    "s_waitcnt lgkmcnt(12)\n"
    "v_mfma_f32_16x16x32_bf16 v[162:165], v[82:85], v[34:37], v[162:165]\n"
    "ds_read_b64_tr_b16 v[98:99], v196 offset:15360\n"
    "ds_read_b64_tr_b16 v[100:101], v196 offset:16000\n"
    "s_mov_b64 exec, s[88:89]\n"
    "ds_read_b64 v[98:99], v200 offset:768\n"
    "ds_read_b64 v[100:101], v200 offset:800\n"
    "s_mov_b64 exec, -1\n"
    "s_waitcnt lgkmcnt(12)\n"
    "v_mfma_f32_16x16x32_bf16 v[162:165], v[86:89], v[38:41], v[162:165]\n"
    "ds_read_b64_tr_b16 v[102:103], v196 offset:16640\n"
    "ds_read_b64_tr_b16 v[104:105], v196 offset:17280\n"
    "s_mov_b64 exec, s[88:89]\n"
    "ds_read_b64 v[102:103], v200 offset:832\n"
    "ds_read_b64 v[104:105], v200 offset:864\n"
    "s_mov_b64 exec, -1\n"
    "s_waitcnt lgkmcnt(12)\n"
    "v_mfma_f32_16x16x32_bf16 v[162:165], v[90:93], v[42:45], v[162:165]\n"
    "ds_read_b64_tr_b16 v[106:107], v196 offset:17920\n"
    "ds_read_b64_tr_b16 v[108:109], v196 offset:18560\n"
    "s_mov_b64 exec, s[88:89]\n"
    "ds_read_b64 v[106:107], v200 offset:896\n"
    "ds_read_b64 v[108:109], v200 offset:928\n"
    "s_mov_b64 exec, -1\n"
    "s_waitcnt lgkmcnt(12)\n"
    "v_mfma_f32_16x16x32_bf16 v[162:165], v[94:97], v[46:49], v[162:165]\n"
    "ds_read_b64_tr_b16 v[110:111], v196 offset:19200\n"
    "ds_read_b64_tr_b16 v[112:113], v196 offset:19840\n"
    "s_mov_b64 exec, s[88:89]\n"
    "ds_read_b64 v[110:111], v200 offset:960\n"
    "ds_read_b64 v[112:113], v200 offset:992\n"
    "s_mov_b64 exec, -1\n"
    "s_waitcnt lgkmcnt(12)\n"
    "v_mfma_f32_16x16x32_bf16 v[162:165], v[98:101], v[50:53], v[162:165]\n"
    "ds_read_b64_tr_b16 v[82:83], v196 offset:20480\n"
    "ds_read_b64_tr_b16 v[84:85], v196 offset:21120\n"
    "s_mov_b64 exec, s[88:89]\n"
    "ds_read_b64 v[82:83], v200 offset:1024\n"
    "ds_read_b64 v[84:85], v200 offset:1056\n"
    "s_mov_b64 exec, -1\n"
    "s_waitcnt lgkmcnt(12)\n"
    "v_mfma_f32_16x16x32_bf16 v[162:165], v[102:105], v[54:57], v[162:165]\n"
    "ds_read_b64_tr_b16 v[86:87], v196 offset:21760\n"
    "ds_read_b64_tr_b16 v[88:89], v196 offset:22400\n"
    "s_mov_b64 exec, s[88:89]\n"
    "ds_read_b64 v[86:87], v200 offset:1088\n"
    "ds_read_b64 v[88:89], v200 offset:1120\n"
    "s_mov_b64 exec, -1\n"
    "s_waitcnt lgkmcnt(12)\n"
    "v_mfma_f32_16x16x32_bf16 v[162:165], v[106:109], v[58:61], v[162:165]\n"
    "ds_read_b64_tr_b16 v[90:91], v196 offset:23040\n"
    "ds_read_b64_tr_b16 v[92:93], v196 offset:23680\n"
    "s_mov_b64 exec, s[88:89]\n"
    "ds_read_b64 v[90:91], v200 offset:1152\n"
    "ds_read_b64 v[92:93], v200 offset:1184\n"
    "s_mov_b64 exec, -1\n"
    "s_waitcnt lgkmcnt(12)\n"
    "v_mfma_f32_16x16x32_bf16 v[162:165], v[110:113], v[62:65], v[162:165]\n"
    "ds_read_b64_tr_b16 v[94:95], v196 offset:24320\n"
    "ds_read_b64_tr_b16 v[96:97], v196 offset:24960\n"
    "s_mov_b64 exec, s[88:89]\n"
    "ds_read_b64 v[94:95], v200 offset:1216\n"
    "ds_read_b64 v[96:97], v200 offset:1248\n"
    "s_mov_b64 exec, -1\n"
    "s_waitcnt lgkmcnt(12)\n"
    "v_mfma_f32_16x16x32_bf16 v[162:165], v[82:85], v[66:69], v[162:165]\n"
    "s_waitcnt lgkmcnt(8)\n"
    "v_mfma_f32_16x16x32_bf16 v[162:165], v[86:89], v[70:73], v[162:165]\n"
    "s_waitcnt lgkmcnt(4)\n"
    "v_mfma_f32_16x16x32_bf16 v[162:165], v[90:93], v[74:77], v[162:165]\n"
    "s_waitcnt lgkmcnt(0)\n"
    "v_mfma_f32_16x16x32_bf16 v[162:165], v[94:97], v[78:81], v[162:165]\n"
    "s_branch .LkL_17_%=\n"
    ".LkL_16_%=:\n"
    "ds_read_b64_tr_b16 v[82:83], v196 offset:0\n"
    "ds_read_b64_tr_b16 v[84:85], v196 offset:640\n"
    "ds_read_b64_tr_b16 v[86:87], v196 offset:1280\n"
    "ds_read_b64_tr_b16 v[88:89], v196 offset:1920\n"
    "ds_read_b64_tr_b16 v[90:91], v196 offset:2560\n"
    "ds_read_b64_tr_b16 v[92:93], v196 offset:3200\n"
    "ds_read_b64_tr_b16 v[94:95], v196 offset:3840\n"
    "ds_read_b64_tr_b16 v[96:97], v196 offset:4480\n"
    "ds_read_b64_tr_b16 v[98:99], v196 offset:5120\n"
    "ds_read_b64_tr_b16 v[100:101], v196 offset:5760\n"
    "ds_read_b64_tr_b16 v[102:103], v196 offset:6400\n"
    "ds_read_b64_tr_b16 v[104:105], v196 offset:7040\n"
    "ds_read_b64_tr_b16 v[106:107], v196 offset:7680\n"
    "ds_read_b64_tr_b16 v[108:109], v196 offset:8320\n"
    "s_waitcnt lgkmcnt(12)\n"
    "ds_add_u32 v254, v235 offset:4\n"
    "v_mfma_f32_16x16x32_bf16 v[162:165], v[2:5], v[82:85], 0\n"
    "ds_read_b64_tr_b16 v[110:111], v196 offset:8960\n"
    "ds_read_b64_tr_b16 v[112:113], v196 offset:9600\n"
    "s_waitcnt lgkmcnt(13)\n"
    "v_mfma_f32_16x16x32_bf16 v[162:165], v[6:9], v[86:89], v[162:165]\n"
    "ds_read_b64_tr_b16 v[82:83], v196 offset:10240\n"
    "ds_read_b64_tr_b16 v[84:85], v196 offset:10880\n"
    "s_waitcnt lgkmcnt(13)\n"
    "v_mfma_f32_16x16x32_bf16 v[162:165], v[10:13], v[90:93], v[162:165]\n"
    "ds_read_b64_tr_b16 v[86:87], v196 offset:11520\n"
    "ds_read_b64_tr_b16 v[88:89], v196 offset:12160\n"
    "s_waitcnt lgkmcnt(13)\n"
    "v_mfma_f32_16x16x32_bf16 v[162:165], v[14:17], v[94:97], v[162:165]\n"
    "ds_read_b64_tr_b16 v[90:91], v196 offset:12800\n"
    "ds_read_b64_tr_b16 v[92:93], v196 offset:13440\n"
    "s_waitcnt lgkmcnt(13)\n"
    "v_mfma_f32_16x16x32_bf16 v[162:165], v[18:21], v[98:101], v[162:165]\n"
    "ds_read_b64_tr_b16 v[94:95], v196 offset:14080\n"
    "ds_read_b64_tr_b16 v[96:97], v196 offset:14720\n"
    "s_waitcnt lgkmcnt(13)\n"
    "v_mfma_f32_16x16x32_bf16 v[162:165], v[22:25], v[102:105], v[162:165]\n"
    "ds_read_b64_tr_b16 v[98:99], v196 offset:15360\n"
    "ds_read_b64_tr_b16 v[100:101], v196 offset:16000\n"
    "s_waitcnt lgkmcnt(13)\n"
    "v_mfma_f32_16x16x32_bf16 v[162:165], v[26:29], v[106:109], v[162:165]\n"
    "ds_read_b64_tr_b16 v[102:103], v196 offset:16640\n"
    "ds_read_b64_tr_b16 v[104:105], v196 offset:17280\n"
    "s_waitcnt lgkmcnt(12)\n"
    "v_mfma_f32_16x16x32_bf16 v[162:165], v[30:33], v[110:113], v[162:165]\n"
    "ds_read_b64_tr_b16 v[106:107], v196 offset:17920\n"
    "ds_read_b64_tr_b16 v[108:109], v196 offset:18560\n"
    "s_waitcnt lgkmcnt(12)\n"
    "v_mfma_f32_16x16x32_bf16 v[162:165], v[34:37], v[82:85], v[162:165]\n"
    "ds_read_b64_tr_b16 v[110:111], v196 offset:19200\n"
    "ds_read_b64_tr_b16 v[112:113], v196 offset:19840\n"
    "s_waitcnt lgkmcnt(12)\n"
    "v_mfma_f32_16x16x32_bf16 v[162:165], v[38:41], v[86:89], v[162:165]\n"
    "ds_read_b64_tr_b16 v[82:83], v196 offset:20480\n"
    "ds_read_b64_tr_b16 v[84:85], v196 offset:21120\n"
    "s_waitcnt lgkmcnt(12)\n"
    "v_mfma_f32_16x16x32_bf16 v[162:165], v[42:45], v[90:93], v[162:165]\n"
    "ds_read_b64_tr_b16 v[86:87], v196 offset:21760\n"
    "ds_read_b64_tr_b16 v[88:89], v196 offset:22400\n"
    "s_waitcnt lgkmcnt(12)\n"
    "v_mfma_f32_16x16x32_bf16 v[162:165], v[46:49], v[94:97], v[162:165]\n"
    "ds_read_b64_tr_b16 v[90:91], v196 offset:23040\n"
    "ds_read_b64_tr_b16 v[92:93], v196 offset:23680\n"
    "s_waitcnt lgkmcnt(12)\n"
    "v_mfma_f32_16x16x32_bf16 v[162:165], v[50:53], v[98:101], v[162:165]\n"
    "ds_read_b64_tr_b16 v[94:95], v196 offset:24320\n"
    "ds_read_b64_tr_b16 v[96:97], v196 offset:24960\n"
    "s_waitcnt lgkmcnt(12)\n"
    "v_mfma_f32_16x16x32_bf16 v[162:165], v[54:57], v[102:105], v[162:165]\n"
    "s_waitcnt lgkmcnt(10)\n"
    "v_mfma_f32_16x16x32_bf16 v[162:165], v[58:61], v[106:109], v[162:165]\n"
    "s_waitcnt lgkmcnt(8)\n"
    "v_mfma_f32_16x16x32_bf16 v[162:165], v[62:65], v[110:113], v[162:165]\n"
    "s_waitcnt lgkmcnt(6)\n"
    "v_mfma_f32_16x16x32_bf16 v[162:165], v[66:69], v[82:85], v[162:165]\n"
    "s_waitcnt lgkmcnt(4)\n"
    "v_mfma_f32_16x16x32_bf16 v[162:165], v[70:73], v[86:89], v[162:165]\n"
    "s_waitcnt lgkmcnt(2)\n"
    "v_mfma_f32_16x16x32_bf16 v[162:165], v[74:77], v[90:93], v[162:165]\n"
    "s_waitcnt lgkmcnt(0)\n"
    "v_mfma_f32_16x16x32_bf16 v[162:165], v[78:81], v[94:97], v[162:165]\n"
    ".LkL_17_%=:\n"
    "ds_add_u32 v254, v235 offset:8\n"
    "s_waitcnt vmcnt(5)\n"
    "v_readfirstlane_b32 s36, v252\n"
    "v_readfirstlane_b32 s37, v253\n"
    "s_waitcnt lgkmcnt(0)\n"
    "s_add_u32 s36, s36, 1\n"
    "s_addc_u32 s37, s37, 0\n"
    "s_lshl_b64 s[34:35], s[36:37], 16\n"
    "s_lshl_b32 s38, s30, 4\n"
    "s_add_u32 s38, s38, s29\n"
    "s_lshl_b32 s38, s38, 3\n"
    "s_add_u32 s38, s38, 311296\n"
    "s_nop 1\n"
    "v_mov_b32 v202, v162\n"
    "v_mov_b32 v203, s36\n"
    "v_mul_f32 v162, s42, v162\n"
    "v_mul_f32 v163, s42, v163\n"
    "v_mul_f32 v164, s42, v164\n"
    "v_mul_f32 v165, s42, v165\n"
    "v_cvt_pk_bf16_f32 v200, v162, v163\n"
    "v_cvt_pk_bf16_f32 v201, v164, v165\n"
    "v_cndmask_b32 v200, v200, v198, s[98:99]\n"
    "v_cndmask_b32 v201, v201, v199, s[98:99]\n"
    "buffer_store_dwordx2 v[200:201], v197, s[12:15], s43 offen sc1\n"
    "s_bitcmp1_b32 s26, 0\n"
    "s_cbranch_scc1 .LkL_18_%=\n"
    "v_lshlrev_b32 v237, 3, v231\n"
    "s_mov_b32 exec_hi, 0\n"
    "s_mov_b32 exec_lo, 0xffff0000\n"
    "buffer_store_dwordx2 v[202:203], v237, s[12:15], s38 offen sc1\n"
    "s_mov_b64 exec, -1\n"
    ".LkL_18_%=:\n"
    "v_mul_f32 v198, v162, v162\n"
    "v_cmp_gt_u32 vcc, 16, v230\n"
    "v_fmac_f32 v198, v163, v163\n"
    "v_fmac_f32 v198, v164, v164\n"
    "v_fmac_f32 v198, v165, v165\n"
    "v_cndmask_b32 v198, 0, v198, vcc\n"
    "s_nop 1\n"
    "v_max_u32_dpp v198, v198, v198 row_ror:8 row_mask:0xf bank_mask:0xf\n"
    "s_nop 1\n"
    "v_max_u32_dpp v198, v198, v198 row_ror:4 row_mask:0xf bank_mask:0xf\n"
    "s_nop 1\n"
    "v_max_u32_dpp v198, v198, v198 row_ror:2 row_mask:0xf bank_mask:0xf\n"
    "s_nop 1\n"
    "v_max_u32_dpp v198, v198, v198 row_ror:1 row_mask:0xf bank_mask:0xf\n"
    "s_nop 1\n"
    "v_mul_f32 v198, 0x3f828f5c, v198\n"
    "s_bitcmp1_b32 s26, 0\n"
    "s_cselect_b32 s38, 0, 0x7f80\n"
    "v_lshrrev_b32 v198, 16, v198\n"
    "v_add_u32 v198, 1, v198\n"
    "v_min_u32 v198, s38, v198\n"
    "s_waitcnt vmcnt(1)\n"
    "ds_add_u32 v254, v235 offset:12\n"
    ".Lkw_19_%=:\n"
    "ds_read_b32 v1, v245 offset:8\n"
    "s_waitcnt lgkmcnt(0)\n"
    "v_readfirstlane_b32 s47, v1\n"
    "s_cmp_ge_u32 s47, 2\n"
    "s_cbranch_scc1 .Lkd_20_%=\n"
    "s_sleep 1\n"
    "s_sub_u32 s80, s80, 1\n"
    "s_cbranch_scc0 .Lkw_19_%=\n"
    "s_mov_b32 s80, 0\n"
    ".Lkd_20_%=:\n"
    "s_mov_b32 s41, 512\n"
    "s_mov_b32 s38, 368640\n"
    "s_add_u32 m0, s41, 46080\n"
    "s_mov_b32 exec_hi, 0x0fffffff\n"
    "buffer_load_dwordx4 v248, s[8:11], s38 offen lds\n"
    "s_add_u32 m0, m0, 960\n"
    "s_add_u32 s38, s38, 7680\n"
    "buffer_load_dwordx4 v248, s[8:11], s38 offen lds\n"
    "s_add_u32 m0, m0, 960\n"
    "s_add_u32 s38, s38, 7680\n"
    "buffer_load_dwordx4 v248, s[8:11], s38 offen lds\n"
    "s_add_u32 m0, m0, 960\n"
    "s_add_u32 s38, s38, 7680\n"
    "buffer_load_dwordx4 v248, s[8:11], s38 offen lds\n"
    "s_add_u32 m0, m0, 960\n"
    "s_add_u32 s38, s38, 7680\n"
    "buffer_load_dwordx4 v248, s[8:11], s38 offen lds\n"
    "s_add_u32 m0, m0, 960\n"
    "s_add_u32 s38, s38, 7680\n"
    "buffer_load_dwordx4 v248, s[8:11], s38 offen lds\n"
    "s_add_u32 m0, m0, 960\n"
    "s_add_u32 s38, s38, 7680\n"
    "buffer_load_dwordx4 v248, s[8:11], s38 offen lds\n"
    "s_add_u32 m0, m0, 960\n"
    "s_add_u32 s38, s38, 7680\n"
    "buffer_load_dwordx4 v248, s[8:11], s38 offen lds\n"
    "s_add_u32 m0, m0, 960\n"
    "s_add_u32 s38, s38, 7680\n"
    "s_waitcnt vmcnt(8)\n"
    "v_or_b32 v252, s34, v198\n"
    "v_mov_b32 v253, s35\n"
    "buffer_store_dwordx2 v[252:253], v254, s[12:15], s44 offen sc1\n"
    "buffer_load_dwordx4 v248, s[8:11], s38 offen lds\n"
    "s_mov_b64 exec, -1\n"
    ".Lkw_21_%=:\n"
    "ds_read_b32 v1, v245 offset:4\n"
    "s_waitcnt lgkmcnt(0)\n"
    "v_readfirstlane_b32 s47, v1\n"
    "s_cmp_ge_u32 s47, 8\n"
    "s_cbranch_scc1 .Lkd_22_%=\n"
    "s_sleep 1\n"
    "s_sub_u32 s80, s80, 1\n"
    "s_cbranch_scc0 .Lkw_21_%=\n"
    "s_mov_b32 s80, 0\n"
    ".Lkd_22_%=:\n"
    "s_mov_b32 s38, 714240\n"
    "s_add_u32 m0, s41, 89280\n"
    "s_mov_b32 exec_hi, 0x0fffffff\n"
    "buffer_load_dwordx4 v248, s[8:11], s38 offen lds\n"
    "s_add_u32 m0, m0, 960\n"
    "s_add_u32 s38, s38, 7680\n"
    "buffer_load_dwordx4 v248, s[8:11], s38 offen lds\n"
    "s_add_u32 m0, m0, 960\n"
    "s_add_u32 s38, s38, 7680\n"
    "buffer_load_dwordx4 v248, s[8:11], s38 offen lds\n"
    "s_add_u32 m0, m0, 960\n"
    "s_add_u32 s38, s38, 7680\n"
    "buffer_load_dwordx4 v248, s[8:11], s38 offen lds\n"
    "s_add_u32 m0, m0, 960\n"
    "s_add_u32 s38, s38, 7680\n"
    "buffer_load_dwordx4 v248, s[8:11], s38 offen lds\n"
    "s_add_u32 m0, m0, 960\n"
    "s_add_u32 s38, s38, 7680\n"
    "buffer_load_dwordx4 v248, s[8:11], s38 offen lds\n"
    "s_add_u32 m0, m0, 960\n"
    "s_add_u32 s38, s38, 7680\n"
    "buffer_load_dwordx4 v248, s[8:11], s38 offen lds\n"
    "s_add_u32 m0, m0, 960\n"
    "s_add_u32 s38, s38, 7680\n"
    "buffer_load_dwordx4 v248, s[8:11], s38 offen lds\n"
    "s_add_u32 m0, m0, 960\n"
    "s_add_u32 s38, s38, 7680\n"
    "buffer_load_dwordx4 v248, s[8:11], s38 offen lds\n"
    "s_add_u32 m0, m0, 960\n"
    "s_add_u32 s38, s38, 7680\n"
    "buffer_load_dwordx4 v248, s[8:11], s38 offen lds\n"
    "s_add_u32 m0, m0, 960\n"
    "s_add_u32 s38, s38, 7680\n"
    "buffer_load_dwordx4 v248, s[8:11], s38 offen lds\n"
    "s_add_u32 m0, m0, 960\n"
    "s_add_u32 s38, s38, 7680\n"
    "buffer_load_dwordx4 v248, s[8:11], s38 offen lds\n"
    "s_add_u32 m0, m0, 960\n"
    "s_add_u32 s38, s38, 7680\n"
    "buffer_load_dwordx4 v248, s[8:11], s38 offen lds\n"
    "s_add_u32 m0, m0, 960\n"
    "s_add_u32 s38, s38, 7680\n"
    "s_mov_b32 exec_hi, 0xff\n"
    "buffer_load_dwordx4 v248, s[8:11], s38 offen lds\n"
    "s_mov_b64 exec, -1\n"
    "s_waitcnt vmcnt(14)\n"
    "ds_add_u32 v254, v235 offset:16\n"
    "s_waitcnt vmcnt(11)\n"
    "ds_add_u32 v254, v235 offset:48\n"
    "s_waitcnt vmcnt(0)\n"
    "ds_add_u32 v254, v235 offset:48\n"
    ".Lkp_23_%=:\n"
    "buffer_load_dwordx2 v[220:221], v158, s[12:15], 0 offen sc1\n"
    "s_waitcnt vmcnt(0)\n"
    "v_and_b32 v222, 0xffff0000, v220\n"
    "v_cmp_eq_u32 s[86:87], s35, v221\n"
    "v_cmp_eq_u32 vcc, s34, v222\n"
    "s_and_b64 vcc, vcc, s[86:87]\n"
    "s_cmp_eq_u64 vcc, -1\n"
    "s_cbranch_scc1 .Lkd_24_%=\n"
    "s_sleep 5\n"
    "s_sub_u32 s80, s80, 1\n"
    "s_cbranch_scc0 .Lkp_23_%=\n"
    "s_mov_b32 s80, 0\n"
    ".Lkd_24_%=:\n"
    "s_add_u32 s38, s90, 0\n"
    "buffer_load_dwordx4 v[128:131], v247, s[12:15], s38 offen sc1\n"
    "buffer_load_dwordx2 v[132:133], v156, s[12:15], s38 offen sc1\n"
    "buffer_load_dwordx4 v[134:137], v157, s[12:15], s38 offen sc1\n"
    "buffer_load_dwordx4 v[138:141], v157, s[12:15], s38 offen offset:672 sc1\n"
    "s_add_u32 s38, s90, 2688\n"
    "buffer_load_dwordx4 v[142:145], v247, s[12:15], s38 offen sc1\n"
    "buffer_load_dwordx2 v[146:147], v156, s[12:15], s38 offen sc1\n"
    "buffer_load_dwordx4 v[148:151], v157, s[12:15], s38 offen sc1\n"
    "buffer_load_dwordx4 v[152:155], v157, s[12:15], s38 offen offset:672 sc1\n"
    "s_lshl_b32 s38, s27, 8\n"
    "s_add_u32 s38, s38, 245760\n"
    "v_lshlrev_b32 v2, 3, v233\n"
    ".Lkp_25_%=:\n"
    "buffer_load_dwordx2 v[4:5], v2, s[12:15], s38 offen sc1\n"
    "s_waitcnt vmcnt(0)\n"
    "v_and_b32 v6, 0xffff0000, v4\n"
    "v_cmp_eq_u32 s[86:87], s35, v5\n"
    "v_cmp_eq_u32 vcc, s34, v6\n"
    "s_and_b64 vcc, vcc, s[86:87]\n"
    "s_cmp_eq_u64 vcc, -1\n"
    "s_cbranch_scc1 .Lkd_26_%=\n"
    "s_sleep 5\n"
    "s_sub_u32 s80, s80, 1\n"
    "s_cbranch_scc0 .Lkp_25_%=\n"
    "s_mov_b32 s80, 0\n"
    ".Lkd_26_%=:\n"
    "s_lshl_b32 s38, s31, 1\n"
    "s_add_u32 s38, s38, s83\n"
    "s_lshl_b32 s38, s38, 12\n"
    "s_add_u32 s38, s38, 327680\n"
    "buffer_load_dwordx4 v[8:11], v247, s[12:15], s38 offen offset:0 sc1\n"
    "buffer_load_dwordx4 v[12:15], v247, s[12:15], s38 offen offset:1024 sc1\n"
    "buffer_load_dwordx4 v[16:19], v247, s[12:15], s38 offen offset:2048 sc1\n"
    "buffer_load_dwordx4 v[20:23], v247, s[12:15], s38 offen offset:3072 sc1\n"
    "s_add_u32 s38, s38, 4096\n"
    "buffer_load_dwordx4 v[24:27], v247, s[12:15], s38 offen offset:0 sc1\n"
    "buffer_load_dwordx4 v[28:31], v247, s[12:15], s38 offen offset:1024 sc1\n"
    "buffer_load_dwordx4 v[32:35], v247, s[12:15], s38 offen offset:2048 sc1\n"
    "buffer_load_dwordx4 v[36:39], v247, s[12:15], s38 offen offset:3072 sc1\n"
    "s_add_u32 s38, s83, 4\n"
    "s_lshl_b32 s38, s38, 12\n"
    "s_add_u32 s38, s38, 327680\n"
    "buffer_load_dwordx4 v[40:43], v247, s[12:15], s38 offen offset:0 sc1\n"
    "buffer_load_dwordx4 v[44:47], v247, s[12:15], s38 offen offset:1024 sc1\n"
    "buffer_load_dwordx4 v[48:51], v247, s[12:15], s38 offen offset:2048 sc1\n"
    "buffer_load_dwordx4 v[52:55], v247, s[12:15], s38 offen offset:3072 sc1\n"
    "v_add_u32 v60, 61440, v247\n"
    "v_lshrrev_b32 v61, 3, v230\n"
    "v_lshl_add_u32 v61, v61, 4, v60\n"
    "v_add_u32 v62, 1, v230\n"
    "v_lshrrev_b32 v62, 3, v62\n"
    "v_lshl_add_u32 v62, v62, 4, v60\n"
    "v_mov_b32 v64, 0\n"
    "v_mov_b32 v65, 0\n"
    "v_mov_b32 v66, 0\n"
    "v_mov_b32 v67, 0\n"
    ".Lkw_27_%=:\n"
    "ds_read_b32 v1, v245 offset:32\n"
    "s_waitcnt lgkmcnt(0)\n"
    "v_readfirstlane_b32 s47, v1\n"
    "s_cmp_ge_u32 s47, 4\n"
    "s_cbranch_scc1 .Lkd_28_%=\n"
    "s_sleep 1\n"
    "s_sub_u32 s80, s80, 1\n"
    "s_cbranch_scc0 .Lkw_27_%=\n"
    "s_mov_b32 s80, 0\n"
    ".Lkd_28_%=:\n"
    "ds_write_b128 v60, v[64:67] offset:0\n"
    "ds_write_b128 v60, v[64:67] offset:1024\n"
    "ds_write_b128 v60, v[64:67] offset:6784\n"
    "ds_write_b128 v60, v[64:67] offset:7808\n"
    "ds_write_b128 v60, v[64:67] offset:13568\n"
    "ds_write_b128 v60, v[64:67] offset:14592\n"
    "ds_write_b128 v60, v[64:67] offset:20352\n"
    "ds_write_b128 v60, v[64:67] offset:21376\n"
    "ds_write_b128 v60, v[64:67] offset:27136\n"
    "ds_write_b128 v60, v[64:67] offset:28160\n"
    "ds_write_b128 v60, v[64:67] offset:33920\n"
    "ds_write_b128 v60, v[64:67] offset:34944\n"
    "s_mov_b64 exec, 0x7f\n"
    "ds_write_b128 v60, v[64:67] offset:2048\n"
    "ds_write_b128 v60, v[64:67] offset:8832\n"
    "ds_write_b128 v60, v[64:67] offset:15616\n"
    "ds_write_b128 v60, v[64:67] offset:22400\n"
    "ds_write_b128 v60, v[64:67] offset:29184\n"
    "ds_write_b128 v60, v[64:67] offset:35968\n"
    "s_mov_b64 exec, 1\n"
    "ds_write_b64 v60, v[64:65] offset:8944\n"
    "ds_write_b64 v60, v[64:65] offset:22512\n"
    "ds_write_b64 v60, v[64:65] offset:36080\n"
    "s_mov_b64 exec, -1\n"
    "s_waitcnt vmcnt(8)\n"
    "ds_write_b128 v61, v[8:11] offset:2160\n"
    "ds_write_b64 v61, v[8:9] offset:8952\n"
    "ds_write_b64 v62, v[10:11] offset:8960\n"
    "ds_write_b128 v61, v[12:15] offset:3312\n"
    "ds_write_b64 v61, v[12:13] offset:10104\n"
    "ds_write_b64 v62, v[14:15] offset:10112\n"
    "ds_write_b128 v61, v[16:19] offset:4464\n"
    "ds_write_b64 v61, v[16:17] offset:11256\n"
    "ds_write_b64 v62, v[18:19] offset:11264\n"
    "ds_write_b128 v61, v[20:23] offset:5616\n"
    "ds_write_b64 v61, v[20:21] offset:12408\n"
    "ds_write_b64 v62, v[22:23] offset:12416\n"
    "s_waitcnt vmcnt(4)\n"
    "ds_write_b128 v61, v[24:27] offset:15728\n"
    "ds_write_b64 v61, v[24:25] offset:22520\n"
    "ds_write_b64 v62, v[26:27] offset:22528\n"
    "ds_write_b128 v61, v[28:31] offset:16880\n"
    "ds_write_b64 v61, v[28:29] offset:23672\n"
    "ds_write_b64 v62, v[30:31] offset:23680\n"
    "ds_write_b128 v61, v[32:35] offset:18032\n"
    "ds_write_b64 v61, v[32:33] offset:24824\n"
    "ds_write_b64 v62, v[34:35] offset:24832\n"
    "ds_write_b128 v61, v[36:39] offset:19184\n"
    "ds_write_b64 v61, v[36:37] offset:25976\n"
    "ds_write_b64 v62, v[38:39] offset:25984\n"
    "s_waitcnt vmcnt(0)\n"
    "ds_write_b128 v61, v[40:43] offset:29296\n"
    "ds_write_b64 v61, v[40:41] offset:36088\n"
    "ds_write_b64 v62, v[42:43] offset:36096\n"
    "ds_write_b128 v61, v[44:47] offset:30448\n"
    "ds_write_b64 v61, v[44:45] offset:37240\n"
    "ds_write_b64 v62, v[46:47] offset:37248\n"
    "ds_write_b128 v61, v[48:51] offset:31600\n"
    "ds_write_b64 v61, v[48:49] offset:38392\n"
    "ds_write_b64 v62, v[50:51] offset:38400\n"
    "ds_write_b128 v61, v[52:55] offset:32752\n"
    "ds_write_b64 v61, v[52:53] offset:39544\n"
    "ds_write_b64 v62, v[54:55] offset:39552\n"
    "s_branch .Lkbq_8_%=\n"
    ".Lkkv_9_%=:\n"
    "s_lshl_b32 s38, s30, 4\n"
    "v_add_u32 v185, s38, v231\n"
    "v_lshrrev_b32 v237, 1, v232\n"
    "v_and_b32 v238, 1, v232\n"
    "v_lshlrev_b32 v238, 3, v238\n"
    "v_mul_u32_u24 v226, 1040, v185\n"
    "v_lshl_add_u32 v226, v237, 4, v226\n"
    "v_add3_u32 v226, v226, v238, s46\n"
    "v_lshrrev_b32 v239, 2, v185\n"
    "v_mul_u32_u24 v239, 1088, v239\n"
    "v_and_b32 v1, 3, v185\n"
    "v_lshl_add_u32 v239, v1, 8, v239\n"
    "v_add3_u32 v239, v239, v238, s45\n"
    "v_xor_b32 v237, v237, v1\n"
    "v_lshl_add_u32 v227, v237, 4, v239\n"
    "v_xor_b32 v237, 2, v237\n"
    "v_lshl_add_u32 v228, v237, 4, v239\n"
    "s_mul_i32 s38, s24, 3072\n"
    "s_add_u32 s38, s38, 49152\n"
    "v_mul_u32_u24 v237, 192, v231\n"
    "v_add_u32 v237, s38, v237\n"
    "v_lshl_add_u32 v186, v232, 3, v237\n"
    "v_min_u32 v238, 1, v232\n"
    "v_lshl_add_u32 v187, v238, 3, v186\n"
    "v_lshrrev_b32 v239, 1, v232\n"
    "v_lshl_add_u32 v188, v239, 3, v186\n"
    "v_and_b32 v239, v239, v232\n"
    "v_lshl_add_u32 v189, v239, 3, v186\n"
    "v_mul_u32_u24 v238, 48, v232\n"
    "v_add_u32 v190, v237, v238\n"
    "v_lshrrev_b32 v237, 2, v231\n"
    "v_lshl_add_u32 v237, v232, 2, v237\n"
    "v_mul_u32_u24 v192, 160, v237\n"
    "v_and_b32 v237, 3, v231\n"
    "v_lshl_add_u32 v192, v237, 3, v192\n"
    "v_add_u32 v193, 46592, v192\n"
    "v_add_u32 v192, 512, v192\n"
    "v_lshrrev_b32 v237, 2, v231\n"
    "v_lshl_add_u32 v237, v232, 2, v237\n"
    "v_mul_u32_u24 v196, 40, v237\n"
    "v_and_b32 v237, 3, v231\n"
    "v_lshl_add_u32 v196, v237, 3, v196\n"
    "v_add_u32 v196, 46592, v196\n"
    "s_and_b32 s39, s30, 1\n"
    "v_lshrrev_b32 v237, 1, v232\n"
    "v_and_b32 v238, 1, v232\n"
    "v_mov_b32 v198, 0x3f803f80\n"
    "v_mov_b32 v1, 2147483392\n"
    "s_bitcmp1_b32 s26, 0\n"
    "s_cbranch_scc1 .LkL_29_%=\n"
    "s_lshl_b32 s40, s39, 4\n"
    "v_add_u32 v239, s40, v231\n"
    "v_lshl_add_u32 v197, v237, 5, v239\n"
    "v_lshlrev_b32 v197, 4, v197\n"
    "v_lshl_add_u32 v197, v238, 3, v197\n"
    "s_branch .LkL_30_%=\n"
    ".LkL_29_%=:\n"
    "s_lshl_b32 s40, s39, 1\n"
    "v_add_u32 v238, s40, v238\n"
    "v_mul_u32_u24 v197, 336, v238\n"
    "v_mov_b32 v199, 0x3f803f80\n"
    "v_lshl_add_u32 v197, v231, 4, v197\n"
    "v_lshl_add_u32 v197, v237, 3, v197\n"
    "v_add_u32 v197, 1296, v197\n"
    ".LkL_30_%=:\n"
    "s_lshl_b32 s43, s25, 1\n"
    "s_lshr_b32 s40, s26, 2\n"
    "s_add_u32 s43, s43, s40\n"
    "s_mul_i32 s43, s43, 2688\n"
    "s_add_u32 s43, s43, 4096\n"
    "s_lshl_b32 s44, s25, 3\n"
    "s_add_u32 s44, s44, s26\n"
    "s_lshl_b32 s44, s44, 4\n"
    "s_add_u32 s44, s44, 180224\n"
    "s_bitcmp1_b32 s26, 0\n"
    "s_cselect_b32 s42, 1.0, 1051011931\n"
    "s_waitcnt vmcnt(1)\n"
    "ds_add_u32 v254, v235 offset:0\n"
    "s_mov_b32 s41, 512\n"
    "s_mov_b32 s38, 92160\n"
    "s_add_u32 m0, s41, 11520\n"
    "s_mov_b32 exec_hi, 0x0fffffff\n"
    "buffer_load_dwordx4 v248, s[8:11], s38 offen lds\n"
    "s_add_u32 m0, m0, 960\n"
    "s_add_u32 s38, s38, 7680\n"
    "buffer_load_dwordx4 v248, s[8:11], s38 offen lds\n"
    "s_add_u32 m0, m0, 960\n"
    "s_add_u32 s38, s38, 7680\n"
    "buffer_load_dwordx4 v248, s[8:11], s38 offen lds\n"
    "s_add_u32 m0, m0, 960\n"
    "s_add_u32 s38, s38, 7680\n"
    "buffer_load_dwordx4 v248, s[8:11], s38 offen lds\n"
    "s_add_u32 m0, m0, 960\n"
    "s_add_u32 s38, s38, 7680\n"
    "buffer_load_dwordx4 v248, s[8:11], s38 offen lds\n"
    "s_mov_b64 exec, -1\n"
    ".Lkw_31_%=:\n"
    "ds_read_b32 v1, v245 offset:0\n"
    "s_waitcnt lgkmcnt(0)\n"
    "v_readfirstlane_b32 s47, v1\n"
    "s_cmp_ge_u32 s47, 8\n"
    "s_cbranch_scc1 .Lkd_32_%=\n"
    "s_sleep 1\n"
    "s_sub_u32 s80, s80, 1\n"
    "s_cbranch_scc0 .Lkw_31_%=\n"
    "s_mov_b32 s80, 0\n"
    ".Lkd_32_%=:\n"
    "ds_read_b64 v[2:3], v226 offset:0\n"
    "ds_read_b64 v[4:5], v226 offset:32\n"
    "ds_read_b64 v[6:7], v226 offset:64\n"
    "ds_read_b64 v[8:9], v226 offset:96\n"
    "ds_read_b64 v[10:11], v226 offset:128\n"
    "ds_read_b64 v[12:13], v226 offset:160\n"
    "ds_read_b64 v[14:15], v226 offset:192\n"
    "ds_read_b64 v[16:17], v226 offset:224\n"
    "ds_read_b64 v[18:19], v226 offset:256\n"
    "ds_read_b64 v[20:21], v226 offset:288\n"
    "ds_read_b64 v[22:23], v226 offset:320\n"
    "ds_read_b64 v[24:25], v226 offset:352\n"
    "ds_read_b64 v[26:27], v226 offset:384\n"
    "ds_read_b64 v[28:29], v226 offset:416\n"
    "ds_read_b64 v[30:31], v226 offset:448\n"
    "ds_read_b64 v[32:33], v226 offset:480\n"
    "ds_read_b64 v[34:35], v226 offset:512\n"
    "ds_read_b64 v[36:37], v226 offset:544\n"
    "ds_read_b64 v[38:39], v226 offset:576\n"
    "ds_read_b64 v[40:41], v226 offset:608\n"
    "ds_read_b64 v[42:43], v226 offset:640\n"
    "ds_read_b64 v[44:45], v226 offset:672\n"
    "ds_read_b64 v[46:47], v226 offset:704\n"
    "ds_read_b64 v[48:49], v226 offset:736\n"
    "ds_read_b64 v[50:51], v226 offset:768\n"
    "ds_read_b64 v[52:53], v226 offset:800\n"
    "ds_read_b64 v[54:55], v226 offset:832\n"
    "ds_read_b64 v[56:57], v226 offset:864\n"
    "ds_read_b64 v[58:59], v226 offset:896\n"
    "ds_read_b64 v[60:61], v226 offset:928\n"
    "ds_read_b64 v[62:63], v226 offset:960\n"
    "ds_read_b64 v[64:65], v226 offset:992\n"
    "ds_read_b64 v[66:67], v227 offset:0\n"
    "ds_read_b64 v[68:69], v228 offset:0\n"
    "ds_read_b64 v[70:71], v227 offset:64\n"
    "ds_read_b64 v[72:73], v228 offset:64\n"
    "ds_read_b64 v[74:75], v227 offset:128\n"
    "ds_read_b64 v[76:77], v228 offset:128\n"
    "ds_read_b64 v[78:79], v227 offset:192\n"
    "ds_read_b64 v[80:81], v228 offset:192\n"
    "s_bitcmp1_b32 s26, 0\n"
    "s_cbranch_scc1 .LkL_33_%=\n"
    "ds_read_b64_tr_b16 v[82:83], v196 offset:0\n"
    "ds_read_b64_tr_b16 v[84:85], v196 offset:640\n"
    "ds_read_b64_tr_b16 v[86:87], v196 offset:1280\n"
    "ds_read_b64_tr_b16 v[88:89], v196 offset:1920\n"
    "ds_read_b64_tr_b16 v[90:91], v196 offset:2560\n"
    "ds_read_b64_tr_b16 v[92:93], v196 offset:3200\n"
    "ds_read_b64_tr_b16 v[94:95], v196 offset:3840\n"
    "ds_read_b64_tr_b16 v[96:97], v196 offset:4480\n"
    "ds_read_b64_tr_b16 v[98:99], v196 offset:5120\n"
    "ds_read_b64_tr_b16 v[100:101], v196 offset:5760\n"
    "ds_read_b64_tr_b16 v[102:103], v196 offset:6400\n"
    "ds_read_b64_tr_b16 v[104:105], v196 offset:7040\n"
    "ds_read_b64_tr_b16 v[106:107], v196 offset:7680\n"
    "ds_read_b64_tr_b16 v[108:109], v196 offset:8320\n"
    "s_waitcnt lgkmcnt(12)\n"
    "ds_add_u32 v254, v235 offset:4\n"
    "v_mfma_f32_16x16x32_bf16 v[162:165], v[82:85], v[2:5], 0\n"
    "ds_read_b64_tr_b16 v[110:111], v196 offset:8960\n"
    "ds_read_b64_tr_b16 v[112:113], v196 offset:9600\n"
    "s_waitcnt lgkmcnt(13)\n"
    "v_mfma_f32_16x16x32_bf16 v[162:165], v[86:89], v[6:9], v[162:165]\n"
    "ds_read_b64_tr_b16 v[82:83], v196 offset:10240\n"
    "ds_read_b64_tr_b16 v[84:85], v196 offset:10880\n"
    "s_waitcnt lgkmcnt(13)\n"
    "v_mfma_f32_16x16x32_bf16 v[162:165], v[90:93], v[10:13], v[162:165]\n"
    "ds_read_b64_tr_b16 v[86:87], v196 offset:11520\n"
    "ds_read_b64_tr_b16 v[88:89], v196 offset:12160\n"
    "s_waitcnt lgkmcnt(13)\n"
    "v_mfma_f32_16x16x32_bf16 v[162:165], v[94:97], v[14:17], v[162:165]\n"
    "ds_read_b64_tr_b16 v[90:91], v196 offset:12800\n"
    "ds_read_b64_tr_b16 v[92:93], v196 offset:13440\n"
    "s_waitcnt lgkmcnt(13)\n"
    "v_mfma_f32_16x16x32_bf16 v[162:165], v[98:101], v[18:21], v[162:165]\n"
    "ds_read_b64_tr_b16 v[94:95], v196 offset:14080\n"
    "ds_read_b64_tr_b16 v[96:97], v196 offset:14720\n"
    "s_waitcnt lgkmcnt(13)\n"
    "v_mfma_f32_16x16x32_bf16 v[162:165], v[102:105], v[22:25], v[162:165]\n"
    "ds_read_b64_tr_b16 v[98:99], v196 offset:15360\n"
    "ds_read_b64_tr_b16 v[100:101], v196 offset:16000\n"
    "s_waitcnt lgkmcnt(13)\n"
    "v_mfma_f32_16x16x32_bf16 v[162:165], v[106:109], v[26:29], v[162:165]\n"
    "ds_read_b64_tr_b16 v[102:103], v196 offset:16640\n"
    "ds_read_b64_tr_b16 v[104:105], v196 offset:17280\n"
    "s_waitcnt lgkmcnt(12)\n"
    "v_mfma_f32_16x16x32_bf16 v[162:165], v[110:113], v[30:33], v[162:165]\n"
    "ds_read_b64_tr_b16 v[106:107], v196 offset:17920\n"
    "ds_read_b64_tr_b16 v[108:109], v196 offset:18560\n"
    "s_waitcnt lgkmcnt(12)\n"
    "v_mfma_f32_16x16x32_bf16 v[162:165], v[82:85], v[34:37], v[162:165]\n"
    "ds_read_b64_tr_b16 v[110:111], v196 offset:19200\n"
    "ds_read_b64_tr_b16 v[112:113], v196 offset:19840\n"
    "s_waitcnt lgkmcnt(12)\n"
    "v_mfma_f32_16x16x32_bf16 v[162:165], v[86:89], v[38:41], v[162:165]\n"
    "ds_read_b64_tr_b16 v[82:83], v196 offset:20480\n"
    "ds_read_b64_tr_b16 v[84:85], v196 offset:21120\n"
    "s_waitcnt lgkmcnt(12)\n"
    "v_mfma_f32_16x16x32_bf16 v[162:165], v[90:93], v[42:45], v[162:165]\n"
    "ds_read_b64_tr_b16 v[86:87], v196 offset:21760\n"
    "ds_read_b64_tr_b16 v[88:89], v196 offset:22400\n"
    "s_waitcnt lgkmcnt(12)\n"
    "v_mfma_f32_16x16x32_bf16 v[162:165], v[94:97], v[46:49], v[162:165]\n"
    "ds_read_b64_tr_b16 v[90:91], v196 offset:23040\n"
    "ds_read_b64_tr_b16 v[92:93], v196 offset:23680\n"
    "s_waitcnt lgkmcnt(12)\n"
    "v_mfma_f32_16x16x32_bf16 v[162:165], v[98:101], v[50:53], v[162:165]\n"
    "ds_read_b64_tr_b16 v[94:95], v196 offset:24320\n"
    "ds_read_b64_tr_b16 v[96:97], v196 offset:24960\n"
    "s_waitcnt lgkmcnt(12)\n"
    "v_mfma_f32_16x16x32_bf16 v[162:165], v[102:105], v[54:57], v[162:165]\n"
    "s_waitcnt lgkmcnt(10)\n"
    "v_mfma_f32_16x16x32_bf16 v[162:165], v[106:109], v[58:61], v[162:165]\n"
    "s_waitcnt lgkmcnt(8)\n"
    "v_mfma_f32_16x16x32_bf16 v[162:165], v[110:113], v[62:65], v[162:165]\n"
    "s_waitcnt lgkmcnt(6)\n"
    "v_mfma_f32_16x16x32_bf16 v[162:165], v[82:85], v[66:69], v[162:165]\n"
    "s_waitcnt lgkmcnt(4)\n"
    "v_mfma_f32_16x16x32_bf16 v[162:165], v[86:89], v[70:73], v[162:165]\n"
    "s_waitcnt lgkmcnt(2)\n"
    "v_mfma_f32_16x16x32_bf16 v[162:165], v[90:93], v[74:77], v[162:165]\n"
    "s_waitcnt lgkmcnt(0)\n"
    "v_mfma_f32_16x16x32_bf16 v[162:165], v[94:97], v[78:81], v[162:165]\n"
    "s_branch .LkL_34_%=\n"
    ".LkL_33_%=:\n"
    "ds_read_b64_tr_b16 v[82:83], v196 offset:0\n"
    "ds_read_b64_tr_b16 v[84:85], v196 offset:640\n"
    "ds_read_b64_tr_b16 v[86:87], v196 offset:1280\n"
    "ds_read_b64_tr_b16 v[88:89], v196 offset:1920\n"
    "ds_read_b64_tr_b16 v[90:91], v196 offset:2560\n"
    "ds_read_b64_tr_b16 v[92:93], v196 offset:3200\n"
    "ds_read_b64_tr_b16 v[94:95], v196 offset:3840\n"
    "ds_read_b64_tr_b16 v[96:97], v196 offset:4480\n"
    "ds_read_b64_tr_b16 v[98:99], v196 offset:5120\n"
    "ds_read_b64_tr_b16 v[100:101], v196 offset:5760\n"
    "ds_read_b64_tr_b16 v[102:103], v196 offset:6400\n"
    "ds_read_b64_tr_b16 v[104:105], v196 offset:7040\n"
    "ds_read_b64_tr_b16 v[106:107], v196 offset:7680\n"
    "ds_read_b64_tr_b16 v[108:109], v196 offset:8320\n"
    "s_waitcnt lgkmcnt(12)\n"
    "ds_add_u32 v254, v235 offset:4\n"
    "v_mfma_f32_16x16x32_bf16 v[162:165], v[2:5], v[82:85], 0\n"
    "ds_read_b64_tr_b16 v[110:111], v196 offset:8960\n"
    "ds_read_b64_tr_b16 v[112:113], v196 offset:9600\n"
    "s_waitcnt lgkmcnt(13)\n"
    "v_mfma_f32_16x16x32_bf16 v[162:165], v[6:9], v[86:89], v[162:165]\n"
    "ds_read_b64_tr_b16 v[82:83], v196 offset:10240\n"
    "ds_read_b64_tr_b16 v[84:85], v196 offset:10880\n"
    "s_waitcnt lgkmcnt(13)\n"
    "v_mfma_f32_16x16x32_bf16 v[162:165], v[10:13], v[90:93], v[162:165]\n"
    "ds_read_b64_tr_b16 v[86:87], v196 offset:11520\n"
    "ds_read_b64_tr_b16 v[88:89], v196 offset:12160\n"
    "s_waitcnt lgkmcnt(13)\n"
    "v_mfma_f32_16x16x32_bf16 v[162:165], v[14:17], v[94:97], v[162:165]\n"
    "ds_read_b64_tr_b16 v[90:91], v196 offset:12800\n"
    "ds_read_b64_tr_b16 v[92:93], v196 offset:13440\n"
    "s_waitcnt lgkmcnt(13)\n"
    "v_mfma_f32_16x16x32_bf16 v[162:165], v[18:21], v[98:101], v[162:165]\n"
    "ds_read_b64_tr_b16 v[94:95], v196 offset:14080\n"
    "ds_read_b64_tr_b16 v[96:97], v196 offset:14720\n"
    "s_waitcnt lgkmcnt(13)\n"
    "v_mfma_f32_16x16x32_bf16 v[162:165], v[22:25], v[102:105], v[162:165]\n"
    "ds_read_b64_tr_b16 v[98:99], v196 offset:15360\n"
    "ds_read_b64_tr_b16 v[100:101], v196 offset:16000\n"
    "s_waitcnt lgkmcnt(13)\n"
    "v_mfma_f32_16x16x32_bf16 v[162:165], v[26:29], v[106:109], v[162:165]\n"
    "ds_read_b64_tr_b16 v[102:103], v196 offset:16640\n"
    "ds_read_b64_tr_b16 v[104:105], v196 offset:17280\n"
    "s_waitcnt lgkmcnt(12)\n"
    "v_mfma_f32_16x16x32_bf16 v[162:165], v[30:33], v[110:113], v[162:165]\n"
    "ds_read_b64_tr_b16 v[106:107], v196 offset:17920\n"
    "ds_read_b64_tr_b16 v[108:109], v196 offset:18560\n"
    "s_waitcnt lgkmcnt(12)\n"
    "v_mfma_f32_16x16x32_bf16 v[162:165], v[34:37], v[82:85], v[162:165]\n"
    "ds_read_b64_tr_b16 v[110:111], v196 offset:19200\n"
    "ds_read_b64_tr_b16 v[112:113], v196 offset:19840\n"
    "s_waitcnt lgkmcnt(12)\n"
    "v_mfma_f32_16x16x32_bf16 v[162:165], v[38:41], v[86:89], v[162:165]\n"
    "ds_read_b64_tr_b16 v[82:83], v196 offset:20480\n"
    "ds_read_b64_tr_b16 v[84:85], v196 offset:21120\n"
    "s_waitcnt lgkmcnt(12)\n"
    "v_mfma_f32_16x16x32_bf16 v[162:165], v[42:45], v[90:93], v[162:165]\n"
    "ds_read_b64_tr_b16 v[86:87], v196 offset:21760\n"
    "ds_read_b64_tr_b16 v[88:89], v196 offset:22400\n"
    "s_waitcnt lgkmcnt(12)\n"
    "v_mfma_f32_16x16x32_bf16 v[162:165], v[46:49], v[94:97], v[162:165]\n"
    "ds_read_b64_tr_b16 v[90:91], v196 offset:23040\n"
    "ds_read_b64_tr_b16 v[92:93], v196 offset:23680\n"
    "s_waitcnt lgkmcnt(12)\n"
    "v_mfma_f32_16x16x32_bf16 v[162:165], v[50:53], v[98:101], v[162:165]\n"
    "ds_read_b64_tr_b16 v[94:95], v196 offset:24320\n"
    "ds_read_b64_tr_b16 v[96:97], v196 offset:24960\n"
    "s_waitcnt lgkmcnt(12)\n"
    "v_mfma_f32_16x16x32_bf16 v[162:165], v[54:57], v[102:105], v[162:165]\n"
    "s_waitcnt lgkmcnt(10)\n"
    "v_mfma_f32_16x16x32_bf16 v[162:165], v[58:61], v[106:109], v[162:165]\n"
    "s_waitcnt lgkmcnt(8)\n"
    "v_mfma_f32_16x16x32_bf16 v[162:165], v[62:65], v[110:113], v[162:165]\n"
    "s_waitcnt lgkmcnt(6)\n"
    "v_mfma_f32_16x16x32_bf16 v[162:165], v[66:69], v[82:85], v[162:165]\n"
    "s_waitcnt lgkmcnt(4)\n"
    "v_mfma_f32_16x16x32_bf16 v[162:165], v[70:73], v[86:89], v[162:165]\n"
    "s_waitcnt lgkmcnt(2)\n"
    "v_mfma_f32_16x16x32_bf16 v[162:165], v[74:77], v[90:93], v[162:165]\n"
    "s_waitcnt lgkmcnt(0)\n"
    "v_mfma_f32_16x16x32_bf16 v[162:165], v[78:81], v[94:97], v[162:165]\n"
    ".LkL_34_%=:\n"
    "ds_add_u32 v254, v235 offset:8\n"
    "s_waitcnt vmcnt(5)\n"
    "v_readfirstlane_b32 s36, v252\n"
    "v_readfirstlane_b32 s37, v253\n"
    "s_waitcnt lgkmcnt(0)\n"
    "s_add_u32 s36, s36, 1\n"
    "s_addc_u32 s37, s37, 0\n"
    "s_lshl_b64 s[34:35], s[36:37], 16\n"
    "s_nop 2\n"
    "s_nop 1\n"
    "v_mul_f32 v162, s42, v162\n"
    "v_mul_f32 v163, s42, v163\n"
    "v_mul_f32 v164, s42, v164\n"
    "v_mul_f32 v165, s42, v165\n"
    "v_cvt_pk_bf16_f32 v200, v162, v163\n"
    "v_cvt_pk_bf16_f32 v201, v164, v165\n"
    "buffer_store_dwordx2 v[200:201], v197, s[12:15], s43 offen sc1\n"
    "v_mul_f32 v198, v162, v162\n"
    "v_cmp_gt_u32 vcc, 16, v230\n"
    "v_fmac_f32 v198, v163, v163\n"
    "v_fmac_f32 v198, v164, v164\n"
    "v_fmac_f32 v198, v165, v165\n"
    "v_mov_b32 v199, v198\n"
    "s_nop 1\n"
    "v_permlane32_swap_b32 v198, v199\n"
    "v_add_f32 v198, v198, v199\n"
    "v_mov_b32 v199, v198\n"
    "s_nop 1\n"
    "v_permlane16_swap_b32 v198, v199\n"
    "v_add_f32 v198, v198, v199\n"
    "s_nop 1\n"
    "v_max_u32_dpp v198, v198, v198 row_ror:8 row_mask:0xf bank_mask:0xf\n"
    "s_nop 1\n"
    "v_max_u32_dpp v198, v198, v198 row_ror:4 row_mask:0xf bank_mask:0xf\n"
    "s_nop 1\n"
    "v_max_u32_dpp v198, v198, v198 row_ror:2 row_mask:0xf bank_mask:0xf\n"
    "s_nop 1\n"
    "v_max_u32_dpp v198, v198, v198 row_ror:1 row_mask:0xf bank_mask:0xf\n"
    "s_nop 1\n"
    "v_mul_f32 v198, 0x3f828f5c, v198\n"
    "s_bitcmp1_b32 s26, 0\n"
    "s_cselect_b32 s38, 0, 0x7f80\n"
    "v_lshrrev_b32 v198, 16, v198\n"
    "v_add_u32 v198, 1, v198\n"
    "v_min_u32 v198, s38, v198\n"
    "s_waitcnt vmcnt(1)\n"
    "ds_add_u32 v254, v235 offset:12\n"
    ".Lkw_35_%=:\n"
    "ds_read_b32 v1, v245 offset:8\n"
    "s_waitcnt lgkmcnt(0)\n"
    "v_readfirstlane_b32 s47, v1\n"
    "s_cmp_ge_u32 s47, 2\n"
    "s_cbranch_scc1 .Lkd_36_%=\n"
    "s_sleep 1\n"
    "s_sub_u32 s80, s80, 1\n"
    "s_cbranch_scc0 .Lkw_35_%=\n"
    "s_mov_b32 s80, 0\n"
    ".Lkd_36_%=:\n"
    "s_mov_b32 s41, 512\n"
    "s_mov_b32 s38, 437760\n"
    "s_add_u32 m0, s41, 54720\n"
    "s_mov_b32 exec_hi, 0x0fffffff\n"
    "buffer_load_dwordx4 v248, s[8:11], s38 offen lds\n"
    "s_add_u32 m0, m0, 960\n"
    "s_add_u32 s38, s38, 7680\n"
    "buffer_load_dwordx4 v248, s[8:11], s38 offen lds\n"
    "s_add_u32 m0, m0, 960\n"
    "s_add_u32 s38, s38, 7680\n"
    "buffer_load_dwordx4 v248, s[8:11], s38 offen lds\n"
    "s_add_u32 m0, m0, 960\n"
    "s_add_u32 s38, s38, 7680\n"
    "buffer_load_dwordx4 v248, s[8:11], s38 offen lds\n"
    "s_add_u32 m0, m0, 960\n"
    "s_add_u32 s38, s38, 7680\n"
    "buffer_load_dwordx4 v248, s[8:11], s38 offen lds\n"
    "s_add_u32 m0, m0, 960\n"
    "s_add_u32 s38, s38, 7680\n"
    "buffer_load_dwordx4 v248, s[8:11], s38 offen lds\n"
    "s_add_u32 m0, m0, 960\n"
    "s_add_u32 s38, s38, 7680\n"
    "buffer_load_dwordx4 v248, s[8:11], s38 offen lds\n"
    "s_add_u32 m0, m0, 960\n"
    "s_add_u32 s38, s38, 7680\n"
    "buffer_load_dwordx4 v248, s[8:11], s38 offen lds\n"
    "s_add_u32 m0, m0, 960\n"
    "s_add_u32 s38, s38, 7680\n"
    "buffer_load_dwordx4 v248, s[8:11], s38 offen lds\n"
    "s_mov_b64 exec, -1\n"
    "s_waitcnt vmcnt(9)\n"
    "v_or_b32 v252, s34, v198\n"
    "v_mov_b32 v253, s35\n"
    "buffer_store_dwordx2 v[252:253], v254, s[12:15], s44 offen sc1\n"
    "v_mul_u32_u24 v243, 192, v233\n"
    "v_add_u32 v243, 49152, v243\n"
    "v_lshl_add_u32 v242, v234, 4, v243\n"
    "v_lshl_add_u32 v243, v234, 3, v243\n"
    "v_lshlrev_b32 v237, 3, v233\n"
    "v_add_u32 v237, 1024, v237\n"
    "v_mov_b32 v238, 1280\n"
    "v_cmp_eq_u32 vcc, 0, v234\n"
    "v_mul_u32_u24 v157, 336, v234\n"
    "v_lshl_add_u32 v157, v233, 4, v157\n"
    "v_cndmask_b32 v156, v238, v237, vcc\n"
    "v_add_u32 v157, 1296, v157\n"
    "v_cmp_gt_u32 vcc, 21, v233\n"
    "v_mov_b32 v237, 2147483392\n"
    "v_lshlrev_b32 v158, 3, v230\n"
    "v_cndmask_b32 v157, v237, v157, vcc\n"
    "s_mul_i32 s90, s24, 21504\n"
    "s_add_u32 s90, s90, 4096\n"
    "s_lshl_b32 s39, s24, 9\n"
    "s_add_u32 s39, s39, 180224\n"
    "v_add_u32 v158, s39, v158\n"
    ".Lkq_10_%=:\n"
    "s_setprio 3\n"
    ".Lkw_37_%=:\n"
    "ds_read_b32 v1, v245 offset:12\n"
    "s_waitcnt lgkmcnt(0)\n"
    "v_readfirstlane_b32 s47, v1\n"
    "s_cmp_ge_u32 s47, 5\n"
    "s_cbranch_scc1 .Lkd_38_%=\n"
    "s_sleep 1\n"
    "s_sub_u32 s80, s80, 1\n"
    "s_cbranch_scc0 .Lkw_37_%=\n"
    "s_mov_b32 s80, 0\n"
    ".Lkd_38_%=:\n"
    "ds_read_b64_tr_b16 v[82:83], v192 offset:0\n"
    "ds_read_b64_tr_b16 v[84:85], v192 offset:2560\n"
    "ds_read_b64_tr_b16 v[86:87], v192 offset:32\n"
    "ds_read_b64_tr_b16 v[88:89], v192 offset:2592\n"
    "ds_read_b64_tr_b16 v[90:91], v192 offset:64\n"
    "ds_read_b64_tr_b16 v[92:93], v192 offset:2624\n"
    "ds_read_b64_tr_b16 v[94:95], v192 offset:96\n"
    "ds_read_b64_tr_b16 v[96:97], v192 offset:2656\n"
    "ds_read_b64_tr_b16 v[98:99], v192 offset:128\n"
    "ds_read_b64_tr_b16 v[100:101], v192 offset:2688\n"
    "ds_read_b64_tr_b16 v[102:103], v192 offset:5120\n"
    "ds_read_b64_tr_b16 v[104:105], v192 offset:7680\n"
    "ds_read_b64_tr_b16 v[106:107], v192 offset:5152\n"
    "ds_read_b64_tr_b16 v[108:109], v192 offset:7712\n"
    "ds_read_b64_tr_b16 v[110:111], v192 offset:5184\n"
    "ds_read_b64_tr_b16 v[112:113], v192 offset:7744\n"
    "s_waitcnt lgkmcnt(12)\n"
    "v_mfma_f32_16x16x32_bf16 v[162:165], v[82:85], v[2:5], 0\n"
    "ds_read_b32 v223, v245 offset:8\n"
    "ds_read_b64_tr_b16 v[114:115], v192 offset:5216\n"
    "ds_read_b64_tr_b16 v[116:117], v192 offset:7776\n"
    "v_mfma_f32_16x16x32_bf16 v[166:169], v[86:89], v[2:5], 0\n"
    "ds_read_b64_tr_b16 v[118:119], v192 offset:5248\n"
    "ds_read_b64_tr_b16 v[120:121], v192 offset:7808\n"
    "s_waitcnt lgkmcnt(13)\n"
    "v_mfma_f32_16x16x32_bf16 v[170:173], v[90:93], v[2:5], 0\n"
    "s_waitcnt lgkmcnt(4)\n"
    ".Lkc_41_%=:\n"
    "v_readfirstlane_b32 s47, v223\n"
    "s_cmp_ge_u32 s47, 2\n"
    "s_cbranch_scc1 .Lkd_42_%=\n"
    "ds_read_b32 v223, v245 offset:8\n"
    "s_sleep 1\n"
    "s_waitcnt lgkmcnt(0)\n"
    "s_sub_u32 s80, s80, 1\n"
    "s_cbranch_scc0 .Lkc_41_%=\n"
    "s_mov_b32 s80, 0\n"
    ".Lkd_42_%=:\n"
    "s_mov_b32 s41, 512\n"
    "s_cmp_eq_u32 s33, 0\n"
    "s_cbranch_scc1 .LkL_40_%=\n"
    "s_mul_i32 s40, s33, 3\n"
    "s_add_u32 s40, s40, 63\n"
    "s_mul_i32 s38, s40, 7680\n"
    "s_mul_i32 s39, s40, 960\n"
    "s_add_u32 m0, s39, s41\n"
    "s_mov_b32 exec_hi, 0x0fffffff\n"
    "buffer_load_dwordx4 v248, s[8:11], s38 offen lds\n"
    "s_add_u32 m0, m0, 960\n"
    "s_add_u32 s38, s38, 7680\n"
    "buffer_load_dwordx4 v248, s[8:11], s38 offen lds\n"
    "s_add_u32 m0, m0, 960\n"
    "s_add_u32 s38, s38, 7680\n"
    "buffer_load_dwordx4 v248, s[8:11], s38 offen lds\n"
    "s_mov_b64 exec, -1\n"
    ".LkL_40_%=:\n"
    "ds_read_b64_tr_b16 v[82:83], v192 offset:10240\n"
    "ds_read_b64_tr_b16 v[84:85], v192 offset:12800\n"
    "v_mfma_f32_16x16x32_bf16 v[174:177], v[94:97], v[2:5], 0\n"
    "ds_read_b64_tr_b16 v[86:87], v192 offset:10272\n"
    "ds_read_b64_tr_b16 v[88:89], v192 offset:12832\n"
    "v_mfma_f32_16x16x32_bf16 v[178:181], v[98:101], v[2:5], 0\n"
    "ds_read_b64_tr_b16 v[90:91], v192 offset:10304\n"
    "ds_read_b64_tr_b16 v[92:93], v192 offset:12864\n"
    "v_mfma_f32_16x16x32_bf16 v[162:165], v[102:105], v[6:9], v[162:165]\n"
    "ds_read_b64_tr_b16 v[94:95], v192 offset:10336\n"
    "ds_read_b64_tr_b16 v[96:97], v192 offset:12896\n"
    "v_mfma_f32_16x16x32_bf16 v[166:169], v[106:109], v[6:9], v[166:169]\n"
    "ds_read_b64_tr_b16 v[98:99], v192 offset:10368\n"
    "ds_read_b64_tr_b16 v[100:101], v192 offset:12928\n"
    "v_mfma_f32_16x16x32_bf16 v[170:173], v[110:113], v[6:9], v[170:173]\n"
    "ds_read_b64_tr_b16 v[102:103], v192 offset:15360\n"
    "ds_read_b64_tr_b16 v[104:105], v192 offset:17920\n"
    "s_waitcnt lgkmcnt(12)\n"
    "v_mfma_f32_16x16x32_bf16 v[174:177], v[114:117], v[6:9], v[174:177]\n"
    "ds_read_b64_tr_b16 v[106:107], v192 offset:15392\n"
    "ds_read_b64_tr_b16 v[108:109], v192 offset:17952\n"
    "v_mfma_f32_16x16x32_bf16 v[178:181], v[118:121], v[6:9], v[178:181]\n"
    "ds_read_b64_tr_b16 v[110:111], v192 offset:15424\n"
    "ds_read_b64_tr_b16 v[112:113], v192 offset:17984\n"
    "s_waitcnt lgkmcnt(12)\n"
    "v_mfma_f32_16x16x32_bf16 v[162:165], v[82:85], v[10:13], v[162:165]\n"
    "ds_read_b32 v222, v245 offset:4\n"
    "ds_read_b32 v223, v245 offset:60\n"
    "ds_read_b64_tr_b16 v[114:115], v192 offset:15456\n"
    "ds_read_b64_tr_b16 v[116:117], v192 offset:18016\n"
    "v_mfma_f32_16x16x32_bf16 v[166:169], v[86:89], v[10:13], v[166:169]\n"
    "ds_read_b64_tr_b16 v[118:119], v192 offset:15488\n"
    "ds_read_b64_tr_b16 v[120:121], v192 offset:18048\n"
    "s_waitcnt lgkmcnt(14)\n"
    "v_mfma_f32_16x16x32_bf16 v[170:173], v[90:93], v[10:13], v[170:173]\n"
    "s_waitcnt lgkmcnt(5)\n"
    ".Lkc_43_%=:\n"
    "v_readfirstlane_b32 s47, v222\n"
    "s_cmp_ge_u32 s47, 8\n"
    "s_cbranch_scc1 .Lkd_44_%=\n"
    "ds_read_b32 v222, v245 offset:4\n"
    "s_sleep 1\n"
    "s_waitcnt lgkmcnt(0)\n"
    "s_sub_u32 s80, s80, 1\n"
    "s_cbranch_scc0 .Lkc_43_%=\n"
    "s_mov_b32 s80, 0\n"
    ".Lkd_44_%=:\n"
    "s_add_u32 s40, s24, 81\n"
    "s_mul_i32 s38, s40, 7680\n"
    "s_mul_i32 s39, s40, 960\n"
    "s_add_u32 m0, s39, s41\n"
    "s_mov_b32 exec_hi, 0x0fffffff\n"
    "buffer_load_dwordx4 v248, s[8:11], s38 offen lds\n"
    "s_add_u32 m0, m0, 3840\n"
    "s_add_u32 s38, s38, 30720\n"
    "buffer_load_dwordx4 v248, s[8:11], s38 offen lds\n"
    "s_add_u32 m0, m0, 3840\n"
    "s_add_u32 s38, s38, 30720\n"
    "buffer_load_dwordx4 v248, s[8:11], s38 offen lds\n"
    "s_mov_b64 exec, -1\n"
    "s_waitcnt lgkmcnt(4)\n"
    ".Lkc_45_%=:\n"
    "v_readfirstlane_b32 s47, v223\n"
    "s_cmp_ge_u32 s47, 3\n"
    "s_cbranch_scc1 .Lkd_46_%=\n"
    "ds_read_b32 v223, v245 offset:60\n"
    "s_sleep 1\n"
    "s_waitcnt lgkmcnt(0)\n"
    "s_sub_u32 s80, s80, 1\n"
    "s_cbranch_scc0 .Lkc_45_%=\n"
    "s_mov_b32 s80, 0\n"
    ".Lkd_46_%=:\n"
    "ds_read_b64_tr_b16 v[82:83], v192 offset:20480\n"
    "ds_read_b64_tr_b16 v[84:85], v192 offset:23040\n"
    "v_mfma_f32_16x16x32_bf16 v[174:177], v[94:97], v[10:13], v[174:177]\n"
    "ds_read_b64_tr_b16 v[86:87], v192 offset:20512\n"
    "ds_read_b64_tr_b16 v[88:89], v192 offset:23072\n"
    "v_mfma_f32_16x16x32_bf16 v[178:181], v[98:101], v[10:13], v[178:181]\n"
    "ds_read_b64_tr_b16 v[90:91], v192 offset:20544\n"
    "ds_read_b64_tr_b16 v[92:93], v192 offset:23104\n"
    "v_mfma_f32_16x16x32_bf16 v[162:165], v[102:105], v[14:17], v[162:165]\n"
    "ds_read_b64_tr_b16 v[94:95], v192 offset:20576\n"
    "ds_read_b64_tr_b16 v[96:97], v192 offset:23136\n"
    "v_mfma_f32_16x16x32_bf16 v[166:169], v[106:109], v[14:17], v[166:169]\n"
    "ds_read_b64_tr_b16 v[98:99], v192 offset:20608\n"
    "ds_read_b64_tr_b16 v[100:101], v192 offset:23168\n"
    "v_mfma_f32_16x16x32_bf16 v[170:173], v[110:113], v[14:17], v[170:173]\n"
    "ds_read_b64_tr_b16 v[102:103], v192 offset:25600\n"
    "ds_read_b64_tr_b16 v[104:105], v192 offset:28160\n"
    "s_waitcnt lgkmcnt(12)\n"
    "v_mfma_f32_16x16x32_bf16 v[174:177], v[114:117], v[14:17], v[174:177]\n"
    "ds_read_b64_tr_b16 v[106:107], v192 offset:25632\n"
    "ds_read_b64_tr_b16 v[108:109], v192 offset:28192\n"
    "v_mfma_f32_16x16x32_bf16 v[178:181], v[118:121], v[14:17], v[178:181]\n"
    "ds_read_b64_tr_b16 v[110:111], v192 offset:25664\n"
    "ds_read_b64_tr_b16 v[112:113], v192 offset:28224\n"
    "s_waitcnt lgkmcnt(12)\n"
    "v_mfma_f32_16x16x32_bf16 v[162:165], v[82:85], v[18:21], v[162:165]\n"
    "ds_read_b64_tr_b16 v[114:115], v192 offset:25696\n"
    "ds_read_b64_tr_b16 v[116:117], v192 offset:28256\n"
    "v_mfma_f32_16x16x32_bf16 v[166:169], v[86:89], v[18:21], v[166:169]\n"
    "ds_read_b64_tr_b16 v[118:119], v192 offset:25728\n"
    "ds_read_b64_tr_b16 v[120:121], v192 offset:28288\n"
    "s_waitcnt lgkmcnt(12)\n"
    "v_mfma_f32_16x16x32_bf16 v[170:173], v[90:93], v[18:21], v[170:173]\n"
    "ds_read_b64_tr_b16 v[82:83], v192 offset:30720\n"
    "ds_read_b64_tr_b16 v[84:85], v192 offset:33280\n"
    "v_mfma_f32_16x16x32_bf16 v[174:177], v[94:97], v[18:21], v[174:177]\n"
    "ds_read_b64_tr_b16 v[86:87], v192 offset:30752\n"
    "ds_read_b64_tr_b16 v[88:89], v192 offset:33312\n"
    "s_waitcnt lgkmcnt(12)\n"
    "v_mfma_f32_16x16x32_bf16 v[178:181], v[98:101], v[18:21], v[178:181]\n"
    "ds_read_b64_tr_b16 v[90:91], v192 offset:30784\n"
    "ds_read_b64_tr_b16 v[92:93], v192 offset:33344\n"
    "v_mfma_f32_16x16x32_bf16 v[162:165], v[102:105], v[22:25], v[162:165]\n"
    "ds_read_b64_tr_b16 v[94:95], v192 offset:30816\n"
    "ds_read_b64_tr_b16 v[96:97], v192 offset:33376\n"
    "s_waitcnt lgkmcnt(12)\n"
    "v_mfma_f32_16x16x32_bf16 v[166:169], v[106:109], v[22:25], v[166:169]\n"
    "ds_read_b64_tr_b16 v[98:99], v192 offset:30848\n"
    "ds_read_b64_tr_b16 v[100:101], v192 offset:33408\n"
    "v_mfma_f32_16x16x32_bf16 v[170:173], v[110:113], v[22:25], v[170:173]\n"
    "s_waitcnt vmcnt(3)\n"
    "ds_add_u32 v254, v235 offset:16\n"
    "ds_read_b64_tr_b16 v[102:103], v192 offset:35840\n"
    "ds_read_b64_tr_b16 v[104:105], v192 offset:38400\n"
    "s_waitcnt lgkmcnt(13)\n"
    "v_mfma_f32_16x16x32_bf16 v[174:177], v[114:117], v[22:25], v[174:177]\n"
    "ds_read_b64_tr_b16 v[106:107], v192 offset:35872\n"
    "ds_read_b64_tr_b16 v[108:109], v192 offset:38432\n"
    "v_mfma_f32_16x16x32_bf16 v[178:181], v[118:121], v[22:25], v[178:181]\n"
    "ds_read_b64_tr_b16 v[110:111], v192 offset:35904\n"
    "ds_read_b64_tr_b16 v[112:113], v192 offset:38464\n"
    "s_waitcnt lgkmcnt(13)\n"
    "v_mfma_f32_16x16x32_bf16 v[162:165], v[82:85], v[26:29], v[162:165]\n"
    "ds_read_b64_tr_b16 v[114:115], v192 offset:35936\n"
    "ds_read_b64_tr_b16 v[116:117], v192 offset:38496\n"
    "v_mfma_f32_16x16x32_bf16 v[166:169], v[86:89], v[26:29], v[166:169]\n"
    "ds_read_b64_tr_b16 v[118:119], v192 offset:35968\n"
    "ds_read_b64_tr_b16 v[120:121], v192 offset:38528\n"
    "s_waitcnt lgkmcnt(13)\n"
    "v_mfma_f32_16x16x32_bf16 v[170:173], v[90:93], v[26:29], v[170:173]\n"
    "ds_read_b64_tr_b16 v[82:83], v192 offset:40960\n"
    "ds_read_b64_tr_b16 v[84:85], v192 offset:43520\n"
    "v_mfma_f32_16x16x32_bf16 v[174:177], v[94:97], v[26:29], v[174:177]\n"
    "ds_read_b64_tr_b16 v[86:87], v192 offset:40992\n"
    "ds_read_b64_tr_b16 v[88:89], v192 offset:43552\n"
    "s_waitcnt lgkmcnt(12)\n"
    "v_mfma_f32_16x16x32_bf16 v[178:181], v[98:101], v[26:29], v[178:181]\n"
    "ds_read_b64_tr_b16 v[90:91], v192 offset:41024\n"
    "ds_read_b64_tr_b16 v[92:93], v192 offset:43584\n"
    "v_mfma_f32_16x16x32_bf16 v[162:165], v[102:105], v[30:33], v[162:165]\n"
    "ds_read_b32 v223, v245 offset:16\n"
    "ds_read_b64_tr_b16 v[94:95], v192 offset:41056\n"
    "ds_read_b64_tr_b16 v[96:97], v192 offset:43616\n"
    "s_waitcnt lgkmcnt(13)\n"
    "v_mfma_f32_16x16x32_bf16 v[166:169], v[106:109], v[30:33], v[166:169]\n"
    "ds_read_b64_tr_b16 v[98:99], v192 offset:41088\n"
    "ds_read_b64_tr_b16 v[100:101], v192 offset:43648\n"
    "v_mfma_f32_16x16x32_bf16 v[170:173], v[110:113], v[30:33], v[170:173]\n"
    "s_waitcnt lgkmcnt(4)\n"
    ".Lkc_47_%=:\n"
    "v_readfirstlane_b32 s47, v223\n"
    "s_cmp_ge_u32 s47, 5\n"
    "s_cbranch_scc1 .Lkd_48_%=\n"
    "ds_read_b32 v223, v245 offset:16\n"
    "s_sleep 1\n"
    "s_waitcnt lgkmcnt(0)\n"
    "s_sub_u32 s80, s80, 1\n"
    "s_cbranch_scc0 .Lkc_47_%=\n"
    "s_mov_b32 s80, 0\n"
    ".Lkd_48_%=:\n"
    "ds_read_b64_tr_b16 v[102:103], v193 offset:0\n"
    "ds_read_b64_tr_b16 v[104:105], v193 offset:2560\n"
    "v_mfma_f32_16x16x32_bf16 v[174:177], v[114:117], v[30:33], v[174:177]\n"
    "ds_read_b64_tr_b16 v[106:107], v193 offset:32\n"
    "ds_read_b64_tr_b16 v[108:109], v193 offset:2592\n"
    "v_mfma_f32_16x16x32_bf16 v[178:181], v[118:121], v[30:33], v[178:181]\n"
    "ds_read_b64_tr_b16 v[110:111], v193 offset:64\n"
    "ds_read_b64_tr_b16 v[112:113], v193 offset:2624\n"
    "v_mfma_f32_16x16x32_bf16 v[162:165], v[82:85], v[34:37], v[162:165]\n"
    "ds_read_b64_tr_b16 v[114:115], v193 offset:96\n"
    "ds_read_b64_tr_b16 v[116:117], v193 offset:2656\n"
    "v_mfma_f32_16x16x32_bf16 v[166:169], v[86:89], v[34:37], v[166:169]\n"
    "ds_read_b64_tr_b16 v[118:119], v193 offset:128\n"
    "ds_read_b64_tr_b16 v[120:121], v193 offset:2688\n"
    "s_waitcnt lgkmcnt(12)\n"
    "v_mfma_f32_16x16x32_bf16 v[170:173], v[90:93], v[34:37], v[170:173]\n"
    "ds_read_b64_tr_b16 v[82:83], v193 offset:5120\n"
    "ds_read_b64_tr_b16 v[84:85], v193 offset:7680\n"
    "v_mfma_f32_16x16x32_bf16 v[174:177], v[94:97], v[34:37], v[174:177]\n"
    "ds_read_b64_tr_b16 v[86:87], v193 offset:5152\n"
    "ds_read_b64_tr_b16 v[88:89], v193 offset:7712\n"
    "s_waitcnt lgkmcnt(12)\n"
    "v_mfma_f32_16x16x32_bf16 v[178:181], v[98:101], v[34:37], v[178:181]\n"
    "ds_read_b64_tr_b16 v[90:91], v193 offset:5184\n"
    "ds_read_b64_tr_b16 v[92:93], v193 offset:7744\n"
    "v_mfma_f32_16x16x32_bf16 v[162:165], v[102:105], v[38:41], v[162:165]\n"
    "ds_read_b64_tr_b16 v[94:95], v193 offset:5216\n"
    "ds_read_b64_tr_b16 v[96:97], v193 offset:7776\n"
    "s_waitcnt lgkmcnt(12)\n"
    "v_mfma_f32_16x16x32_bf16 v[166:169], v[106:109], v[38:41], v[166:169]\n"
    "ds_read_b64_tr_b16 v[98:99], v193 offset:5248\n"
    "ds_read_b64_tr_b16 v[100:101], v193 offset:7808\n"
    "v_mfma_f32_16x16x32_bf16 v[170:173], v[110:113], v[38:41], v[170:173]\n"
    "ds_add_u32 v254, v235 offset:52\n"
    "ds_read_b64_tr_b16 v[102:103], v193 offset:10240\n"
    "ds_read_b64_tr_b16 v[104:105], v193 offset:12800\n"
    "s_waitcnt lgkmcnt(13)\n"
    "v_mfma_f32_16x16x32_bf16 v[174:177], v[114:117], v[38:41], v[174:177]\n"
    "ds_read_b64_tr_b16 v[106:107], v193 offset:10272\n"
    "ds_read_b64_tr_b16 v[108:109], v193 offset:12832\n"
    "v_mfma_f32_16x16x32_bf16 v[178:181], v[118:121], v[38:41], v[178:181]\n"
    "ds_read_b64_tr_b16 v[110:111], v193 offset:10304\n"
    "ds_read_b64_tr_b16 v[112:113], v193 offset:12864\n"
    "s_waitcnt lgkmcnt(13)\n"
    "v_mfma_f32_16x16x32_bf16 v[162:165], v[82:85], v[42:45], v[162:165]\n"
    "ds_read_b64_tr_b16 v[114:115], v193 offset:10336\n"
    "ds_read_b64_tr_b16 v[116:117], v193 offset:12896\n"
    "v_mfma_f32_16x16x32_bf16 v[166:169], v[86:89], v[42:45], v[166:169]\n"
    "ds_read_b64_tr_b16 v[118:119], v193 offset:10368\n"
    "ds_read_b64_tr_b16 v[120:121], v193 offset:12928\n"
    "s_waitcnt lgkmcnt(13)\n"
    "v_mfma_f32_16x16x32_bf16 v[170:173], v[90:93], v[42:45], v[170:173]\n"
    "s_waitcnt vmcnt(0)\n"
    "ds_add_u32 v254, v235 offset:20\n"
    "buffer_load_dwordx2 v[220:221], v158, s[12:15], 0 offen sc1\n"
    "ds_read_b64_tr_b16 v[82:83], v193 offset:15360\n"
    "ds_read_b64_tr_b16 v[84:85], v193 offset:17920\n"
    "v_mfma_f32_16x16x32_bf16 v[174:177], v[94:97], v[42:45], v[174:177]\n"
    "ds_read_b64_tr_b16 v[86:87], v193 offset:15392\n"
    "ds_read_b64_tr_b16 v[88:89], v193 offset:17952\n"
    "s_waitcnt lgkmcnt(13)\n"
    "v_mfma_f32_16x16x32_bf16 v[178:181], v[98:101], v[42:45], v[178:181]\n"
    "ds_read_b64_tr_b16 v[90:91], v193 offset:15424\n"
    "ds_read_b64_tr_b16 v[92:93], v193 offset:17984\n"
    "v_mfma_f32_16x16x32_bf16 v[162:165], v[102:105], v[46:49], v[162:165]\n"
    "ds_read_b64_tr_b16 v[94:95], v193 offset:15456\n"
    "ds_read_b64_tr_b16 v[96:97], v193 offset:18016\n"
    "s_waitcnt lgkmcnt(13)\n"
    "v_mfma_f32_16x16x32_bf16 v[166:169], v[106:109], v[46:49], v[166:169]\n"
    "ds_read_b64_tr_b16 v[98:99], v193 offset:15488\n"
    "ds_read_b64_tr_b16 v[100:101], v193 offset:18048\n"
    "v_mfma_f32_16x16x32_bf16 v[170:173], v[110:113], v[46:49], v[170:173]\n"
    "ds_read_b64_tr_b16 v[102:103], v193 offset:20480\n"
    "ds_read_b64_tr_b16 v[104:105], v193 offset:23040\n"
    "s_waitcnt lgkmcnt(13)\n"
    "v_mfma_f32_16x16x32_bf16 v[174:177], v[114:117], v[46:49], v[174:177]\n"
    "ds_read_b64_tr_b16 v[106:107], v193 offset:20512\n"
    "ds_read_b64_tr_b16 v[108:109], v193 offset:23072\n"
    "v_mfma_f32_16x16x32_bf16 v[178:181], v[118:121], v[46:49], v[178:181]\n"
    "ds_read_b64_tr_b16 v[110:111], v193 offset:20544\n"
    "ds_read_b64_tr_b16 v[112:113], v193 offset:23104\n"
    "s_waitcnt lgkmcnt(12)\n"
    "v_mfma_f32_16x16x32_bf16 v[162:165], v[82:85], v[50:53], v[162:165]\n"
    "ds_read_b64_tr_b16 v[114:115], v193 offset:20576\n"
    "ds_read_b64_tr_b16 v[116:117], v193 offset:23136\n"
    "v_mfma_f32_16x16x32_bf16 v[166:169], v[86:89], v[50:53], v[166:169]\n"
    "ds_read_b64_tr_b16 v[118:119], v193 offset:20608\n"
    "ds_read_b64_tr_b16 v[120:121], v193 offset:23168\n"
    "s_waitcnt lgkmcnt(12)\n"
    "v_mfma_f32_16x16x32_bf16 v[170:173], v[90:93], v[50:53], v[170:173]\n"
    "ds_add_u32 v254, v235 offset:28\n"
    "ds_read_b64_tr_b16 v[82:83], v193 offset:25600\n"
    "ds_read_b64_tr_b16 v[84:85], v193 offset:28160\n"
    "v_mfma_f32_16x16x32_bf16 v[174:177], v[94:97], v[50:53], v[174:177]\n"
    "ds_read_b64_tr_b16 v[86:87], v193 offset:25632\n"
    "ds_read_b64_tr_b16 v[88:89], v193 offset:28192\n"
    "s_waitcnt lgkmcnt(13)\n"
    "v_mfma_f32_16x16x32_bf16 v[178:181], v[98:101], v[50:53], v[178:181]\n"
    "ds_read_b64_tr_b16 v[90:91], v193 offset:25664\n"
    "ds_read_b64_tr_b16 v[92:93], v193 offset:28224\n"
    "v_mfma_f32_16x16x32_bf16 v[162:165], v[102:105], v[54:57], v[162:165]\n"
    "ds_read_b32 v223, v245 offset:20\n"
    "ds_read_b64_tr_b16 v[94:95], v193 offset:25696\n"
    "ds_read_b64_tr_b16 v[96:97], v193 offset:28256\n"
    "s_waitcnt lgkmcnt(14)\n"
    "v_mfma_f32_16x16x32_bf16 v[166:169], v[106:109], v[54:57], v[166:169]\n"
    "ds_read_b64_tr_b16 v[98:99], v193 offset:25728\n"
    "ds_read_b64_tr_b16 v[100:101], v193 offset:28288\n"
    "v_mfma_f32_16x16x32_bf16 v[170:173], v[110:113], v[54:57], v[170:173]\n"
    "s_waitcnt vmcnt(0)\n"
    "v_and_b32 v222, 0xffff0000, v220\n"
    "v_cmp_eq_u32 s[86:87], s35, v221\n"
    "v_cmp_eq_u32 vcc, s34, v222\n"
    "s_and_b64 vcc, vcc, s[86:87]\n"
    "s_cmp_eq_u64 vcc, -1\n"
    "s_cselect_b32 s82, 1, 0\n"
    "s_cbranch_scc0 .LkL_39_%=\n"
    "s_add_u32 s38, s90, 0\n"
    "buffer_load_dwordx4 v[128:131], v247, s[12:15], s38 offen sc1\n"
    "buffer_load_dwordx2 v[132:133], v156, s[12:15], s38 offen sc1\n"
    "buffer_load_dwordx4 v[134:137], v157, s[12:15], s38 offen sc1\n"
    "buffer_load_dwordx4 v[138:141], v157, s[12:15], s38 offen offset:672 sc1\n"
    "s_add_u32 s38, s90, 2688\n"
    "buffer_load_dwordx4 v[142:145], v247, s[12:15], s38 offen sc1\n"
    "buffer_load_dwordx2 v[146:147], v156, s[12:15], s38 offen sc1\n"
    "buffer_load_dwordx4 v[148:151], v157, s[12:15], s38 offen sc1\n"
    "buffer_load_dwordx4 v[152:155], v157, s[12:15], s38 offen offset:672 sc1\n"
    ".LkL_39_%=:\n"
    "s_waitcnt lgkmcnt(4)\n"
    ".Lkc_49_%=:\n"
    "v_readfirstlane_b32 s47, v223\n"
    "s_cmp_ge_u32 s47, 4\n"
    "s_cbranch_scc1 .Lkd_50_%=\n"
    "ds_read_b32 v223, v245 offset:20\n"
    "s_sleep 1\n"
    "s_waitcnt lgkmcnt(0)\n"
    "s_sub_u32 s80, s80, 1\n"
    "s_cbranch_scc0 .Lkc_49_%=\n"
    "s_mov_b32 s80, 0\n"
    ".Lkd_50_%=:\n"
    "ds_read_b64_tr_b16 v[102:103], v193 offset:30720\n"
    "ds_read_b64_tr_b16 v[104:105], v193 offset:33280\n"
    "v_mfma_f32_16x16x32_bf16 v[174:177], v[114:117], v[54:57], v[174:177]\n"
    "ds_read_b64_tr_b16 v[106:107], v193 offset:30752\n"
    "ds_read_b64_tr_b16 v[108:109], v193 offset:33312\n"
    "v_mfma_f32_16x16x32_bf16 v[178:181], v[118:121], v[54:57], v[178:181]\n"
    "ds_read_b64_tr_b16 v[110:111], v193 offset:30784\n"
    "ds_read_b64_tr_b16 v[112:113], v193 offset:33344\n"
    "v_mfma_f32_16x16x32_bf16 v[162:165], v[82:85], v[58:61], v[162:165]\n"
    "ds_read_b64_tr_b16 v[114:115], v193 offset:30816\n"
    "ds_read_b64_tr_b16 v[116:117], v193 offset:33376\n"
    "v_mfma_f32_16x16x32_bf16 v[166:169], v[86:89], v[58:61], v[166:169]\n"
    "ds_read_b64_tr_b16 v[118:119], v193 offset:30848\n"
    "ds_read_b64_tr_b16 v[120:121], v193 offset:33408\n"
    "s_waitcnt lgkmcnt(12)\n"
    "v_mfma_f32_16x16x32_bf16 v[170:173], v[90:93], v[58:61], v[170:173]\n"
    "ds_read_b64_tr_b16 v[82:83], v193 offset:35840\n"
    "ds_read_b64_tr_b16 v[84:85], v193 offset:38400\n"
    "v_mfma_f32_16x16x32_bf16 v[174:177], v[94:97], v[58:61], v[174:177]\n"
    "ds_read_b64_tr_b16 v[86:87], v193 offset:35872\n"
    "ds_read_b64_tr_b16 v[88:89], v193 offset:38432\n"
    "s_waitcnt lgkmcnt(12)\n"
    "v_mfma_f32_16x16x32_bf16 v[178:181], v[98:101], v[58:61], v[178:181]\n"
    "ds_read_b64_tr_b16 v[90:91], v193 offset:35904\n"
    "ds_read_b64_tr_b16 v[92:93], v193 offset:38464\n"
    "v_mfma_f32_16x16x32_bf16 v[162:165], v[102:105], v[62:65], v[162:165]\n"
    "ds_read_b32 v223, v245 offset:48\n"
    "ds_read_b64_tr_b16 v[94:95], v193 offset:35936\n"
    "ds_read_b64_tr_b16 v[96:97], v193 offset:38496\n"
    "s_waitcnt lgkmcnt(13)\n"
    "v_mfma_f32_16x16x32_bf16 v[166:169], v[106:109], v[62:65], v[166:169]\n"
    "ds_read_b64_tr_b16 v[98:99], v193 offset:35968\n"
    "ds_read_b64_tr_b16 v[100:101], v193 offset:38528\n"
    "v_mfma_f32_16x16x32_bf16 v[170:173], v[110:113], v[62:65], v[170:173]\n"
    "s_waitcnt lgkmcnt(4)\n"
    ".Lkc_51_%=:\n"
    "v_readfirstlane_b32 s47, v223\n"
    "s_cmp_ge_u32 s47, 1\n"
    "s_cbranch_scc1 .Lkd_52_%=\n"
    "ds_read_b32 v223, v245 offset:48\n"
    "s_sleep 1\n"
    "s_waitcnt lgkmcnt(0)\n"
    "s_sub_u32 s80, s80, 1\n"
    "s_cbranch_scc0 .Lkc_51_%=\n"
    "s_mov_b32 s80, 0\n"
    ".Lkd_52_%=:\n"
    "ds_read_b64_tr_b16 v[102:103], v193 offset:40960\n"
    "ds_read_b64_tr_b16 v[104:105], v193 offset:43520\n"
    "v_mfma_f32_16x16x32_bf16 v[174:177], v[114:117], v[62:65], v[174:177]\n"
    "ds_read_b64_tr_b16 v[106:107], v193 offset:40992\n"
    "ds_read_b64_tr_b16 v[108:109], v193 offset:43552\n"
    "v_mfma_f32_16x16x32_bf16 v[178:181], v[118:121], v[62:65], v[178:181]\n"
    "ds_read_b64_tr_b16 v[110:111], v193 offset:41024\n"
    "ds_read_b64_tr_b16 v[112:113], v193 offset:43584\n"
    "v_mfma_f32_16x16x32_bf16 v[162:165], v[82:85], v[66:69], v[162:165]\n"
    "ds_read_b32 v223, v245 offset:48\n"
    "ds_read_b64_tr_b16 v[114:115], v193 offset:41056\n"
    "ds_read_b64_tr_b16 v[116:117], v193 offset:43616\n"
    "v_mfma_f32_16x16x32_bf16 v[166:169], v[86:89], v[66:69], v[166:169]\n"
    "ds_read_b64_tr_b16 v[118:119], v193 offset:41088\n"
    "ds_read_b64_tr_b16 v[120:121], v193 offset:43648\n"
    "s_waitcnt lgkmcnt(13)\n"
    "v_mfma_f32_16x16x32_bf16 v[170:173], v[90:93], v[66:69], v[170:173]\n"
    "s_waitcnt lgkmcnt(4)\n"
    ".Lkc_53_%=:\n"
    "v_readfirstlane_b32 s47, v223\n"
    "s_cmp_ge_u32 s47, 2\n"
    "s_cbranch_scc1 .Lkd_54_%=\n"
    "ds_read_b32 v223, v245 offset:48\n"
    "s_sleep 1\n"
    "s_waitcnt lgkmcnt(0)\n"
    "s_sub_u32 s80, s80, 1\n"
    "s_cbranch_scc0 .Lkc_53_%=\n"
    "s_mov_b32 s80, 0\n"
    ".Lkd_54_%=:\n"
    "ds_read_b64_tr_b16 v[82:83], v193 offset:46080\n"
    "ds_read_b64_tr_b16 v[84:85], v193 offset:48640\n"
    "v_mfma_f32_16x16x32_bf16 v[174:177], v[94:97], v[66:69], v[174:177]\n"
    "ds_read_b64_tr_b16 v[86:87], v193 offset:46112\n"
    "ds_read_b64_tr_b16 v[88:89], v193 offset:48672\n"
    "v_mfma_f32_16x16x32_bf16 v[178:181], v[98:101], v[66:69], v[178:181]\n"
    "ds_read_b64_tr_b16 v[90:91], v193 offset:46144\n"
    "ds_read_b64_tr_b16 v[92:93], v193 offset:48704\n"
    "v_mfma_f32_16x16x32_bf16 v[162:165], v[102:105], v[70:73], v[162:165]\n"
    "ds_read_b64_tr_b16 v[94:95], v193 offset:46176\n"
    "ds_read_b64_tr_b16 v[96:97], v193 offset:48736\n"
    "v_mfma_f32_16x16x32_bf16 v[166:169], v[106:109], v[70:73], v[166:169]\n"
    "ds_read_b64_tr_b16 v[98:99], v193 offset:46208\n"
    "ds_read_b64_tr_b16 v[100:101], v193 offset:48768\n"
    "v_mfma_f32_16x16x32_bf16 v[170:173], v[110:113], v[70:73], v[170:173]\n"
    "ds_read_b64_tr_b16 v[102:103], v193 offset:51200\n"
    "ds_read_b64_tr_b16 v[104:105], v193 offset:53760\n"
    "s_waitcnt lgkmcnt(12)\n"
    "v_mfma_f32_16x16x32_bf16 v[174:177], v[114:117], v[70:73], v[174:177]\n"
    "ds_read_b64_tr_b16 v[106:107], v193 offset:51232\n"
    "ds_read_b64_tr_b16 v[108:109], v193 offset:53792\n"
    "v_mfma_f32_16x16x32_bf16 v[178:181], v[118:121], v[70:73], v[178:181]\n"
    "ds_read_b64_tr_b16 v[110:111], v193 offset:51264\n"
    "ds_read_b64_tr_b16 v[112:113], v193 offset:53824\n"
    "s_waitcnt lgkmcnt(12)\n"
    "v_mfma_f32_16x16x32_bf16 v[162:165], v[82:85], v[74:77], v[162:165]\n"
    "ds_read_b32 v216, v245 offset:28\n"
    "ds_read_b64_tr_b16 v[114:115], v193 offset:51296\n"
    "ds_read_b64_tr_b16 v[116:117], v193 offset:53856\n"
    "v_mfma_f32_16x16x32_bf16 v[166:169], v[86:89], v[74:77], v[166:169]\n"
    "ds_read_b64_tr_b16 v[118:119], v193 offset:51328\n"
    "ds_read_b64_tr_b16 v[120:121], v193 offset:53888\n"
    "s_waitcnt lgkmcnt(13)\n"
    "v_mfma_f32_16x16x32_bf16 v[170:173], v[90:93], v[74:77], v[170:173]\n"
    "v_mfma_f32_16x16x32_bf16 v[174:177], v[94:97], v[74:77], v[174:177]\n"
    "s_waitcnt lgkmcnt(9)\n"
    "v_mfma_f32_16x16x32_bf16 v[178:181], v[98:101], v[74:77], v[178:181]\n"
    "v_mfma_f32_16x16x32_bf16 v[162:165], v[102:105], v[78:81], v[162:165]\n"
    "s_waitcnt lgkmcnt(5)\n"
    "v_mfma_f32_16x16x32_bf16 v[166:169], v[106:109], v[78:81], v[166:169]\n"
    "v_mfma_f32_16x16x32_bf16 v[170:173], v[110:113], v[78:81], v[170:173]\n"
    "s_waitcnt lgkmcnt(0)\n"
    "v_mfma_f32_16x16x32_bf16 v[174:177], v[114:117], v[78:81], v[174:177]\n"
    "v_mfma_f32_16x16x32_bf16 v[178:181], v[118:121], v[78:81], v[178:181]\n"
    ".Lkc_55_%=:\n"
    "v_readfirstlane_b32 s47, v216\n"
    "s_cmp_ge_u32 s47, 4\n"
    "s_cbranch_scc1 .Lkd_56_%=\n"
    "ds_read_b32 v216, v245 offset:28\n"
    "s_sleep 1\n"
    "s_waitcnt lgkmcnt(0)\n"
    "s_sub_u32 s80, s80, 1\n"
    "s_cbranch_scc0 .Lkc_55_%=\n"
    "s_mov_b32 s80, 0\n"
    ".Lkd_56_%=:\n"
    "v_mov_b32 v216, 0\n"
    "v_mov_b32 v217, 0\n"
    "ds_write_b64 v190, v[216:217] offset:40\n"
    "v_cvt_pk_bf16_f32 v162, v162, v163\n"
    "v_cvt_pk_bf16_f32 v163, v164, v165\n"
    "ds_write_b64 v186, v[162:163] offset:0\n"
    "v_cvt_pk_bf16_f32 v166, v166, v167\n"
    "v_cvt_pk_bf16_f32 v167, v168, v169\n"
    "ds_write_b64 v187, v[166:167] offset:32\n"
    "v_cvt_pk_bf16_f32 v170, v170, v171\n"
    "v_cvt_pk_bf16_f32 v171, v172, v173\n"
    "ds_write_b64 v188, v[170:171] offset:72\n"
    "v_cvt_pk_bf16_f32 v174, v174, v175\n"
    "v_cvt_pk_bf16_f32 v175, v176, v177\n"
    "ds_write_b64 v189, v[174:175] offset:112\n"
    "v_cvt_pk_bf16_f32 v178, v178, v179\n"
    "v_cvt_pk_bf16_f32 v179, v180, v181\n"
    "ds_write_b64 v186, v[178:179] offset:152\n"
    "s_waitcnt lgkmcnt(0)\n"
    "ds_add_u32 v254, v235 offset:32\n"
    "s_cmp_eq_u32 s82, 1\n"
    "s_cbranch_scc1 .Lkbq_8_%=\n"
    ".Lkp_57_%=:\n"
    "buffer_load_dwordx2 v[220:221], v158, s[12:15], 0 offen sc1\n"
    "s_waitcnt vmcnt(0)\n"
    "v_and_b32 v222, 0xffff0000, v220\n"
    "v_cmp_eq_u32 s[86:87], s35, v221\n"
    "v_cmp_eq_u32 vcc, s34, v222\n"
    "s_and_b64 vcc, vcc, s[86:87]\n"
    "s_cmp_eq_u64 vcc, -1\n"
    "s_cbranch_scc1 .Lkd_58_%=\n"
    "s_sleep 5\n"
    "s_sub_u32 s80, s80, 1\n"
    "s_cbranch_scc0 .Lkp_57_%=\n"
    "s_mov_b32 s80, 0\n"
    ".Lkd_58_%=:\n"
    "s_add_u32 s38, s90, 0\n"
    "buffer_load_dwordx4 v[128:131], v247, s[12:15], s38 offen sc1\n"
    "buffer_load_dwordx2 v[132:133], v156, s[12:15], s38 offen sc1\n"
    "buffer_load_dwordx4 v[134:137], v157, s[12:15], s38 offen sc1\n"
    "buffer_load_dwordx4 v[138:141], v157, s[12:15], s38 offen offset:672 sc1\n"
    "s_add_u32 s38, s90, 2688\n"
    "buffer_load_dwordx4 v[142:145], v247, s[12:15], s38 offen sc1\n"
    "buffer_load_dwordx2 v[146:147], v156, s[12:15], s38 offen sc1\n"
    "buffer_load_dwordx4 v[148:151], v157, s[12:15], s38 offen sc1\n"
    "buffer_load_dwordx4 v[152:155], v157, s[12:15], s38 offen offset:672 sc1\n"
    "s_branch .Lkbq_8_%=\n"
    ".Lkkgen_7_%=:\n"
    "s_mov_b32 s82, 0\n"
    "s_and_b32 s38, s24, 1\n"
    "s_lshl_b32 s39, s31, 1\n"
    "s_add_u32 s38, s38, s39\n"
    "s_cmp_eq_u32 s24, 6\n"
    "s_cselect_b32 s38, 4, s38\n"
    "s_add_u32 s81, s83, s38\n"
    "s_lshl_b32 s38, s81, 8\n"
    "v_lshl_add_u32 v237, v230, 2, s38\n"
    "global_load_dword v2, v237, s[50:51]\n"
    "global_load_dword v3, v237, s[52:53]\n"
    "global_load_dword v4, v237, s[54:55]\n"
    "global_load_dword v5, v237, s[56:57]\n"
    "global_load_dword v6, v237, s[58:59]\n"
    "global_load_dword v7, v237, s[60:61]\n"
    "s_lshr_b32 s38, s81, 1\n"
    "s_lshl_b32 s38, s38, 2\n"
    "s_load_dword s98, s[62:63], s38\n"
    "s_load_dword s99, s[64:65], s38\n"
    "s_sub_u32 s43, s24, 4\n"
    "s_mul_i32 s44, s43, 14416\n"
    "s_add_u32 s44, s44, 117760\n"
    "s_mov_b32 s88, s43\n"
    "s_mov_b32 s89, s44\n"
    "v_mul_u32_u24 v243, 192, v233\n"
    "v_add_u32 v243, 49152, v243\n"
    "v_lshl_add_u32 v242, v234, 4, v243\n"
    "v_lshl_add_u32 v243, v234, 3, v243\n"
    "v_lshlrev_b32 v237, 3, v233\n"
    "v_add_u32 v237, 1024, v237\n"
    "v_mov_b32 v238, 1280\n"
    "v_cmp_eq_u32 vcc, 0, v234\n"
    "v_mul_u32_u24 v157, 336, v234\n"
    "v_lshl_add_u32 v157, v233, 4, v157\n"
    "v_cndmask_b32 v156, v238, v237, vcc\n"
    "v_add_u32 v157, 1296, v157\n"
    "v_cmp_gt_u32 vcc, 21, v233\n"
    "v_mov_b32 v237, 2147483392\n"
    "v_lshlrev_b32 v158, 3, v230\n"
    "v_cndmask_b32 v157, v237, v157, vcc\n"
    "s_mul_i32 s90, s24, 21504\n"
    "s_add_u32 s90, s90, 4096\n"
    "s_lshl_b32 s39, s24, 9\n"
    "s_add_u32 s39, s39, 180224\n"
    "v_add_u32 v158, s39, v158\n"
    "s_waitcnt vmcnt(7)\n"
    "ds_add_u32 v254, v235 offset:0\n"
    "s_mul_i32 s40, s88, 9\n"
    "s_add_u32 s40, s40, 22\n"
    "s_min_u32 s40, s40, 39\n"
    "s_mul_i32 s94, s40, 7680\n"
    "s_mul_i32 s40, s40, 960\n"
    "s_add_u32 m0, s40, 512\n"
    "s_mov_b32 exec_hi, 0x0fffffff\n"
    "buffer_load_dwordx4 v248, s[8:11], s94 offen lds\n"
    "s_mov_b64 exec, -1\n"
    "s_waitcnt vmcnt(7)\n"
    "v_readfirstlane_b32 s36, v252\n"
    "v_readfirstlane_b32 s37, v253\n"
    "s_waitcnt lgkmcnt(0)\n"
    "s_add_u32 s36, s36, 1\n"
    "s_addc_u32 s37, s37, 0\n"
    "s_lshl_b64 s[34:35], s[36:37], 16\n"
    "s_setprio 3\n"
    "s_mov_b32 s43, s88\n"
    "s_mov_b32 s44, s89\n"
    "s_mov_b32 s39, s98\n"
    "s_mov_b32 s40, s99\n"
    "v_mul_u32_u24 v237, 8, v230\n"
    "v_cvt_f32_u32 v8, v237\n"
    "v_mov_b32 v10, s44\n"
    "v_mul_u32_u24 v16, 0x10001, v237\n"
    "v_mov_b32 v15, 0x42804280\n"
    "v_mov_b32 v17, 0x00800080\n"
    "v_mov_b32 v18, 42009217\n"
    "v_sub_f32 v12, 0x43ff8000, v8\n"
    "v_sub_f32 v13, 0x43ff0000, v8\n"
    "v_mov_b32 v14, 33423871\n"
    "v_cvt_pk_bf16_f32 v12, v12, v13\n"
    "v_pk_sub_i16 v14, v14, v16\n"
    "v_pk_sub_i16 v12, v12, v15\n"
    "v_pk_min_i16 v13, v14, v17\n"
    "v_pk_max_i16 v12, v12, v13\n"
    "v_pk_min_i16 v12, v12, v14\n"
    "v_pk_min_u16 v12, v12, v18\n"
    "v_mad_u32_u16 v172, v12, 2, v10\n"
    "v_mad_u32_u16 v173, v12, 2, v10 op_sel:[1,0,0,0]\n"
    "v_sub_f32 v12, 0x43fe8000, v8\n"
    "v_sub_f32 v13, 0x43fe0000, v8\n"
    "v_mov_b32 v14, 33292797\n"
    "v_cvt_pk_bf16_f32 v12, v12, v13\n"
    "v_pk_sub_i16 v14, v14, v16\n"
    "v_pk_sub_i16 v12, v12, v15\n"
    "v_pk_min_i16 v13, v14, v17\n"
    "v_pk_max_i16 v12, v12, v13\n"
    "v_pk_min_i16 v12, v12, v14\n"
    "v_pk_min_u16 v12, v12, v18\n"
    "v_mad_u32_u16 v174, v12, 2, v10\n"
    "v_mad_u32_u16 v175, v12, 2, v10 op_sel:[1,0,0,0]\n"
    "s_add_u32 m0, m0, 960\n"
    "s_add_u32 s94, s94, 7680\n"
    "s_mov_b32 exec_hi, 0x0fffffff\n"
    "buffer_load_dwordx4 v248, s[8:11], s94 offen lds\n"
    "s_mov_b64 exec, -1\n"
    "v_sub_f32 v12, 0x43fd8000, v8\n"
    "v_sub_f32 v13, 0x43fd0000, v8\n"
    "v_mov_b32 v14, 33161723\n"
    "v_cvt_pk_bf16_f32 v12, v12, v13\n"
    "v_pk_sub_i16 v14, v14, v16\n"
    "v_pk_sub_i16 v12, v12, v15\n"
    "v_pk_min_i16 v13, v14, v17\n"
    "v_pk_max_i16 v12, v12, v13\n"
    "v_pk_min_i16 v12, v12, v14\n"
    "v_pk_min_u16 v12, v12, v18\n"
    "v_mad_u32_u16 v176, v12, 2, v10\n"
    "v_mad_u32_u16 v177, v12, 2, v10 op_sel:[1,0,0,0]\n"
    "v_sub_f32 v12, 0x43fc8000, v8\n"
    "v_sub_f32 v13, 0x43fc0000, v8\n"
    "v_mov_b32 v14, 33030649\n"
    "v_cvt_pk_bf16_f32 v12, v12, v13\n"
    "v_pk_sub_i16 v14, v14, v16\n"
    "v_pk_sub_i16 v12, v12, v15\n"
    "v_pk_min_i16 v13, v14, v17\n"
    "v_pk_max_i16 v12, v12, v13\n"
    "v_pk_min_i16 v12, v12, v14\n"
    "v_pk_min_u16 v12, v12, v18\n"
    "v_mad_u32_u16 v178, v12, 2, v10\n"
    "v_mad_u32_u16 v179, v12, 2, v10 op_sel:[1,0,0,0]\n"
    "s_add_u32 m0, m0, 960\n"
    "s_add_u32 s94, s94, 7680\n"
    "s_mov_b32 exec_hi, 0x0fffffff\n"
    "buffer_load_dwordx4 v248, s[8:11], s94 offen lds\n"
    "s_mov_b64 exec, -1\n"
    "v_sub_f32 v12, 0x43fb8000, v8\n"
    "v_sub_f32 v13, 0x43fb0000, v8\n"
    "v_mov_b32 v14, 32899575\n"
    "v_cvt_pk_bf16_f32 v12, v12, v13\n"
    "v_pk_sub_i16 v14, v14, v16\n"
    "v_pk_sub_i16 v12, v12, v15\n"
    "v_pk_min_i16 v13, v14, v17\n"
    "v_pk_max_i16 v12, v12, v13\n"
    "v_pk_min_i16 v12, v12, v14\n"
    "v_pk_min_u16 v12, v12, v18\n"
    "v_mad_u32_u16 v180, v12, 2, v10\n"
    "v_mad_u32_u16 v181, v12, 2, v10 op_sel:[1,0,0,0]\n"
    "v_sub_f32 v12, 0x43fa8000, v8\n"
    "v_sub_f32 v13, 0x43fa0000, v8\n"
    "v_mov_b32 v14, 32768501\n"
    "v_cvt_pk_bf16_f32 v12, v12, v13\n"
    "v_pk_sub_i16 v14, v14, v16\n"
    "v_pk_sub_i16 v12, v12, v15\n"
    "v_pk_min_i16 v13, v14, v17\n"
    "v_pk_max_i16 v12, v12, v13\n"
    "v_pk_min_i16 v12, v12, v14\n"
    "v_pk_min_u16 v12, v12, v18\n"
    "v_mad_u32_u16 v182, v12, 2, v10\n"
    "s_add_u32 m0, m0, 960\n"
    "s_add_u32 s94, s94, 7680\n"
    "s_mov_b32 exec_hi, 0x0fffffff\n"
    "buffer_load_dwordx4 v248, s[8:11], s94 offen lds\n"
    "s_mov_b64 exec, -1\n"
    "v_lshrrev_b32 v237, 2, v231\n"
    "v_lshl_add_u32 v237, v232, 3, v237\n"
    "v_lshlrev_b32 v226, 4, v232\n"
    "v_lshl_add_u32 v226, v231, 12, v226\n"
    "s_mul_i32 s89, s26, 10\n"
    "s_lshl_b32 s41, s43, 2\n"
    "s_add_u32 s89, s89, s41\n"
    "v_bfe_u32 v238, v231, 1, 1\n"
    "v_add_u32 v238, s89, v238\n"
    "v_and_b32 v239, 1, v231\n"
    "v_lshlrev_b32 v239, 3, v239\n"
    "s_cmp_ge_u32 s89, 64\n"
    "s_cbranch_scc1 .LkL_60_%=\n"
    "v_mul_u32_u24 v224, 1040, v237\n"
    "v_lshl_add_u32 v224, v238, 4, v224\n"
    "v_add_u32 v224, 79872, v224\n"
    "v_add_u32 v224, v224, v239\n"
    "v_add_u32 v225, 32, v224\n"
    "s_branch .LkL_59_%=\n"
    ".LkL_60_%=:\n"
    "v_and_b32 v1, 3, v237\n"
    "v_lshrrev_b32 v237, 2, v237\n"
    "v_mul_u32_u24 v237, 1088, v237\n"
    "v_lshl_add_u32 v237, v1, 8, v237\n"
    "v_add_u32 v237, 145408, v237\n"
    "v_add_u32 v237, v237, v239\n"
    "v_add_u32 v239, 2, v238\n"
    "v_xor_b32 v239, v239, v1\n"
    "v_lshl_add_u32 v225, v239, 4, v237\n"
    "v_xor_b32 v239, v238, v1\n"
    "v_lshl_add_u32 v224, v239, 4, v237\n"
    ".LkL_59_%=:\n"
    "s_mul_i32 s88, s26, 327680\n"
    "s_lshl_b32 s41, s43, 17\n"
    "s_add_u32 s88, s88, s41\n"
    "s_lshl_b32 s41, s29, 1\n"
    "s_add_u32 s88, s88, s41\n"
    "s_add_u32 s88, s88, 327680\n"
    "s_add_u32 m0, m0, 960\n"
    "s_add_u32 s94, s94, 7680\n"
    "s_mov_b32 exec_hi, 0x0fffffff\n"
    "buffer_load_dwordx4 v248, s[8:11], s94 offen lds\n"
    "s_mov_b64 exec, -1\n"
    "s_waitcnt vmcnt(5)\n"
    "s_waitcnt lgkmcnt(0)\n"
    "s_bitcmp1_b32 s81, 0\n"
    "s_cselect_b32 s38, 0, 16\n"
    "s_lshl_b32 s39, s39, s38\n"
    "s_lshl_b32 s40, s40, s38\n"
    "s_and_b32 s39, s39, 0xffff0000\n"
    "s_and_b32 s40, s40, 0xffff0000\n"
    "v_mov_b32 v8, 0x3fb8aa3b\n"
    "v_mul_f32 v8, s39, v8\n"
    "v_exp_f32 v8, v8\n"
    "s_nop 0\n"
    "v_cvt_pk_bf16_f32 v8, v8, v8\n"
    "v_and_b32 v8, 0xffff0000, v8\n"
    "v_lshlrev_b32 v10, 16, v2\n"
    "v_and_b32 v16, 0xffff0000, v2\n"
    "v_lshlrev_b32 v11, 16, v3\n"
    "v_and_b32 v17, 0xffff0000, v3\n"
    "v_lshlrev_b32 v12, 16, v4\n"
    "v_and_b32 v18, 0xffff0000, v4\n"
    "v_lshlrev_b32 v13, 16, v5\n"
    "v_and_b32 v19, 0xffff0000, v5\n"
    "v_lshlrev_b32 v14, 16, v6\n"
    "v_and_b32 v20, 0xffff0000, v6\n"
    "v_lshlrev_b32 v15, 16, v7\n"
    "v_and_b32 v21, 0xffff0000, v7\n"
    "v_mul_f32 v10, v8, v10\n"
    "v_mul_f32 v11, v8, v11\n"
    "v_mul_f32 v12, v8, v12\n"
    "v_mul_f32 v13, v8, v13\n"
    "v_mul_f32 v10, 0x3fb8aa3b, v10\n"
    "v_mul_f32 v11, 0.15915494, v11\n"
    "v_mul_f32 v16, v8, v16\n"
    "v_mul_f32 v17, v8, v17\n"
    "v_mul_f32 v18, v8, v18\n"
    "v_mul_f32 v19, v8, v19\n"
    "v_mul_f32 v16, 0x3fb8aa3b, v16\n"
    "v_mul_f32 v17, 0.15915494, v17\n"
    "s_add_u32 m0, m0, 960\n"
    "s_add_u32 s94, s94, 7680\n"
    "s_mov_b32 exec_hi, 0x0fffffff\n"
    "buffer_load_dwordx4 v248, s[8:11], s94 offen lds\n"
    "s_mov_b64 exec, -1\n"
    "v_pk_mul_f32 v[52:53], v[14:15], v[12:13] op_sel:[1,1] op_sel_hi:[1,0] neg_lo:[1,0]\n"
    "v_pk_mul_f32 v[54:55], v[20:21], v[18:19] op_sel:[1,1] op_sel_hi:[1,0] neg_lo:[1,0]\n"
    "v_pk_fma_f32 v[40:41], v[14:15], v[12:13], v[52:53] op_sel_hi:[0,1,1]\n"
    "v_pk_fma_f32 v[42:43], v[20:21], v[18:19], v[54:55] op_sel_hi:[0,1,1]\n"
    "v_mul_f32 v2, 0x41000000, v10\n"
    "v_mul_f32 v28, 0x41000000, v11\n"
    "v_mul_f32 v3, 0x3f800000, v10\n"
    "v_mul_f32 v24, 0x3f800000, v11\n"
    "v_mul_f32 v4, 0x43800000, v10\n"
    "v_mul_f32 v32, 0x43800000, v11\n"
    "v_mul_f32 v5, 0x41000000, v16\n"
    "v_mul_f32 v30, 0x41000000, v17\n"
    "v_mul_f32 v6, 0x3f800000, v16\n"
    "v_mul_f32 v26, 0x3f800000, v17\n"
    "v_mul_f32 v7, 0x43800000, v16\n"
    "v_mul_f32 v34, 0x43800000, v17\n"
    "v_fract_f32 v28, v28\n"
    "v_exp_f32 v2, v2\n"
    "v_fract_f32 v24, v24\n"
    "v_exp_f32 v3, v3\n"
    "v_fract_f32 v32, v32\n"
    "v_exp_f32 v4, v4\n"
    "v_fract_f32 v30, v30\n"
    "v_exp_f32 v5, v5\n"
    "v_fract_f32 v26, v26\n"
    "v_exp_f32 v6, v6\n"
    "v_fract_f32 v34, v34\n"
    "v_exp_f32 v7, v7\n"
    "v_sin_f32 v29, v28\n"
    "v_cos_f32 v28, v28\n"
    "v_sin_f32 v25, v24\n"
    "v_cos_f32 v24, v24\n"
    "v_sin_f32 v33, v32\n"
    "v_cos_f32 v32, v32\n"
    "v_sin_f32 v31, v30\n"
    "v_cos_f32 v30, v30\n"
    "v_sin_f32 v27, v26\n"
    "v_cos_f32 v26, v26\n"
    "v_sin_f32 v35, v34\n"
    "v_cos_f32 v34, v34\n"
    "s_nop 0\n"
    "v_mul_f32 v28, v2, v28\n"
    "v_mul_f32 v29, v2, v29\n"
    "v_mul_f32 v24, v3, v24\n"
    "v_mul_f32 v25, v3, v25\n"
    "v_mul_f32 v32, v4, v32\n"
    "v_mul_f32 v33, v4, v33\n"
    "v_mul_f32 v30, v5, v30\n"
    "v_mul_f32 v31, v5, v31\n"
    "v_mul_f32 v26, v6, v26\n"
    "v_mul_f32 v27, v6, v27\n"
    "v_mul_f32 v34, v7, v34\n"
    "v_mul_f32 v35, v7, v35\n"
    "s_add_u32 m0, m0, 960\n"
    "s_add_u32 s94, s94, 7680\n"
    "s_mov_b32 exec_hi, 0x0fffffff\n"
    "buffer_load_dwordx4 v248, s[8:11], s94 offen lds\n"
    "s_mov_b64 exec, -1\n"
    "v_pk_mul_f32 v[52:53], v[28:29], v[28:29] op_sel:[1,1] op_sel_hi:[1,0] neg_lo:[1,0]\n"
    "v_pk_mul_f32 v[54:55], v[30:31], v[30:31] op_sel:[1,1] op_sel_hi:[1,0] neg_lo:[1,0]\n"
    "v_pk_fma_f32 v[36:37], v[28:29], v[28:29], v[52:53] op_sel_hi:[0,1,1]\n"
    "v_pk_fma_f32 v[38:39], v[30:31], v[30:31], v[54:55] op_sel_hi:[0,1,1]\n"
    "v_lshl_add_u32 v60, v230, 2, s44\n"
    "v_mul_u32_u24 v61, 272, v233\n"
    "v_lshl_add_u32 v61, v234, 4, v61\n"
    "v_add_u32 v61, s44, v61\n"
    "v_mov_b32 v64, 0x3f80\n"
    "v_mov_b32 v96, 0x3f80\n"
    "v_cvt_pk_bf16_f32 v65, v28, v29\n"
    "v_cvt_pk_bf16_f32 v66, v36, v37\n"
    "v_cvt_pk_bf16_f32 v128, v40, -v41\n"
    "v_mov_b32 v44, v40\n"
    "v_mov_b32 v45, v41\n"
    "v_cvt_pk_bf16_f32 v97, v30, v31\n"
    "v_cvt_pk_bf16_f32 v98, v38, v39\n"
    "v_cvt_pk_bf16_f32 v184, v42, -v43\n"
    "v_mov_b32 v46, v42\n"
    "v_mov_b32 v47, v43\n"
    "v_pk_mul_f32 v[52:53], v[28:29], v[36:37] op_sel:[1,1] op_sel_hi:[1,0] neg_lo:[1,0]\n"
    "v_pk_mul_f32 v[54:55], v[30:31], v[38:39] op_sel:[1,1] op_sel_hi:[1,0] neg_lo:[1,0]\n"
    "v_pk_mul_f32 v[56:57], v[36:37], v[36:37] op_sel:[1,1] op_sel_hi:[1,0] neg_lo:[1,0]\n"
    "v_pk_mul_f32 v[58:59], v[38:39], v[38:39] op_sel:[1,1] op_sel_hi:[1,0] neg_lo:[1,0]\n"
    "v_pk_fma_f32 v[28:29], v[28:29], v[36:37], v[52:53] op_sel_hi:[0,1,1]\n"
    "v_pk_fma_f32 v[30:31], v[30:31], v[38:39], v[54:55] op_sel_hi:[0,1,1]\n"
    "v_pk_fma_f32 v[48:49], v[36:37], v[36:37], v[56:57] op_sel_hi:[0,1,1]\n"
    "v_pk_fma_f32 v[50:51], v[38:39], v[38:39], v[58:59] op_sel_hi:[0,1,1]\n"
    "s_add_u32 m0, m0, 960\n"
    "s_add_u32 s94, s94, 7680\n"
    "s_mov_b32 exec_hi, 0x0fffffff\n"
    "buffer_load_dwordx4 v248, s[8:11], s94 offen lds\n"
    "s_mov_b64 exec, -1\n"
    "v_pk_mul_f32 v[52:53], v[28:29], v[36:37] op_sel:[1,1] op_sel_hi:[1,0] neg_lo:[1,0]\n"
    "v_cvt_pk_bf16_f32 v67, v28, v29\n"
    "v_pk_mul_f32 v[54:55], v[30:31], v[38:39] op_sel:[1,1] op_sel_hi:[1,0] neg_lo:[1,0]\n"
    "v_cvt_pk_bf16_f32 v99, v30, v31\n"
    "v_pk_mul_f32 v[56:57], v[48:49], v[36:37] op_sel:[1,1] op_sel_hi:[1,0] neg_lo:[1,0]\n"
    "v_cvt_pk_bf16_f32 v68, v48, v49\n"
    "v_pk_mul_f32 v[58:59], v[50:51], v[38:39] op_sel:[1,1] op_sel_hi:[1,0] neg_lo:[1,0]\n"
    "v_cvt_pk_bf16_f32 v100, v50, v51\n"
    "v_pk_fma_f32 v[28:29], v[28:29], v[36:37], v[52:53] op_sel_hi:[0,1,1]\n"
    "v_pk_fma_f32 v[30:31], v[30:31], v[38:39], v[54:55] op_sel_hi:[0,1,1]\n"
    "v_pk_fma_f32 v[48:49], v[48:49], v[36:37], v[56:57] op_sel_hi:[0,1,1]\n"
    "v_pk_fma_f32 v[50:51], v[50:51], v[38:39], v[58:59] op_sel_hi:[0,1,1]\n"
    "s_add_u32 m0, m0, 960\n"
    "s_add_u32 s94, s94, 7680\n"
    "s_mov_b32 exec_hi, 0x0fffffff\n"
    "buffer_load_dwordx4 v248, s[8:11], s94 offen lds\n"
    "s_mov_b64 exec, -1\n"
    "v_pk_mul_f32 v[52:53], v[28:29], v[36:37] op_sel:[1,1] op_sel_hi:[1,0] neg_lo:[1,0]\n"
    "v_cvt_pk_bf16_f32 v69, v28, v29\n"
    "v_pk_mul_f32 v[54:55], v[30:31], v[38:39] op_sel:[1,1] op_sel_hi:[1,0] neg_lo:[1,0]\n"
    "v_cvt_pk_bf16_f32 v101, v30, v31\n"
    "v_pk_mul_f32 v[56:57], v[48:49], v[36:37] op_sel:[1,1] op_sel_hi:[1,0] neg_lo:[1,0]\n"
    "v_cvt_pk_bf16_f32 v70, v48, v49\n"
    "v_pk_mul_f32 v[58:59], v[50:51], v[38:39] op_sel:[1,1] op_sel_hi:[1,0] neg_lo:[1,0]\n"
    "v_cvt_pk_bf16_f32 v102, v50, v51\n"
    "v_pk_fma_f32 v[28:29], v[28:29], v[36:37], v[52:53] op_sel_hi:[0,1,1]\n"
    "v_pk_fma_f32 v[30:31], v[30:31], v[38:39], v[54:55] op_sel_hi:[0,1,1]\n"
    "v_pk_fma_f32 v[48:49], v[48:49], v[36:37], v[56:57] op_sel_hi:[0,1,1]\n"
    "v_pk_fma_f32 v[50:51], v[50:51], v[38:39], v[58:59] op_sel_hi:[0,1,1]\n"
    "v_pk_mul_f32 v[52:53], v[28:29], v[36:37] op_sel:[1,1] op_sel_hi:[1,0] neg_lo:[1,0]\n"
    "v_cvt_pk_bf16_f32 v71, v28, v29\n"
    "v_pk_mul_f32 v[54:55], v[30:31], v[38:39] op_sel:[1,1] op_sel_hi:[1,0] neg_lo:[1,0]\n"
    "v_cvt_pk_bf16_f32 v103, v30, v31\n"
    "v_pk_mul_f32 v[56:57], v[48:49], v[36:37] op_sel:[1,1] op_sel_hi:[1,0] neg_lo:[1,0]\n"
    "v_cvt_pk_bf16_f32 v72, v48, v49\n"
    "v_pk_mul_f32 v[58:59], v[50:51], v[38:39] op_sel:[1,1] op_sel_hi:[1,0] neg_lo:[1,0]\n"
    "v_cvt_pk_bf16_f32 v104, v50, v51\n"
    "v_pk_fma_f32 v[28:29], v[28:29], v[36:37], v[52:53] op_sel_hi:[0,1,1]\n"
    "v_pk_fma_f32 v[30:31], v[30:31], v[38:39], v[54:55] op_sel_hi:[0,1,1]\n"
    "v_pk_fma_f32 v[48:49], v[48:49], v[36:37], v[56:57] op_sel_hi:[0,1,1]\n"
    "v_pk_fma_f32 v[50:51], v[50:51], v[38:39], v[58:59] op_sel_hi:[0,1,1]\n"
    "v_pk_mul_f32 v[52:53], v[28:29], v[36:37] op_sel:[1,1] op_sel_hi:[1,0] neg_lo:[1,0]\n"
    "v_cvt_pk_bf16_f32 v73, v28, v29\n"
    "v_pk_mul_f32 v[54:55], v[30:31], v[38:39] op_sel:[1,1] op_sel_hi:[1,0] neg_lo:[1,0]\n"
    "v_cvt_pk_bf16_f32 v105, v30, v31\n"
    "v_pk_mul_f32 v[56:57], v[48:49], v[36:37] op_sel:[1,1] op_sel_hi:[1,0] neg_lo:[1,0]\n"
    "v_cvt_pk_bf16_f32 v74, v48, v49\n"
    "v_pk_mul_f32 v[58:59], v[50:51], v[38:39] op_sel:[1,1] op_sel_hi:[1,0] neg_lo:[1,0]\n"
    "v_cvt_pk_bf16_f32 v106, v50, v51\n"
    "v_pk_fma_f32 v[28:29], v[28:29], v[36:37], v[52:53] op_sel_hi:[0,1,1]\n"
    "v_pk_fma_f32 v[30:31], v[30:31], v[38:39], v[54:55] op_sel_hi:[0,1,1]\n"
    "v_pk_fma_f32 v[48:49], v[48:49], v[36:37], v[56:57] op_sel_hi:[0,1,1]\n"
    "v_pk_fma_f32 v[50:51], v[50:51], v[38:39], v[58:59] op_sel_hi:[0,1,1]\n"
    "v_pk_mul_f32 v[52:53], v[28:29], v[36:37] op_sel:[1,1] op_sel_hi:[1,0] neg_lo:[1,0]\n"
    "v_cvt_pk_bf16_f32 v75, v28, v29\n"
    "v_pk_mul_f32 v[54:55], v[30:31], v[38:39] op_sel:[1,1] op_sel_hi:[1,0] neg_lo:[1,0]\n"
    "v_cvt_pk_bf16_f32 v107, v30, v31\n"
    "v_pk_mul_f32 v[56:57], v[48:49], v[36:37] op_sel:[1,1] op_sel_hi:[1,0] neg_lo:[1,0]\n"
    "v_cvt_pk_bf16_f32 v76, v48, v49\n"
    "v_pk_mul_f32 v[58:59], v[50:51], v[38:39] op_sel:[1,1] op_sel_hi:[1,0] neg_lo:[1,0]\n"
    "v_cvt_pk_bf16_f32 v108, v50, v51\n"
    "v_pk_fma_f32 v[28:29], v[28:29], v[36:37], v[52:53] op_sel_hi:[0,1,1]\n"
    "v_pk_fma_f32 v[30:31], v[30:31], v[38:39], v[54:55] op_sel_hi:[0,1,1]\n"
    "v_pk_fma_f32 v[48:49], v[48:49], v[36:37], v[56:57] op_sel_hi:[0,1,1]\n"
    "v_pk_fma_f32 v[50:51], v[50:51], v[38:39], v[58:59] op_sel_hi:[0,1,1]\n"
    "v_pk_mul_f32 v[52:53], v[28:29], v[36:37] op_sel:[1,1] op_sel_hi:[1,0] neg_lo:[1,0]\n"
    "v_cvt_pk_bf16_f32 v77, v28, v29\n"
    "v_pk_mul_f32 v[54:55], v[30:31], v[38:39] op_sel:[1,1] op_sel_hi:[1,0] neg_lo:[1,0]\n"
    "v_cvt_pk_bf16_f32 v109, v30, v31\n"
    "v_pk_mul_f32 v[56:57], v[48:49], v[36:37] op_sel:[1,1] op_sel_hi:[1,0] neg_lo:[1,0]\n"
    "v_cvt_pk_bf16_f32 v78, v48, v49\n"
    "v_pk_mul_f32 v[58:59], v[50:51], v[38:39] op_sel:[1,1] op_sel_hi:[1,0] neg_lo:[1,0]\n"
    "v_cvt_pk_bf16_f32 v110, v50, v51\n"
    "v_pk_fma_f32 v[28:29], v[28:29], v[36:37], v[52:53] op_sel_hi:[0,1,1]\n"
    "v_pk_fma_f32 v[30:31], v[30:31], v[38:39], v[54:55] op_sel_hi:[0,1,1]\n"
    "v_pk_fma_f32 v[48:49], v[48:49], v[36:37], v[56:57] op_sel_hi:[0,1,1]\n"
    "v_pk_fma_f32 v[50:51], v[50:51], v[38:39], v[58:59] op_sel_hi:[0,1,1]\n"
    "v_pk_mul_f32 v[52:53], v[28:29], v[36:37] op_sel:[1,1] op_sel_hi:[1,0] neg_lo:[1,0]\n"
    "v_cvt_pk_bf16_f32 v79, v28, v29\n"
    "v_pk_mul_f32 v[54:55], v[30:31], v[38:39] op_sel:[1,1] op_sel_hi:[1,0] neg_lo:[1,0]\n"
    "v_cvt_pk_bf16_f32 v111, v30, v31\n"
    "v_pk_mul_f32 v[56:57], v[48:49], v[36:37] op_sel:[1,1] op_sel_hi:[1,0] neg_lo:[1,0]\n"
    "v_cvt_pk_bf16_f32 v80, v48, v49\n"
    "v_pk_mul_f32 v[58:59], v[50:51], v[38:39] op_sel:[1,1] op_sel_hi:[1,0] neg_lo:[1,0]\n"
    "v_cvt_pk_bf16_f32 v112, v50, v51\n"
    "v_pk_fma_f32 v[28:29], v[28:29], v[36:37], v[52:53] op_sel_hi:[0,1,1]\n"
    "v_pk_fma_f32 v[30:31], v[30:31], v[38:39], v[54:55] op_sel_hi:[0,1,1]\n"
    "v_pk_fma_f32 v[48:49], v[48:49], v[36:37], v[56:57] op_sel_hi:[0,1,1]\n"
    "v_pk_fma_f32 v[50:51], v[50:51], v[38:39], v[58:59] op_sel_hi:[0,1,1]\n"
    "v_pk_mul_f32 v[52:53], v[28:29], v[36:37] op_sel:[1,1] op_sel_hi:[1,0] neg_lo:[1,0]\n"
    "v_cvt_pk_bf16_f32 v81, v28, v29\n"
    "v_pk_mul_f32 v[54:55], v[30:31], v[38:39] op_sel:[1,1] op_sel_hi:[1,0] neg_lo:[1,0]\n"
    "v_cvt_pk_bf16_f32 v113, v30, v31\n"
    "v_pk_mul_f32 v[56:57], v[48:49], v[36:37] op_sel:[1,1] op_sel_hi:[1,0] neg_lo:[1,0]\n"
    "v_cvt_pk_bf16_f32 v82, v48, v49\n"
    "v_pk_mul_f32 v[58:59], v[50:51], v[38:39] op_sel:[1,1] op_sel_hi:[1,0] neg_lo:[1,0]\n"
    "v_cvt_pk_bf16_f32 v114, v50, v51\n"
    "v_pk_fma_f32 v[28:29], v[28:29], v[36:37], v[52:53] op_sel_hi:[0,1,1]\n"
    "v_pk_fma_f32 v[30:31], v[30:31], v[38:39], v[54:55] op_sel_hi:[0,1,1]\n"
    "v_pk_fma_f32 v[48:49], v[48:49], v[36:37], v[56:57] op_sel_hi:[0,1,1]\n"
    "v_pk_fma_f32 v[50:51], v[50:51], v[38:39], v[58:59] op_sel_hi:[0,1,1]\n"
    "v_pk_mul_f32 v[52:53], v[28:29], v[36:37] op_sel:[1,1] op_sel_hi:[1,0] neg_lo:[1,0]\n"
    "v_cvt_pk_bf16_f32 v83, v28, v29\n"
    "v_pk_mul_f32 v[54:55], v[30:31], v[38:39] op_sel:[1,1] op_sel_hi:[1,0] neg_lo:[1,0]\n"
    "v_cvt_pk_bf16_f32 v115, v30, v31\n"
    "v_pk_mul_f32 v[56:57], v[48:49], v[36:37] op_sel:[1,1] op_sel_hi:[1,0] neg_lo:[1,0]\n"
    "v_cvt_pk_bf16_f32 v84, v48, v49\n"
    "v_pk_mul_f32 v[58:59], v[50:51], v[38:39] op_sel:[1,1] op_sel_hi:[1,0] neg_lo:[1,0]\n"
    "v_cvt_pk_bf16_f32 v116, v50, v51\n"
    "v_pk_fma_f32 v[28:29], v[28:29], v[36:37], v[52:53] op_sel_hi:[0,1,1]\n"
    "v_pk_fma_f32 v[30:31], v[30:31], v[38:39], v[54:55] op_sel_hi:[0,1,1]\n"
    "v_pk_fma_f32 v[48:49], v[48:49], v[36:37], v[56:57] op_sel_hi:[0,1,1]\n"
    "v_pk_fma_f32 v[50:51], v[50:51], v[38:39], v[58:59] op_sel_hi:[0,1,1]\n"
    "v_pk_mul_f32 v[52:53], v[28:29], v[36:37] op_sel:[1,1] op_sel_hi:[1,0] neg_lo:[1,0]\n"
    "v_cvt_pk_bf16_f32 v85, v28, v29\n"
    "v_pk_mul_f32 v[54:55], v[30:31], v[38:39] op_sel:[1,1] op_sel_hi:[1,0] neg_lo:[1,0]\n"
    "v_cvt_pk_bf16_f32 v117, v30, v31\n"
    "v_pk_mul_f32 v[56:57], v[48:49], v[36:37] op_sel:[1,1] op_sel_hi:[1,0] neg_lo:[1,0]\n"
    "v_cvt_pk_bf16_f32 v86, v48, v49\n"
    "v_pk_mul_f32 v[58:59], v[50:51], v[38:39] op_sel:[1,1] op_sel_hi:[1,0] neg_lo:[1,0]\n"
    "v_cvt_pk_bf16_f32 v118, v50, v51\n"
    "v_pk_fma_f32 v[28:29], v[28:29], v[36:37], v[52:53] op_sel_hi:[0,1,1]\n"
    "v_pk_fma_f32 v[30:31], v[30:31], v[38:39], v[54:55] op_sel_hi:[0,1,1]\n"
    "v_pk_fma_f32 v[48:49], v[48:49], v[36:37], v[56:57] op_sel_hi:[0,1,1]\n"
    "v_pk_fma_f32 v[50:51], v[50:51], v[38:39], v[58:59] op_sel_hi:[0,1,1]\n"
    "ds_read_b32 v227, v245 offset:36\n"
    "ds_read_b32 v228, v245 offset:0\n"
    "v_pk_mul_f32 v[52:53], v[28:29], v[36:37] op_sel:[1,1] op_sel_hi:[1,0] neg_lo:[1,0]\n"
    "v_cvt_pk_bf16_f32 v87, v28, v29\n"
    "v_pk_mul_f32 v[54:55], v[30:31], v[38:39] op_sel:[1,1] op_sel_hi:[1,0] neg_lo:[1,0]\n"
    "v_cvt_pk_bf16_f32 v119, v30, v31\n"
    "v_pk_mul_f32 v[56:57], v[48:49], v[36:37] op_sel:[1,1] op_sel_hi:[1,0] neg_lo:[1,0]\n"
    "v_cvt_pk_bf16_f32 v88, v48, v49\n"
    "v_pk_mul_f32 v[58:59], v[50:51], v[38:39] op_sel:[1,1] op_sel_hi:[1,0] neg_lo:[1,0]\n"
    "v_cvt_pk_bf16_f32 v120, v50, v51\n"
    "v_pk_fma_f32 v[28:29], v[28:29], v[36:37], v[52:53] op_sel_hi:[0,1,1]\n"
    "v_pk_fma_f32 v[30:31], v[30:31], v[38:39], v[54:55] op_sel_hi:[0,1,1]\n"
    "v_pk_fma_f32 v[48:49], v[48:49], v[36:37], v[56:57] op_sel_hi:[0,1,1]\n"
    "v_pk_fma_f32 v[50:51], v[50:51], v[38:39], v[58:59] op_sel_hi:[0,1,1]\n"
    "s_waitcnt lgkmcnt(1)\n"
    ".Lkc_61_%=:\n"
    "v_readfirstlane_b32 s47, v227\n"
    "s_cmp_ge_u32 s47, 3\n"
    "s_cbranch_scc1 .Lkd_62_%=\n"
    "ds_read_b32 v227, v245 offset:36\n"
    "s_sleep 1\n"
    "s_waitcnt lgkmcnt(0)\n"
    "s_sub_u32 s80, s80, 1\n"
    "s_cbranch_scc0 .Lkc_61_%=\n"
    "s_mov_b32 s80, 0\n"
    ".Lkd_62_%=:\n"
    "s_waitcnt lgkmcnt(0)\n"
    ".Lkc_63_%=:\n"
    "v_readfirstlane_b32 s47, v228\n"
    "s_cmp_ge_u32 s47, 8\n"
    "s_cbranch_scc1 .Lkd_64_%=\n"
    "ds_read_b32 v228, v245 offset:0\n"
    "s_sleep 1\n"
    "s_waitcnt lgkmcnt(0)\n"
    "s_sub_u32 s80, s80, 1\n"
    "s_cbranch_scc0 .Lkc_63_%=\n"
    "s_mov_b32 s80, 0\n"
    ".Lkd_64_%=:\n"
    "s_cmp_ge_u32 s89, 64\n"
    "s_cbranch_scc1 .LkL_65_%=\n"
    "ds_read_b64_tr_b16 v[206:207], v224 offset:0\n"
    "ds_read_b64_tr_b16 v[208:209], v224 offset:4160\n"
    "ds_read_b64_tr_b16 v[210:211], v224 offset:33280\n"
    "ds_read_b64_tr_b16 v[212:213], v224 offset:37440\n"
    "ds_read_b64_tr_b16 v[214:215], v225 offset:0\n"
    "ds_read_b64_tr_b16 v[216:217], v225 offset:4160\n"
    "ds_read_b64_tr_b16 v[218:219], v225 offset:33280\n"
    "ds_read_b64_tr_b16 v[220:221], v225 offset:37440\n"
    "s_branch .LkL_66_%=\n"
    ".LkL_65_%=:\n"
    "ds_read_b64_tr_b16 v[206:207], v224 offset:0\n"
    "ds_read_b64_tr_b16 v[208:209], v224 offset:1088\n"
    "ds_read_b64_tr_b16 v[210:211], v224 offset:8704\n"
    "ds_read_b64_tr_b16 v[212:213], v224 offset:9792\n"
    "ds_read_b64_tr_b16 v[214:215], v225 offset:0\n"
    "ds_read_b64_tr_b16 v[216:217], v225 offset:1088\n"
    "ds_read_b64_tr_b16 v[218:219], v225 offset:8704\n"
    "ds_read_b64_tr_b16 v[220:221], v225 offset:9792\n"
    ".LkL_66_%=:\n"
    "v_pk_mul_f32 v[52:53], v[28:29], v[36:37] op_sel:[1,1] op_sel_hi:[1,0] neg_lo:[1,0]\n"
    "v_cvt_pk_bf16_f32 v89, v28, v29\n"
    "v_pk_mul_f32 v[54:55], v[30:31], v[38:39] op_sel:[1,1] op_sel_hi:[1,0] neg_lo:[1,0]\n"
    "v_cvt_pk_bf16_f32 v121, v30, v31\n"
    "v_pk_mul_f32 v[56:57], v[48:49], v[36:37] op_sel:[1,1] op_sel_hi:[1,0] neg_lo:[1,0]\n"
    "v_cvt_pk_bf16_f32 v90, v48, v49\n"
    "v_pk_mul_f32 v[58:59], v[50:51], v[38:39] op_sel:[1,1] op_sel_hi:[1,0] neg_lo:[1,0]\n"
    "v_cvt_pk_bf16_f32 v122, v50, v51\n"
    "v_pk_fma_f32 v[28:29], v[28:29], v[36:37], v[52:53] op_sel_hi:[0,1,1]\n"
    "v_pk_fma_f32 v[30:31], v[30:31], v[38:39], v[54:55] op_sel_hi:[0,1,1]\n"
    "v_pk_fma_f32 v[48:49], v[48:49], v[36:37], v[56:57] op_sel_hi:[0,1,1]\n"
    "v_pk_fma_f32 v[50:51], v[50:51], v[38:39], v[58:59] op_sel_hi:[0,1,1]\n"
    "v_pk_mul_f32 v[52:53], v[28:29], v[36:37] op_sel:[1,1] op_sel_hi:[1,0] neg_lo:[1,0]\n"
    "v_cvt_pk_bf16_f32 v91, v28, v29\n"
    "v_pk_mul_f32 v[54:55], v[30:31], v[38:39] op_sel:[1,1] op_sel_hi:[1,0] neg_lo:[1,0]\n"
    "v_cvt_pk_bf16_f32 v123, v30, v31\n"
    "v_pk_mul_f32 v[56:57], v[48:49], v[36:37] op_sel:[1,1] op_sel_hi:[1,0] neg_lo:[1,0]\n"
    "v_cvt_pk_bf16_f32 v92, v48, v49\n"
    "v_pk_mul_f32 v[58:59], v[50:51], v[38:39] op_sel:[1,1] op_sel_hi:[1,0] neg_lo:[1,0]\n"
    "v_cvt_pk_bf16_f32 v124, v50, v51\n"
    "v_pk_fma_f32 v[28:29], v[28:29], v[36:37], v[52:53] op_sel_hi:[0,1,1]\n"
    "v_pk_fma_f32 v[30:31], v[30:31], v[38:39], v[54:55] op_sel_hi:[0,1,1]\n"
    "v_pk_fma_f32 v[48:49], v[48:49], v[36:37], v[56:57] op_sel_hi:[0,1,1]\n"
    "v_pk_fma_f32 v[50:51], v[50:51], v[38:39], v[58:59] op_sel_hi:[0,1,1]\n"
    "s_waitcnt lgkmcnt(0)\n"
    "ds_add_u32 v254, v235 offset:4\n"
    "s_waitcnt vmcnt(0)\n"
    "ds_add_u32 v254, v235 offset:60\n"
    "buffer_store_dwordx4 v[206:209], v226, s[12:15], s88 offen offset:0 sc1\n"
    "buffer_store_dwordx4 v[210:213], v226, s[12:15], s88 offen offset:64 sc1\n"
    "s_add_u32 s88, s88, 65536\n"
    "s_cmp_eq_u32 s24, 6\n"
    "s_cselect_b32 s88, 0x7f000000, s88\n"
    "buffer_store_dwordx4 v[214:217], v226, s[12:15], s88 offen offset:0 sc1\n"
    "buffer_store_dwordx4 v[218:221], v226, s[12:15], s88 offen offset:64 sc1\n"
    "v_pk_mul_f32 v[52:53], v[28:29], v[36:37] op_sel:[1,1] op_sel_hi:[1,0] neg_lo:[1,0]\n"
    "v_cvt_pk_bf16_f32 v93, v28, v29\n"
    "v_pk_mul_f32 v[54:55], v[30:31], v[38:39] op_sel:[1,1] op_sel_hi:[1,0] neg_lo:[1,0]\n"
    "v_cvt_pk_bf16_f32 v125, v30, v31\n"
    "v_cvt_pk_bf16_f32 v94, v48, v49\n"
    "v_cvt_pk_bf16_f32 v126, v50, v51\n"
    "v_pk_fma_f32 v[28:29], v[28:29], v[36:37], v[52:53] op_sel_hi:[0,1,1]\n"
    "v_pk_fma_f32 v[30:31], v[30:31], v[38:39], v[54:55] op_sel_hi:[0,1,1]\n"
    "v_cvt_pk_bf16_f32 v95, v28, v29\n"
    "v_cvt_pk_bf16_f32 v127, v30, v31\n"
    "v_pk_mul_f32 v[52:53], v[24:25], v[24:25] op_sel:[1,1] op_sel_hi:[1,0] neg_lo:[1,0]\n"
    "v_pk_mul_f32 v[54:55], v[26:27], v[26:27] op_sel:[1,1] op_sel_hi:[1,0] neg_lo:[1,0]\n"
    "v_pk_fma_f32 v[36:37], v[24:25], v[24:25], v[52:53] op_sel_hi:[0,1,1]\n"
    "v_pk_fma_f32 v[38:39], v[26:27], v[26:27], v[54:55] op_sel_hi:[0,1,1]\n"
    "v_pk_mul_f32 v[52:53], v[36:37], v[36:37] op_sel:[1,1] op_sel_hi:[1,0] neg_lo:[1,0]\n"
    "v_pk_mul_f32 v[54:55], v[38:39], v[38:39] op_sel:[1,1] op_sel_hi:[1,0] neg_lo:[1,0]\n"
    "v_pk_fma_f32 v[36:37], v[36:37], v[36:37], v[52:53] op_sel_hi:[0,1,1]\n"
    "v_pk_fma_f32 v[38:39], v[38:39], v[38:39], v[54:55] op_sel_hi:[0,1,1]\n"
    "v_pk_mul_f32 v[52:53], v[44:45], v[32:33] op_sel:[1,1] op_sel_hi:[1,0] neg_lo:[1,0]\n"
    "v_pk_mul_f32 v[54:55], v[46:47], v[34:35] op_sel:[1,1] op_sel_hi:[1,0] neg_lo:[1,0]\n"
    "v_pk_mul_f32 v[56:57], v[40:41], v[24:25] op_sel:[1,1] op_sel_hi:[1,0] neg_lo:[1,0]\n"
    "v_pk_mul_f32 v[58:59], v[42:43], v[26:27] op_sel:[1,1] op_sel_hi:[1,0] neg_lo:[1,0]\n"
    "v_pk_fma_f32 v[44:45], v[44:45], v[32:33], v[52:53] op_sel_hi:[0,1,1]\n"
    "v_pk_fma_f32 v[46:47], v[46:47], v[34:35], v[54:55] op_sel_hi:[0,1,1]\n"
    "v_pk_fma_f32 v[40:41], v[40:41], v[24:25], v[56:57] op_sel_hi:[0,1,1]\n"
    "v_pk_fma_f32 v[42:43], v[42:43], v[26:27], v[58:59] op_sel_hi:[0,1,1]\n"
    "v_cvt_pk_bf16_f32 v136, v44, -v45\n"
    "v_cvt_pk_bf16_f32 v129, v40, -v41\n"
    "v_cvt_pk_bf16_f32 v192, v46, -v47\n"
    "v_cvt_pk_bf16_f32 v185, v42, -v43\n"
    "v_pk_mul_f32 v[52:53], v[44:45], v[32:33] op_sel:[1,1] op_sel_hi:[1,0] neg_lo:[1,0]\n"
    "v_pk_mul_f32 v[54:55], v[46:47], v[34:35] op_sel:[1,1] op_sel_hi:[1,0] neg_lo:[1,0]\n"
    "v_pk_mul_f32 v[56:57], v[40:41], v[24:25] op_sel:[1,1] op_sel_hi:[1,0] neg_lo:[1,0]\n"
    "v_pk_mul_f32 v[58:59], v[42:43], v[26:27] op_sel:[1,1] op_sel_hi:[1,0] neg_lo:[1,0]\n"
    "v_pk_fma_f32 v[44:45], v[44:45], v[32:33], v[52:53] op_sel_hi:[0,1,1]\n"
    "v_pk_fma_f32 v[46:47], v[46:47], v[34:35], v[54:55] op_sel_hi:[0,1,1]\n"
    "v_pk_fma_f32 v[40:41], v[40:41], v[24:25], v[56:57] op_sel_hi:[0,1,1]\n"
    "v_pk_fma_f32 v[42:43], v[42:43], v[26:27], v[58:59] op_sel_hi:[0,1,1]\n"
    "v_pk_mul_f32 v[52:53], v[40:41], v[32:33] op_sel:[1,1] op_sel_hi:[1,0] neg_lo:[1,0]\n"
    "v_pk_mul_f32 v[54:55], v[42:43], v[34:35] op_sel:[1,1] op_sel_hi:[1,0] neg_lo:[1,0]\n"
    "v_pk_mul_f32 v[56:57], v[44:45], v[36:37] op_sel:[1,1] op_sel_hi:[1,0] neg_lo:[1,0]\n"
    "v_pk_mul_f32 v[58:59], v[46:47], v[38:39] op_sel:[1,1] op_sel_hi:[1,0] neg_lo:[1,0]\n"
    "v_pk_fma_f32 v[48:49], v[40:41], v[32:33], v[52:53] op_sel_hi:[0,1,1]\n"
    "v_pk_fma_f32 v[50:51], v[42:43], v[34:35], v[54:55] op_sel_hi:[0,1,1]\n"
    "v_pk_fma_f32 v[28:29], v[44:45], v[36:37], v[56:57] op_sel_hi:[0,1,1]\n"
    "v_pk_fma_f32 v[30:31], v[46:47], v[38:39], v[58:59] op_sel_hi:[0,1,1]\n"
    "v_cvt_pk_bf16_f32 v140, v44, -v45\n"
    "v_cvt_pk_bf16_f32 v130, v40, -v41\n"
    "v_cvt_pk_bf16_f32 v137, v48, -v49\n"
    "v_cvt_pk_bf16_f32 v141, v28, -v29\n"
    "v_cvt_pk_bf16_f32 v196, v46, -v47\n"
    "v_cvt_pk_bf16_f32 v186, v42, -v43\n"
    "v_cvt_pk_bf16_f32 v193, v50, -v51\n"
    "v_cvt_pk_bf16_f32 v197, v30, -v31\n"
    "v_pk_mul_f32 v[52:53], v[44:45], v[32:33] op_sel:[1,1] op_sel_hi:[1,0] neg_lo:[1,0]\n"
    "v_pk_mul_f32 v[54:55], v[46:47], v[34:35] op_sel:[1,1] op_sel_hi:[1,0] neg_lo:[1,0]\n"
    "v_pk_mul_f32 v[56:57], v[40:41], v[24:25] op_sel:[1,1] op_sel_hi:[1,0] neg_lo:[1,0]\n"
    "v_pk_mul_f32 v[58:59], v[42:43], v[26:27] op_sel:[1,1] op_sel_hi:[1,0] neg_lo:[1,0]\n"
    "v_pk_fma_f32 v[44:45], v[44:45], v[32:33], v[52:53] op_sel_hi:[0,1,1]\n"
    "v_pk_fma_f32 v[46:47], v[46:47], v[34:35], v[54:55] op_sel_hi:[0,1,1]\n"
    "v_pk_fma_f32 v[40:41], v[40:41], v[24:25], v[56:57] op_sel_hi:[0,1,1]\n"
    "v_pk_fma_f32 v[42:43], v[42:43], v[26:27], v[58:59] op_sel_hi:[0,1,1]\n"
    "v_pk_mul_f32 v[52:53], v[44:45], v[36:37] op_sel:[1,1] op_sel_hi:[1,0] neg_lo:[1,0]\n"
    "v_pk_mul_f32 v[54:55], v[46:47], v[38:39] op_sel:[1,1] op_sel_hi:[1,0] neg_lo:[1,0]\n"
    "v_pk_fma_f32 v[28:29], v[44:45], v[36:37], v[52:53] op_sel_hi:[0,1,1]\n"
    "v_pk_fma_f32 v[30:31], v[46:47], v[38:39], v[54:55] op_sel_hi:[0,1,1]\n"
    "v_cvt_pk_bf16_f32 v142, v44, -v45\n"
    "v_cvt_pk_bf16_f32 v131, v40, -v41\n"
    "v_cvt_pk_bf16_f32 v143, v28, -v29\n"
    "v_cvt_pk_bf16_f32 v198, v46, -v47\n"
    "v_cvt_pk_bf16_f32 v187, v42, -v43\n"
    "v_cvt_pk_bf16_f32 v199, v30, -v31\n"
    "v_pk_mul_f32 v[52:53], v[44:45], v[32:33] op_sel:[1,1] op_sel_hi:[1,0] neg_lo:[1,0]\n"
    "v_pk_mul_f32 v[54:55], v[46:47], v[34:35] op_sel:[1,1] op_sel_hi:[1,0] neg_lo:[1,0]\n"
    "v_pk_mul_f32 v[56:57], v[40:41], v[24:25] op_sel:[1,1] op_sel_hi:[1,0] neg_lo:[1,0]\n"
    "v_pk_mul_f32 v[58:59], v[42:43], v[26:27] op_sel:[1,1] op_sel_hi:[1,0] neg_lo:[1,0]\n"
    "v_pk_fma_f32 v[44:45], v[44:45], v[32:33], v[52:53] op_sel_hi:[0,1,1]\n"
    "v_pk_fma_f32 v[46:47], v[46:47], v[34:35], v[54:55] op_sel_hi:[0,1,1]\n"
    "v_pk_fma_f32 v[40:41], v[40:41], v[24:25], v[56:57] op_sel_hi:[0,1,1]\n"
    "v_pk_fma_f32 v[42:43], v[42:43], v[26:27], v[58:59] op_sel_hi:[0,1,1]\n"
    "v_pk_mul_f32 v[52:53], v[40:41], v[32:33] op_sel:[1,1] op_sel_hi:[1,0] neg_lo:[1,0]\n"
    "v_pk_mul_f32 v[54:55], v[42:43], v[34:35] op_sel:[1,1] op_sel_hi:[1,0] neg_lo:[1,0]\n"
    "v_pk_fma_f32 v[48:49], v[40:41], v[32:33], v[52:53] op_sel_hi:[0,1,1]\n"
    "v_pk_fma_f32 v[50:51], v[42:43], v[34:35], v[54:55] op_sel_hi:[0,1,1]\n"
    "v_cvt_pk_bf16_f32 v144, v44, -v45\n"
    "v_cvt_pk_bf16_f32 v132, v40, -v41\n"
    "v_cvt_pk_bf16_f32 v138, v48, -v49\n"
    "v_cvt_pk_bf16_f32 v200, v46, -v47\n"
    "v_cvt_pk_bf16_f32 v188, v42, -v43\n"
    "v_cvt_pk_bf16_f32 v194, v50, -v51\n"
    "v_pk_mul_f32 v[52:53], v[44:45], v[32:33] op_sel:[1,1] op_sel_hi:[1,0] neg_lo:[1,0]\n"
    "v_pk_mul_f32 v[54:55], v[46:47], v[34:35] op_sel:[1,1] op_sel_hi:[1,0] neg_lo:[1,0]\n"
    "v_pk_mul_f32 v[56:57], v[40:41], v[24:25] op_sel:[1,1] op_sel_hi:[1,0] neg_lo:[1,0]\n"
    "v_pk_mul_f32 v[58:59], v[42:43], v[26:27] op_sel:[1,1] op_sel_hi:[1,0] neg_lo:[1,0]\n"
    "v_pk_fma_f32 v[44:45], v[44:45], v[32:33], v[52:53] op_sel_hi:[0,1,1]\n"
    "v_pk_fma_f32 v[46:47], v[46:47], v[34:35], v[54:55] op_sel_hi:[0,1,1]\n"
    "v_pk_fma_f32 v[40:41], v[40:41], v[24:25], v[56:57] op_sel_hi:[0,1,1]\n"
    "v_pk_fma_f32 v[42:43], v[42:43], v[26:27], v[58:59] op_sel_hi:[0,1,1]\n"
    "v_cvt_pk_bf16_f32 v145, v44, -v45\n"
    "v_cvt_pk_bf16_f32 v133, v40, -v41\n"
    "v_cvt_pk_bf16_f32 v201, v46, -v47\n"
    "v_cvt_pk_bf16_f32 v189, v42, -v43\n"
    "v_pk_mul_f32 v[52:53], v[44:45], v[32:33] op_sel:[1,1] op_sel_hi:[1,0] neg_lo:[1,0]\n"
    "v_pk_mul_f32 v[54:55], v[46:47], v[34:35] op_sel:[1,1] op_sel_hi:[1,0] neg_lo:[1,0]\n"
    "v_pk_mul_f32 v[56:57], v[40:41], v[24:25] op_sel:[1,1] op_sel_hi:[1,0] neg_lo:[1,0]\n"
    "v_pk_mul_f32 v[58:59], v[42:43], v[26:27] op_sel:[1,1] op_sel_hi:[1,0] neg_lo:[1,0]\n"
    "v_pk_fma_f32 v[44:45], v[44:45], v[32:33], v[52:53] op_sel_hi:[0,1,1]\n"
    "v_pk_fma_f32 v[46:47], v[46:47], v[34:35], v[54:55] op_sel_hi:[0,1,1]\n"
    "v_pk_fma_f32 v[40:41], v[40:41], v[24:25], v[56:57] op_sel_hi:[0,1,1]\n"
    "v_pk_fma_f32 v[42:43], v[42:43], v[26:27], v[58:59] op_sel_hi:[0,1,1]\n"
    "v_pk_mul_f32 v[52:53], v[40:41], v[32:33] op_sel:[1,1] op_sel_hi:[1,0] neg_lo:[1,0]\n"
    "v_pk_mul_f32 v[54:55], v[42:43], v[34:35] op_sel:[1,1] op_sel_hi:[1,0] neg_lo:[1,0]\n"
    "v_pk_fma_f32 v[48:49], v[40:41], v[32:33], v[52:53] op_sel_hi:[0,1,1]\n"
    "v_pk_fma_f32 v[50:51], v[42:43], v[34:35], v[54:55] op_sel_hi:[0,1,1]\n"
    "v_cvt_pk_bf16_f32 v146, v44, -v45\n"
    "v_cvt_pk_bf16_f32 v134, v40, -v41\n"
    "v_cvt_pk_bf16_f32 v139, v48, -v49\n"
    "v_cvt_pk_bf16_f32 v202, v46, -v47\n"
    "v_cvt_pk_bf16_f32 v190, v42, -v43\n"
    "v_cvt_pk_bf16_f32 v195, v50, -v51\n"
    "v_pk_mul_f32 v[52:53], v[44:45], v[32:33] op_sel:[1,1] op_sel_hi:[1,0] neg_lo:[1,0]\n"
    "v_pk_mul_f32 v[54:55], v[46:47], v[34:35] op_sel:[1,1] op_sel_hi:[1,0] neg_lo:[1,0]\n"
    "v_pk_mul_f32 v[56:57], v[40:41], v[24:25] op_sel:[1,1] op_sel_hi:[1,0] neg_lo:[1,0]\n"
    "v_pk_mul_f32 v[58:59], v[42:43], v[26:27] op_sel:[1,1] op_sel_hi:[1,0] neg_lo:[1,0]\n"
    "v_pk_fma_f32 v[44:45], v[44:45], v[32:33], v[52:53] op_sel_hi:[0,1,1]\n"
    "v_pk_fma_f32 v[46:47], v[46:47], v[34:35], v[54:55] op_sel_hi:[0,1,1]\n"
    "v_pk_fma_f32 v[40:41], v[40:41], v[24:25], v[56:57] op_sel_hi:[0,1,1]\n"
    "v_pk_fma_f32 v[42:43], v[42:43], v[26:27], v[58:59] op_sel_hi:[0,1,1]\n"
    "v_cvt_pk_bf16_f32 v147, v44, -v45\n"
    "v_cvt_pk_bf16_f32 v135, v40, -v41\n"
    "v_cvt_pk_bf16_f32 v203, v46, -v47\n"
    "v_cvt_pk_bf16_f32 v191, v42, -v43\n"
    "v_pk_mul_f32 v[52:53], v[44:45], v[32:33] op_sel:[1,1] op_sel_hi:[1,0] neg_lo:[1,0]\n"
    "v_pk_mul_f32 v[54:55], v[46:47], v[34:35] op_sel:[1,1] op_sel_hi:[1,0] neg_lo:[1,0]\n"
    "v_pk_fma_f32 v[44:45], v[44:45], v[32:33], v[52:53] op_sel_hi:[0,1,1]\n"
    "v_pk_fma_f32 v[46:47], v[46:47], v[34:35], v[54:55] op_sel_hi:[0,1,1]\n"
    "v_cvt_pk_bf16_f32 v148, v44, -v45\n"
    "v_cvt_pk_bf16_f32 v204, v46, -v47\n"
    ".Lkw_67_%=:\n"
    "ds_read_b32 v1, v245 offset:4\n"
    "s_waitcnt lgkmcnt(0)\n"
    "v_readfirstlane_b32 s47, v1\n"
    "s_cmp_ge_u32 s47, 8\n"
    "s_cbranch_scc1 .Lkd_68_%=\n"
    "s_sleep 1\n"
    "s_sub_u32 s80, s80, 1\n"
    "s_cbranch_scc0 .Lkw_67_%=\n"
    "s_mov_b32 s80, 0\n"
    ".Lkd_68_%=:\n"
    "s_setprio 3\n"
    "ds_write_b32 v60, v128 offset:0\n"
    "ds_write_b32 v60, v129 offset:272\n"
    "ds_write_b32 v60, v130 offset:544\n"
    "ds_write_b32 v60, v131 offset:816\n"
    "ds_write_b32 v60, v132 offset:1088\n"
    "ds_write_b32 v60, v133 offset:1360\n"
    "ds_write_b32 v60, v134 offset:1632\n"
    "ds_write_b32 v60, v135 offset:1904\n"
    "ds_write_b32 v60, v136 offset:2176\n"
    "ds_write_b32 v60, v137 offset:2448\n"
    "ds_write_b32 v60, v138 offset:2720\n"
    "ds_write_b32 v60, v139 offset:2992\n"
    "ds_write_b32 v60, v140 offset:3264\n"
    "ds_write_b32 v60, v141 offset:3536\n"
    "ds_write_b32 v60, v142 offset:3808\n"
    "ds_write_b32 v60, v143 offset:4080\n"
    "ds_write_b32 v60, v144 offset:4352\n"
    "ds_write_b32 v60, v145 offset:4624\n"
    "ds_write_b32 v60, v146 offset:4896\n"
    "ds_write_b32 v60, v147 offset:5168\n"
    "ds_write_b32 v60, v148 offset:5440\n"
    "ds_write_b32 v60, v64 offset:5712\n"
    "ds_write_b32 v60, v65 offset:5984\n"
    "ds_write_b32 v60, v66 offset:6256\n"
    "ds_write_b32 v60, v67 offset:6528\n"
    "ds_write_b32 v60, v68 offset:6800\n"
    "ds_write_b32 v60, v69 offset:7072\n"
    "ds_write_b32 v60, v70 offset:7344\n"
    "ds_write_b32 v60, v71 offset:7616\n"
    "ds_write_b32 v60, v72 offset:7888\n"
    "ds_write_b32 v60, v73 offset:8160\n"
    "ds_write_b32 v60, v74 offset:8432\n"
    "ds_write_b32 v60, v75 offset:8704\n"
    "ds_write_b32 v60, v76 offset:8976\n"
    "ds_write_b32 v60, v77 offset:9248\n"
    "ds_write_b32 v60, v78 offset:9520\n"
    "ds_write_b32 v60, v79 offset:9792\n"
    "ds_write_b32 v60, v80 offset:10064\n"
    "ds_write_b32 v60, v81 offset:10336\n"
    "ds_write_b32 v60, v82 offset:10608\n"
    "ds_write_b32 v60, v83 offset:10880\n"
    "ds_write_b32 v60, v84 offset:11152\n"
    "ds_write_b32 v60, v85 offset:11424\n"
    "ds_write_b32 v60, v86 offset:11696\n"
    "ds_write_b32 v60, v87 offset:11968\n"
    "ds_write_b32 v60, v88 offset:12240\n"
    "ds_write_b32 v60, v89 offset:12512\n"
    "ds_write_b32 v60, v90 offset:12784\n"
    "ds_write_b32 v60, v91 offset:13056\n"
    "ds_write_b32 v60, v92 offset:13328\n"
    "ds_write_b32 v60, v93 offset:13600\n"
    "ds_write_b32 v60, v94 offset:13872\n"
    "ds_write_b32 v60, v95 offset:14144\n"
    "s_waitcnt vmcnt(0)\n"
    "ds_add_rtn_u32 v227, v254, v235 offset:24\n"
    "s_waitcnt lgkmcnt(0)\n"
    "v_readfirstlane_b32 s47, v227\n"
    "s_lshl_b32 s38, s26, 5\n"
    "s_cmp_lg_u32 s47, 2\n"
    "s_cbranch_scc1 .LkL_69_%=\n"
    "s_add_u32 s38, s38, s25\n"
    "s_lshl_b32 s38, s38, 3\n"
    "s_add_u32 s38, s38, 245760\n"
    "v_mov_b32 v252, s34\n"
    "v_mov_b32 v253, s35\n"
    "buffer_store_dwordx2 v[252:253], v254, s[12:15], s38 offen sc1\n"
    ".LkL_69_%=:\n"
    "ds_read_b128 v[20:23], v61 offset:0\n"
    "ds_read_b128 v[24:27], v61 offset:5712\n"
    "ds_read_b128 v[28:31], v61 offset:32\n"
    "ds_read_b128 v[32:35], v61 offset:5744\n"
    "ds_read_b128 v[36:39], v61 offset:64\n"
    "ds_read_b128 v[40:43], v61 offset:5776\n"
    "ds_read_b128 v[44:47], v61 offset:96\n"
    "ds_read_b128 v[48:51], v61 offset:5808\n"
    "s_waitcnt lgkmcnt(6)\n"
    "v_mfma_f32_32x32x16_bf16 v[4:19], v[20:23], v[24:27], 0\n"
    "ds_read_b128 v[20:23], v61 offset:128\n"
    "ds_read_b128 v[24:27], v61 offset:5840\n"
    "s_waitcnt lgkmcnt(6)\n"
    "v_mfma_f32_32x32x16_bf16 v[4:19], v[28:31], v[32:35], v[4:19]\n"
    "ds_read_b128 v[28:31], v61 offset:160\n"
    "ds_read_b128 v[32:35], v61 offset:5872\n"
    "s_waitcnt lgkmcnt(6)\n"
    "v_mfma_f32_32x32x16_bf16 v[4:19], v[36:39], v[40:43], v[4:19]\n"
    "ds_read_b128 v[36:39], v61 offset:192\n"
    "ds_read_b128 v[40:43], v61 offset:5904\n"
    "s_waitcnt lgkmcnt(6)\n"
    "v_mfma_f32_32x32x16_bf16 v[4:19], v[44:47], v[48:51], v[4:19]\n"
    "ds_read_b128 v[44:47], v61 offset:224\n"
    "ds_read_b128 v[48:51], v61 offset:5936\n"
    "s_waitcnt lgkmcnt(6)\n"
    "v_mfma_f32_32x32x16_bf16 v[4:19], v[20:23], v[24:27], v[4:19]\n"
    "s_waitcnt lgkmcnt(4)\n"
    "v_mfma_f32_32x32x16_bf16 v[4:19], v[28:31], v[32:35], v[4:19]\n"
    "s_waitcnt lgkmcnt(2)\n"
    "v_mfma_f32_32x32x16_bf16 v[4:19], v[36:39], v[40:43], v[4:19]\n"
    "s_waitcnt lgkmcnt(0)\n"
    "v_mfma_f32_32x32x16_bf16 v[4:19], v[44:47], v[48:51], v[4:19]\n"
    "ds_write_b32 v60, v184 offset:0\n"
    "ds_write_b32 v60, v185 offset:272\n"
    "ds_write_b32 v60, v186 offset:544\n"
    "ds_write_b32 v60, v187 offset:816\n"
    "ds_write_b32 v60, v188 offset:1088\n"
    "ds_write_b32 v60, v189 offset:1360\n"
    "ds_write_b32 v60, v190 offset:1632\n"
    "ds_write_b32 v60, v191 offset:1904\n"
    "ds_write_b32 v60, v192 offset:2176\n"
    "ds_write_b32 v60, v193 offset:2448\n"
    "ds_write_b32 v60, v194 offset:2720\n"
    "ds_write_b32 v60, v195 offset:2992\n"
    "ds_write_b32 v60, v196 offset:3264\n"
    "ds_write_b32 v60, v197 offset:3536\n"
    "ds_write_b32 v60, v198 offset:3808\n"
    "ds_write_b32 v60, v199 offset:4080\n"
    "ds_write_b32 v60, v200 offset:4352\n"
    "ds_write_b32 v60, v201 offset:4624\n"
    "ds_write_b32 v60, v202 offset:4896\n"
    "ds_write_b32 v60, v203 offset:5168\n"
    "ds_write_b32 v60, v204 offset:5440\n"
    "ds_write_b32 v60, v96 offset:5712\n"
    "ds_write_b32 v60, v97 offset:5984\n"
    "ds_write_b32 v60, v98 offset:6256\n"
    "ds_write_b32 v60, v99 offset:6528\n"
    "ds_write_b32 v60, v100 offset:6800\n"
    "ds_write_b32 v60, v101 offset:7072\n"
    "ds_write_b32 v60, v102 offset:7344\n"
    "ds_write_b32 v60, v103 offset:7616\n"
    "ds_write_b32 v60, v104 offset:7888\n"
    "ds_write_b32 v60, v105 offset:8160\n"
    "ds_write_b32 v60, v106 offset:8432\n"
    "ds_write_b32 v60, v107 offset:8704\n"
    "ds_write_b32 v60, v108 offset:8976\n"
    "ds_write_b32 v60, v109 offset:9248\n"
    "ds_write_b32 v60, v110 offset:9520\n"
    "ds_write_b32 v60, v111 offset:9792\n"
    "ds_write_b32 v60, v112 offset:10064\n"
    "ds_write_b32 v60, v113 offset:10336\n"
    "ds_write_b32 v60, v114 offset:10608\n"
    "ds_write_b32 v60, v115 offset:10880\n"
    "ds_write_b32 v60, v116 offset:11152\n"
    "ds_write_b32 v60, v117 offset:11424\n"
    "ds_write_b32 v60, v118 offset:11696\n"
    "ds_write_b32 v60, v119 offset:11968\n"
    "ds_write_b32 v60, v120 offset:12240\n"
    "ds_write_b32 v60, v121 offset:12512\n"
    "ds_write_b32 v60, v122 offset:12784\n"
    "ds_write_b32 v60, v123 offset:13056\n"
    "ds_write_b32 v60, v124 offset:13328\n"
    "ds_write_b32 v60, v125 offset:13600\n"
    "ds_write_b32 v60, v126 offset:13872\n"
    "ds_write_b32 v60, v127 offset:14144\n"
    "buffer_load_dwordx2 v[220:221], v158, s[12:15], 0 offen sc1\n"
    "ds_read_b128 v[20:23], v61 offset:0\n"
    "ds_read_b128 v[24:27], v61 offset:5712\n"
    "ds_read_b128 v[28:31], v61 offset:32\n"
    "ds_read_b128 v[32:35], v61 offset:5744\n"
    "ds_read_b128 v[36:39], v61 offset:64\n"
    "ds_read_b128 v[40:43], v61 offset:5776\n"
    "ds_read_b128 v[44:47], v61 offset:96\n"
    "ds_read_b128 v[48:51], v61 offset:5808\n"
    "s_waitcnt lgkmcnt(6)\n"
    "v_mfma_f32_32x32x16_bf16 v[4:19], v[20:23], v[24:27], v[4:19]\n"
    "ds_read_b128 v[20:23], v61 offset:128\n"
    "ds_read_b128 v[24:27], v61 offset:5840\n"
    "s_waitcnt lgkmcnt(6)\n"
    "v_mfma_f32_32x32x16_bf16 v[4:19], v[28:31], v[32:35], v[4:19]\n"
    "ds_read_b128 v[28:31], v61 offset:160\n"
    "ds_read_b128 v[32:35], v61 offset:5872\n"
    "s_waitcnt lgkmcnt(6)\n"
    "v_mfma_f32_32x32x16_bf16 v[4:19], v[36:39], v[40:43], v[4:19]\n"
    "ds_read_b128 v[36:39], v61 offset:192\n"
    "ds_read_b128 v[40:43], v61 offset:5904\n"
    "s_waitcnt lgkmcnt(6)\n"
    "v_mfma_f32_32x32x16_bf16 v[4:19], v[44:47], v[48:51], v[4:19]\n"
    "ds_read_b128 v[44:47], v61 offset:224\n"
    "ds_read_b128 v[48:51], v61 offset:5936\n"
    "s_waitcnt lgkmcnt(6)\n"
    "v_mfma_f32_32x32x16_bf16 v[4:19], v[20:23], v[24:27], v[4:19]\n"
    "s_waitcnt lgkmcnt(4)\n"
    "v_mfma_f32_32x32x16_bf16 v[4:19], v[28:31], v[32:35], v[4:19]\n"
    "s_waitcnt lgkmcnt(2)\n"
    "v_mfma_f32_32x32x16_bf16 v[4:19], v[36:39], v[40:43], v[4:19]\n"
    "s_waitcnt lgkmcnt(0)\n"
    "v_mfma_f32_32x32x16_bf16 v[4:19], v[44:47], v[48:51], v[4:19]\n"
    "s_nop 7\n"
    "v_mov_b32 v20, s40\n"
    "v_cmp_eq_u32 vcc, 0, v230\n"
    "v_lshlrev_b32 v21, 1, v233\n"
    "v_add_u32 v21, s44, v21\n"
    "v_cndmask_b32 v20, 0, v20, vcc\n"
    "v_add_f32 v4, v4, v20\n"
    "v_cvt_pk_bf16_f32 v24, v4, v5\n"
    "v_cvt_pk_bf16_f32 v25, v6, v7\n"
    "v_cvt_pk_bf16_f32 v26, v8, v9\n"
    "v_cvt_pk_bf16_f32 v27, v10, v11\n"
    "v_cvt_pk_bf16_f32 v28, v12, v13\n"
    "v_cvt_pk_bf16_f32 v29, v14, v15\n"
    "v_lshlrev_b32 v22, 4, v233\n"
    "v_lshl_add_u32 v22, v234, 3, v22\n"
    "v_add_u32 v22, s44, v22\n"
    "ds_write_b64 v22, v[24:25]\n"
    "v_lshl_add_u32 v22, v233, 3, s44\n"
    "v_lshl_add_u32 v23, v233, 2, s44\n"
    "v_and_b32 v30, 0xffff, v28\n"
    "s_mov_b32 exec_hi, 0\n"
    "ds_write_b64 v22, v[26:27] offset:512\n"
    "ds_write_b16 v21, v28 offset:1024\n"
    "ds_write_b16_d16_hi v21, v28 offset:1088\n"
    "ds_write_b16 v21, v29 offset:1152\n"
    "ds_write_b16_d16_hi v21, v29 offset:1216\n"
    "s_not_b64 exec, exec\n"
    "ds_write_b32 v23, v26 offset:768\n"
    "ds_write_b32 v23, v27 offset:896\n"
    "s_mov_b32 exec_lo, 0\n"
    "s_mov_b32 exec_hi, 1\n"
    "ds_write_b32 v23, v30 offset:1280\n"
    "s_mov_b64 exec, -1\n"
    "s_mov_b32 s38, 0x05040100\n"
    "v_lshlrev_b32 v20, 1, v230\n"
    "v_sub_u32 v20, s44, v20\n"
    "v_lshlrev_b32 v21, 2, v230\n"
    "v_sub_u32 v21, s44, v21\n"
    "v_mov_b32 v22, s44\n"
    "ds_read_u16 v32, v20 offset:1280\n"
    "ds_read_u16 v33, v20 offset:1278\n"
    "ds_read_u16 v34, v20 offset:1152\n"
    "ds_read_u16 v35, v20 offset:1150\n"
    "ds_read_u16 v36, v22 offset:1022\n"
    "ds_read_u16 v38, v21 offset:1024\n"
    "ds_read_u16 v39, v21 offset:1022\n"
    "ds_read_u16 v40, v21 offset:1020\n"
    "ds_read_u16 v41, v21 offset:1018\n"
    "ds_read_u16 v42, v22 offset:764\n"
    "ds_read_u16 v44, v172\n"
    "ds_read_u16 v45, v173\n"
    "ds_read_u16 v46, v174\n"
    "ds_read_u16 v47, v175\n"
    "ds_read_u16 v48, v176\n"
    "ds_read_u16 v49, v177\n"
    "ds_read_u16 v50, v178\n"
    "ds_read_u16 v51, v179\n"
    "ds_read_u16 v52, v180\n"
    "ds_read_u16 v53, v181\n"
    "ds_read_u16 v54, v182\n"
    "v_and_b32 v23, 1, v230\n"
    "v_cmp_eq_u32 s[88:89], 0, v23\n"
    "v_cmp_eq_u32 s[98:99], 63, v230\n"
    "s_waitcnt lgkmcnt(15)\n"
    "v_cndmask_b32 v24, v33, v32, s[88:89]\n"
    "v_cndmask_b32 v25, v35, v34, s[88:89]\n"
    "v_cndmask_b32 v26, v35, v36, s[98:99]\n"
    "v_perm_b32 v64, v32, v32, s38\n"
    "v_perm_b32 v65, v24, v32, s38\n"
    "v_perm_b32 v66, v33, v33, s38\n"
    "v_perm_b32 v67, v33, v33, s38\n"
    "v_perm_b32 v68, v24, v32, s38\n"
    "v_perm_b32 v69, v33, v33, s38\n"
    "v_perm_b32 v70, v33, v33, s38\n"
    "v_perm_b32 v71, v33, v33, s38\n"
    "v_perm_b32 v72, v32, v32, s38\n"
    "v_perm_b32 v73, v33, v24, s38\n"
    "v_perm_b32 v74, v33, v33, s38\n"
    "v_perm_b32 v75, v33, v33, s38\n"
    "v_perm_b32 v76, v33, v24, s38\n"
    "v_perm_b32 v77, v33, v33, s38\n"
    "v_perm_b32 v78, v33, v33, s38\n"
    "v_perm_b32 v79, v33, v33, s38\n"
    "v_perm_b32 v80, v34, v34, s38\n"
    "v_perm_b32 v81, v25, v34, s38\n"
    "v_perm_b32 v82, v35, v35, s38\n"
    "v_perm_b32 v83, v35, v35, s38\n"
    "v_perm_b32 v84, v25, v34, s38\n"
    "v_perm_b32 v85, v35, v35, s38\n"
    "v_perm_b32 v86, v35, v35, s38\n"
    "v_perm_b32 v87, v35, v35, s38\n"
    "v_perm_b32 v88, v34, v34, s38\n"
    "v_perm_b32 v89, v35, v25, s38\n"
    "v_perm_b32 v90, v35, v35, s38\n"
    "v_perm_b32 v91, v35, v35, s38\n"
    "v_perm_b32 v92, v35, v25, s38\n"
    "v_perm_b32 v93, v35, v35, s38\n"
    "v_perm_b32 v94, v35, v35, s38\n"
    "v_perm_b32 v95, v26, v35, s38\n"
    "s_waitcnt lgkmcnt(11)\n"
    "v_cndmask_b32 v27, v40, v41, s[98:99]\n"
    "v_cndmask_b32 v28, v41, v42, s[98:99]\n"
    "v_perm_b32 v96, v38, v38, s38\n"
    "v_perm_b32 v97, v39, v39, s38\n"
    "v_perm_b32 v98, v40, v39, s38\n"
    "v_perm_b32 v99, v40, v40, s38\n"
    "v_perm_b32 v100, v39, v39, s38\n"
    "v_perm_b32 v101, v40, v39, s38\n"
    "v_perm_b32 v102, v40, v40, s38\n"
    "v_perm_b32 v103, v27, v40, s38\n"
    "v_perm_b32 v104, v39, v38, s38\n"
    "v_perm_b32 v105, v39, v39, s38\n"
    "v_perm_b32 v106, v40, v40, s38\n"
    "v_perm_b32 v107, v40, v40, s38\n"
    "v_perm_b32 v108, v39, v39, s38\n"
    "v_perm_b32 v109, v40, v40, s38\n"
    "v_perm_b32 v110, v40, v40, s38\n"
    "v_perm_b32 v111, v28, v27, s38\n"
    "s_waitcnt lgkmcnt(0)\n"
    "v_perm_b32 v112, v45, v44, s38\n"
    "v_perm_b32 v113, v47, v46, s38\n"
    "v_perm_b32 v114, v49, v48, s38\n"
    "v_perm_b32 v115, v51, v50, s38\n"
    "v_perm_b32 v116, v47, v46, s38\n"
    "v_perm_b32 v117, v49, v48, s38\n"
    "v_perm_b32 v118, v51, v50, s38\n"
    "v_perm_b32 v119, v53, v52, s38\n"
    "v_perm_b32 v120, v46, v45, s38\n"
    "v_perm_b32 v121, v48, v47, s38\n"
    "v_perm_b32 v122, v50, v49, s38\n"
    "v_perm_b32 v123, v52, v51, s38\n"
    "v_perm_b32 v124, v48, v47, s38\n"
    "v_perm_b32 v125, v50, v49, s38\n"
    "v_perm_b32 v126, v52, v51, s38\n"
    "v_perm_b32 v127, v54, v53, s38\n"
    "v_mov_b32 v4, 0\n"
    "v_mov_b32 v5, 0\n"
    "v_mov_b32 v6, 0\n"
    "v_mov_b32 v7, 0\n"
    "s_waitcnt vmcnt(0)\n"
    "v_and_b32 v222, 0xffff0000, v220\n"
    "v_cmp_eq_u32 s[86:87], s35, v221\n"
    "v_cmp_eq_u32 vcc, s34, v222\n"
    "s_and_b64 vcc, vcc, s[86:87]\n"
    "s_cmp_eq_u64 vcc, -1\n"
    "s_cbranch_scc0 .LkL_70_%=\n"
    "s_mov_b32 s82, 1\n"
    "s_add_u32 s38, s90, 0\n"
    "buffer_load_dwordx4 v[128:131], v247, s[12:15], s38 offen sc1\n"
    "buffer_load_dwordx2 v[132:133], v156, s[12:15], s38 offen sc1\n"
    "buffer_load_dwordx4 v[134:137], v157, s[12:15], s38 offen sc1\n"
    "buffer_load_dwordx4 v[138:141], v157, s[12:15], s38 offen offset:672 sc1\n"
    "s_add_u32 s38, s90, 2688\n"
    "buffer_load_dwordx4 v[142:145], v247, s[12:15], s38 offen sc1\n"
    "buffer_load_dwordx2 v[146:147], v156, s[12:15], s38 offen sc1\n"
    "buffer_load_dwordx4 v[148:151], v157, s[12:15], s38 offen sc1\n"
    "buffer_load_dwordx4 v[152:155], v157, s[12:15], s38 offen offset:672 sc1\n"
    ".LkL_70_%=:\n"
    ".Lkw_71_%=:\n"
    "ds_read_b32 v1, v245 offset:52\n"
    "s_waitcnt lgkmcnt(0)\n"
    "v_readfirstlane_b32 s47, v1\n"
    "s_cmp_ge_u32 s47, 4\n"
    "s_cbranch_scc1 .Lkd_72_%=\n"
    "s_sleep 1\n"
    "s_sub_u32 s80, s80, 1\n"
    "s_cbranch_scc0 .Lkw_71_%=\n"
    "s_mov_b32 s80, 0\n"
    ".Lkd_72_%=:\n"
    "s_cmp_eq_u32 s24, 6\n"
    "s_cbranch_scc1 .LkL_73_%=\n"
    "s_mul_i32 s38, s43, 17664\n"
    "v_add_u32 v2, s38, v247\n"
    "ds_write_b128 v2, v[64:67] offset:13760\n"
    "ds_write_b128 v2, v[68:71] offset:4928\n"
    "ds_write_b128 v2, v[72:75] offset:9344\n"
    "ds_write_b128 v2, v[76:79] offset:512\n"
    "ds_write_b128 v2, v[80:83] offset:14784\n"
    "ds_write_b128 v2, v[84:87] offset:5952\n"
    "ds_write_b128 v2, v[88:91] offset:10368\n"
    "ds_write_b128 v2, v[92:95] offset:1536\n"
    "ds_write_b128 v2, v[96:99] offset:15808\n"
    "ds_write_b128 v2, v[100:103] offset:6976\n"
    "ds_write_b128 v2, v[104:107] offset:11392\n"
    "ds_write_b128 v2, v[108:111] offset:2560\n"
    "ds_write_b128 v2, v[112:115] offset:16832\n"
    "ds_write_b128 v2, v[116:119] offset:8000\n"
    "ds_write_b128 v2, v[120:123] offset:12416\n"
    "ds_write_b128 v2, v[124:127] offset:3584\n"
    "s_mov_b64 exec, 0x7f\n"
    "ds_write_b128 v2, v[4:7] offset:4608\n"
    "ds_write_b128 v2, v[4:7] offset:9024\n"
    "ds_write_b128 v2, v[4:7] offset:13440\n"
    "ds_write_b128 v2, v[4:7] offset:17856\n"
    "s_mov_b64 exec, -1\n"
    "s_branch .LkL_75_%=\n"
    ".LkL_73_%=:\n"
    "v_mov_b32 v2, v247\n"
    "s_cmp_eq_u32 s31, 1\n"
    "s_cbranch_scc1 .LkL_74_%=\n"
    "ds_write_b128 v2, v[96:99] offset:42944\n"
    "ds_write_b128 v2, v[100:103] offset:38208\n"
    "ds_write_b128 v2, v[104:107] offset:40576\n"
    "ds_write_b128 v2, v[108:111] offset:35840\n"
    "ds_write_b128 v2, v[112:115] offset:43968\n"
    "ds_write_b128 v2, v[116:119] offset:39232\n"
    "ds_write_b128 v2, v[120:123] offset:41600\n"
    "ds_write_b128 v2, v[124:127] offset:36864\n"
    "s_mov_b64 exec, 0x7f\n"
    "ds_write_b128 v2, v[4:7] offset:37888\n"
    "ds_write_b128 v2, v[4:7] offset:40256\n"
    "ds_write_b128 v2, v[4:7] offset:42624\n"
    "ds_write_b128 v2, v[4:7] offset:44992\n"
    "s_mov_b64 exec, -1\n"
    "s_branch .LkL_75_%=\n"
    ".LkL_74_%=:\n"
    "ds_write_b128 v2, v[64:67] offset:42944\n"
    "ds_write_b128 v2, v[68:71] offset:38208\n"
    "ds_write_b128 v2, v[72:75] offset:40576\n"
    "ds_write_b128 v2, v[76:79] offset:35840\n"
    "ds_write_b128 v2, v[80:83] offset:43968\n"
    "ds_write_b128 v2, v[84:87] offset:39232\n"
    "ds_write_b128 v2, v[88:91] offset:41600\n"
    "ds_write_b128 v2, v[92:95] offset:36864\n"
    "s_mov_b64 exec, 0xff\n"
    "ds_write_b128 v2, v[96:99] offset:44992\n"
    "ds_write_b128 v2, v[100:103] offset:40256\n"
    "ds_write_b128 v2, v[104:107] offset:42624\n"
    "ds_write_b128 v2, v[108:111] offset:37888\n"
    "s_mov_b64 exec, -1\n"
    ".LkL_75_%=:\n"
    "s_cmp_eq_u32 s82, 1\n"
    "s_cbranch_scc1 .Lkbq_8_%=\n"
    ".Lkp_76_%=:\n"
    "buffer_load_dwordx2 v[220:221], v158, s[12:15], 0 offen sc1\n"
    "s_waitcnt vmcnt(0)\n"
    "v_and_b32 v222, 0xffff0000, v220\n"
    "v_cmp_eq_u32 s[86:87], s35, v221\n"
    "v_cmp_eq_u32 vcc, s34, v222\n"
    "s_and_b64 vcc, vcc, s[86:87]\n"
    "s_cmp_eq_u64 vcc, -1\n"
    "s_cbranch_scc1 .Lkd_77_%=\n"
    "s_sleep 5\n"
    "s_sub_u32 s80, s80, 1\n"
    "s_cbranch_scc0 .Lkp_76_%=\n"
    "s_mov_b32 s80, 0\n"
    ".Lkd_77_%=:\n"
    "s_add_u32 s38, s90, 0\n"
    "buffer_load_dwordx4 v[128:131], v247, s[12:15], s38 offen sc1\n"
    "buffer_load_dwordx2 v[132:133], v156, s[12:15], s38 offen sc1\n"
    "buffer_load_dwordx4 v[134:137], v157, s[12:15], s38 offen sc1\n"
    "buffer_load_dwordx4 v[138:141], v157, s[12:15], s38 offen offset:672 sc1\n"
    "s_add_u32 s38, s90, 2688\n"
    "buffer_load_dwordx4 v[142:145], v247, s[12:15], s38 offen sc1\n"
    "buffer_load_dwordx2 v[146:147], v156, s[12:15], s38 offen sc1\n"
    "buffer_load_dwordx4 v[148:151], v157, s[12:15], s38 offen sc1\n"
    "buffer_load_dwordx4 v[152:155], v157, s[12:15], s38 offen offset:672 sc1\n"
    ".Lkbq_8_%=:\n"
    ".Lkw_78_%=:\n"
    "ds_read_b32 v124, v245 offset:32\n"
    "s_waitcnt lgkmcnt(0)\n"
    "v_readfirstlane_b32 s47, v124\n"
    "s_cmp_ge_u32 s47, 4\n"
    "s_cbranch_scc1 .Lkd_79_%=\n"
    "s_sleep 1\n"
    "s_sub_u32 s80, s80, 1\n"
    "s_cbranch_scc0 .Lkw_78_%=\n"
    "s_mov_b32 s80, 0\n"
    ".Lkd_79_%=:\n"
    "s_setprio 2\n"
    "s_mov_b32 s95, 0\n"
    "s_cmp_lt_u32 s24, 4\n"
    "s_cbranch_scc1 .LkL_80_%=\n"
    "s_waitcnt lgkmcnt(0)\n"
    "v_mov_b32 v252, 0x10000\n"
    "ds_add_u32 v254, v252 offset:61244\n"
    ".LkL_80_%=:\n"
    ".Lkstart_81_%=:\n"
    "ds_read_b128 v[192:195], v242\n"
    "ds_read_b64 v[196:197], v243 offset:32\n"
    "s_waitcnt lgkmcnt(0)\n"
    "s_waitcnt vmcnt(6)\n"
    "v_mfma_f32_32x32x16_bf16 v[84:99], v[128:131], v[192:195], 0\n"
    "v_mfma_f32_32x32x8_bf16 v[84:99], v[132:133], v[196:197], v[84:99]\n"
    "s_add_u32 s38, s90, 5376\n"
    "buffer_load_dwordx4 v[0:3], v247, s[12:15], s38 offen sc1\n"
    "buffer_load_dwordx2 v[4:5], v156, s[12:15], s38 offen sc1\n"
    "buffer_load_dwordx4 v[6:9], v157, s[12:15], s38 offen sc1\n"
    "buffer_load_dwordx4 v[10:13], v157, s[12:15], s38 offen offset:672 sc1\n"
    "s_add_u32 s38, s90, 8064\n"
    "buffer_load_dwordx4 v[14:17], v247, s[12:15], s38 offen sc1\n"
    "buffer_load_dwordx2 v[18:19], v156, s[12:15], s38 offen sc1\n"
    "buffer_load_dwordx4 v[20:23], v157, s[12:15], s38 offen sc1\n"
    "buffer_load_dwordx4 v[24:27], v157, s[12:15], s38 offen offset:672 sc1\n"
    "s_nop 1\n"
    "v_exp_f32 v84, v84\n"
    "v_exp_f32 v85, v85\n"
    "s_add_u32 s38, s90, 10752\n"
    "buffer_load_dwordx4 v[28:31], v247, s[12:15], s38 offen sc1\n"
    "buffer_load_dwordx2 v[32:33], v156, s[12:15], s38 offen sc1\n"
    "buffer_load_dwordx4 v[34:37], v157, s[12:15], s38 offen sc1\n"
    "buffer_load_dwordx4 v[38:41], v157, s[12:15], s38 offen offset:672 sc1\n"
    "v_exp_f32 v86, v86\n"
    "v_cvt_pk_bf16_f32 v116, v84, v85\n"
    "v_exp_f32 v87, v87\n"
    "v_exp_f32 v88, v88\n"
    "s_waitcnt vmcnt(14)\n"
    "v_mfma_f32_32x32x16_bf16 v[100:115], v[142:145], v[192:195], 0\n"
    "v_cvt_pk_bf16_f32 v117, v86, v87\n"
    "v_exp_f32 v89, v89\n"
    "v_exp_f32 v90, v90\n"
    "v_cvt_pk_bf16_f32 v118, v88, v89\n"
    "v_exp_f32 v91, v91\n"
    "v_mfma_f32_32x32x8_bf16 v[100:115], v[146:147], v[196:197], v[100:115]\n"
    "v_exp_f32 v92, v92\n"
    "v_cvt_pk_bf16_f32 v119, v90, v91\n"
    "v_exp_f32 v93, v93\n"
    "v_exp_f32 v94, v94\n"
    "v_mfma_f32_32x32x16_bf16 v[160:175], v[134:137], v[116:119], 0\n"
    "v_cvt_pk_bf16_f32 v120, v92, v93\n"
    "v_exp_f32 v95, v95\n"
    "v_exp_f32 v96, v96\n"
    "v_cvt_pk_bf16_f32 v121, v94, v95\n"
    "v_exp_f32 v97, v97\n"
    "v_exp_f32 v98, v98\n"
    "v_exp_f32 v99, v99\n"
    "v_cvt_pk_bf16_f32 v122, v96, v97\n"
    "v_cvt_pk_bf16_f32 v123, v98, v99\n"
    "s_cmp_eq_u32 s95, 1\n"
    "s_cbranch_scc1 .LkL_83_%=\n"
    "s_cmp_ge_u32 s24, 4\n"
    "s_cbranch_scc1 .LkL_84_%=\n"
    "s_and_b32 s39, s24, 1\n"
    "s_mul_i32 s39, s39, 6144\n"
    "s_lshr_b32 s40, s24, 1\n"
    "s_mul_i32 s40, s40, 96\n"
    "s_add_u32 s39, s39, s40\n"
    "v_sub_u32 v205, v242, v243\n"
    "v_lshl_add_u32 v205, v205, 2, v242\n"
    "v_add_u32 v205, s39, v205\n"
    "ds_read_b128 v[208:211], v205\n"
    "ds_read_b128 v[212:215], v205 offset:16\n"
    "ds_read_b64 v[216:217], v205 offset:32\n"
    "v_mov_b32 v204, 0\n"
    "s_waitcnt lgkmcnt(0)\n"
    "v_dot2_f32_bf16 v204, v208, v208, v204\n"
    "v_dot2_f32_bf16 v204, v209, v209, v204\n"
    "v_dot2_f32_bf16 v204, v210, v210, v204\n"
    "v_dot2_f32_bf16 v204, v211, v211, v204\n"
    "v_dot2_f32_bf16 v204, v212, v212, v204\n"
    "v_dot2_f32_bf16 v204, v213, v213, v204\n"
    "v_dot2_f32_bf16 v204, v214, v214, v204\n"
    "v_dot2_f32_bf16 v204, v215, v215, v204\n"
    "v_dot2_f32_bf16 v204, v216, v216, v204\n"
    "v_dot2_f32_bf16 v204, v217, v217, v204\n"
    "s_nop 2\n"
    "v_max_u32_dpp v204, v204, v204 row_ror:8 row_mask:0xf bank_mask:0xf\n"
    "s_nop 1\n"
    "v_max_u32_dpp v204, v204, v204 row_ror:4 row_mask:0xf bank_mask:0xf\n"
    "s_nop 1\n"
    "v_max_u32_dpp v204, v204, v204 row_ror:2 row_mask:0xf bank_mask:0xf\n"
    "s_nop 1\n"
    "v_max_u32_dpp v204, v204, v204 row_ror:1 row_mask:0xf bank_mask:0xf\n"
    "s_nop 1\n"
    "v_mov_b32 v205, v204\n"
    "s_nop 1\n"
    "v_permlane16_swap_b32 v204, v205\n"
    "v_max_u32 v204, v204, v205\n"
    "v_mov_b32 v205, v204\n"
    "s_nop 1\n"
    "v_permlane32_swap_b32 v204, v205\n"
    "v_max_u32 v204, v204, v205\n"
    "s_lshl_b32 s39, s24, 2\n"
    "v_add_u32 v205, s39, v254\n"
    "ds_write_b32 v205, v204 offset:96\n"
    ".LkL_84_%=:\n"
    "v_lshlrev_b32 v204, 16, v220\n"
    "s_nop 1\n"
    "v_max_f32_dpp v204, v204, v204 row_ror:8 row_mask:0xf bank_mask:0xf\n"
    "s_nop 1\n"
    "v_max_f32_dpp v204, v204, v204 row_ror:4 row_mask:0xf bank_mask:0xf\n"
    "s_nop 1\n"
    "v_mov_b32 v205, v204\n"
    "s_nop 1\n"
    "v_permlane16_swap_b32 v204, v205\n"
    "v_max_f32 v204, v204, v205\n"
    "v_mov_b32 v205, v204\n"
    "s_nop 1\n"
    "v_permlane32_swap_b32 v204, v205\n"
    "v_max_f32 v204, v204, v205\n"
    "s_nop 1\n"
    "v_add_f32_dpp v204, v204, v204 quad_perm:[1,0,3,2] row_mask:0xf bank_mask:0xf\n"
    "s_lshl_b32 s39, s24, 2\n"
    "v_add_u32 v205, s39, v254\n"
    "ds_write_b32 v205, v204 offset:64\n"
    "s_waitcnt lgkmcnt(0)\n"
    "v_mov_b32 v252, 0x10000\n"
    "ds_add_u32 v254, v252 offset:61436\n"
    ".LkL_83_%=:\n"
    "v_exp_f32 v100, v100\n"
    "v_exp_f32 v101, v101\n"
    "v_mfma_f32_32x32x16_bf16 v[160:175], v[138:141], v[120:123], v[160:175]\n"
    "s_add_u32 s38, s90, 13440\n"
    "buffer_load_dwordx4 v[42:45], v247, s[12:15], s38 offen sc1\n"
    "buffer_load_dwordx2 v[46:47], v156, s[12:15], s38 offen sc1\n"
    "buffer_load_dwordx4 v[48:51], v157, s[12:15], s38 offen sc1\n"
    "buffer_load_dwordx4 v[52:55], v157, s[12:15], s38 offen offset:672 sc1\n"
    "v_exp_f32 v102, v102\n"
    "v_cvt_pk_bf16_f32 v116, v100, v101\n"
    "v_exp_f32 v103, v103\n"
    "v_exp_f32 v104, v104\n"
    "s_waitcnt vmcnt(14)\n"
    "v_mfma_f32_32x32x16_bf16 v[84:99], v[0:3], v[192:195], 0\n"
    "v_cvt_pk_bf16_f32 v117, v102, v103\n"
    "v_exp_f32 v105, v105\n"
    "v_exp_f32 v106, v106\n"
    "v_cvt_pk_bf16_f32 v118, v104, v105\n"
    "v_exp_f32 v107, v107\n"
    "v_mfma_f32_32x32x8_bf16 v[84:99], v[4:5], v[196:197], v[84:99]\n"
    "v_exp_f32 v108, v108\n"
    "v_cvt_pk_bf16_f32 v119, v106, v107\n"
    "v_exp_f32 v109, v109\n"
    "v_exp_f32 v110, v110\n"
    "v_mfma_f32_32x32x16_bf16 v[160:175], v[148:151], v[116:119], v[160:175]\n"
    "v_cvt_pk_bf16_f32 v120, v108, v109\n"
    "v_exp_f32 v111, v111\n"
    "v_exp_f32 v112, v112\n"
    "v_cvt_pk_bf16_f32 v121, v110, v111\n"
    "v_exp_f32 v113, v113\n"
    "v_exp_f32 v114, v114\n"
    "v_exp_f32 v115, v115\n"
    "v_cvt_pk_bf16_f32 v122, v112, v113\n"
    "v_cvt_pk_bf16_f32 v123, v114, v115\n"
    "v_exp_f32 v84, v84\n"
    "v_exp_f32 v85, v85\n"
    "v_mfma_f32_32x32x16_bf16 v[160:175], v[152:155], v[120:123], v[160:175]\n"
    "s_add_u32 s38, s90, 16128\n"
    "buffer_load_dwordx4 v[56:59], v247, s[12:15], s38 offen sc1\n"
    "buffer_load_dwordx2 v[60:61], v156, s[12:15], s38 offen sc1\n"
    "buffer_load_dwordx4 v[62:65], v157, s[12:15], s38 offen sc1\n"
    "buffer_load_dwordx4 v[66:69], v157, s[12:15], s38 offen offset:672 sc1\n"
    "v_exp_f32 v86, v86\n"
    "v_cvt_pk_bf16_f32 v116, v84, v85\n"
    "v_exp_f32 v87, v87\n"
    "v_exp_f32 v88, v88\n"
    "s_waitcnt vmcnt(14)\n"
    "v_mfma_f32_32x32x16_bf16 v[100:115], v[14:17], v[192:195], 0\n"
    "v_cvt_pk_bf16_f32 v117, v86, v87\n"
    "v_exp_f32 v89, v89\n"
    "v_exp_f32 v90, v90\n"
    "v_cvt_pk_bf16_f32 v118, v88, v89\n"
    "v_exp_f32 v91, v91\n"
    "v_mfma_f32_32x32x8_bf16 v[100:115], v[18:19], v[196:197], v[100:115]\n"
    "v_exp_f32 v92, v92\n"
    "v_cvt_pk_bf16_f32 v119, v90, v91\n"
    "v_exp_f32 v93, v93\n"
    "v_exp_f32 v94, v94\n"
    "v_mfma_f32_32x32x16_bf16 v[160:175], v[6:9], v[116:119], v[160:175]\n"
    "v_cvt_pk_bf16_f32 v120, v92, v93\n"
    "v_exp_f32 v95, v95\n"
    "v_exp_f32 v96, v96\n"
    "v_cvt_pk_bf16_f32 v121, v94, v95\n"
    "v_exp_f32 v97, v97\n"
    "v_exp_f32 v98, v98\n"
    "v_exp_f32 v99, v99\n"
    "v_cvt_pk_bf16_f32 v122, v96, v97\n"
    "v_cvt_pk_bf16_f32 v123, v98, v99\n"
    "v_lshrrev_b32 v204, 4, v247\n"
    "v_and_b32 v205, 31, v204\n"
    "v_lshrrev_b32 v206, 5, v204\n"
    "s_mul_i32 s39, s24, 3072\n"
    "s_add_u32 s39, s39, 112640\n"
    "v_lshlrev_b32 v207, 7, v206\n"
    "v_xor_b32 v244, v207, v247\n"
    "v_add_u32 v244, s39, v244\n"
    "v_and_b32 v207, 7, v204\n"
    "v_lshrrev_b32 v208, 3, v204\n"
    "v_min_u32 v209, 5, v208\n"
    "s_lshr_b32 s39, s24, 2\n"
    "s_mul_i32 s39, s39, 24576\n"
    "s_and_b32 s40, s24, 3\n"
    "s_lshl_b32 s40, s40, 3\n"
    "s_add_u32 s39, s39, 112640\n"
    "v_and_b32 v210, 1, v209\n"
    "v_lshlrev_b32 v210, 3, v210\n"
    "v_add_u32 v246, s40, v207\n"
    "v_xor_b32 v246, v246, v210\n"
    "v_lshlrev_b32 v246, 4, v246\n"
    "v_lshl_add_u32 v246, v209, 9, v246\n"
    "v_add_u32 v246, s39, v246\n"
    "s_mul_i32 s39, s26, 3\n"
    "v_lshrrev_b32 v209, 1, v209\n"
    "v_add_u32 v209, s39, v209\n"
    "v_and_b32 v210, 3, v209\n"
    "v_lshrrev_b32 v209, 2, v209\n"
    "s_and_b32 s40, s24, 1\n"
    "s_lshl_b32 s40, s40, 3\n"
    "v_add_u32 v207, s40, v207\n"
    "v_lshl_add_u32 v207, v210, 4, v207\n"
    "v_lshlrev_b32 v207, 4, v207\n"
    "v_lshl_add_u32 v207, v209, 10, v207\n"
    "v_xor_b32 v210, v210, v208\n"
    "v_and_b32 v210, 1, v210\n"
    "v_lshl_add_u32 v207, v210, 3, v207\n"
    "v_mov_b32 v209, 2147483392\n"
    "v_cmp_gt_u32 vcc, 6, v208\n"
    "v_cmp_eq_u32 s[96:97], 5, v208\n"
    "s_nop 0\n"
    "v_cndmask_b32 v241, v209, v207, vcc\n"
    "s_lshl_b32 s92, s25, 4\n"
    "s_lshr_b32 s39, s24, 1\n"
    "s_add_u32 s92, s92, s39\n"
    "s_mul_i32 s92, s92, 6144\n"
    "s_add_u32 s92, s92, 6881280\n"
    "s_lshl_b32 s93, s25, 5\n"
    "s_add_u32 s93, s93, s26\n"
    "s_lshl_b32 s93, s93, 3\n"
    "s_add_u32 s93, s93, s24\n"
    "s_lshl_b32 s93, s93, 3\n"
    "s_add_u32 s93, s93, 10027008\n"
    "s_cmp_ge_u32 s24, 3\n"
    "s_cselect_b32 s39, 1, 0\n"
    "s_cmp_ge_u32 s24, 6\n"
    "s_cselect_b32 s39, 2, s39\n"
    "s_mul_i32 s40, s39, 3\n"
    "s_sub_u32 s40, s24, s40\n"
    "s_lshl_b32 s41, s31, 1\n"
    "s_cmp_eq_u32 s39, 2\n"
    "s_cselect_b32 s42, s41, 0\n"
    "s_max_u32 s40, s40, s42\n"
    "s_add_u32 s41, s41, s39\n"
    "s_cmp_eq_u32 s39, 2\n"
    "s_cselect_b32 s41, 4, s41\n"
    "s_add_u32 s41, s41, s83\n"
    "s_mul_i32 s43, s39, 17664\n"
    "s_add_u32 s43, s43, 512\n"
    "s_movk_i32 s42, 2160\n"
    "s_cmp_eq_u32 s40, 2\n"
    "s_cselect_b32 s42, 112, s42\n"
    "s_movk_i32 s44, 4416\n"
    "s_cmp_eq_u32 s39, 2\n"
    "s_cselect_b32 s42, 112, s42\n"
    "s_cselect_b32 s44, 2368, s44\n"
    "s_add_u32 s42, s42, s43\n"
    "v_and_b32 v207, 3, v205\n"
    "v_mul_u32_u24 v250, s44, v207\n"
    "v_lshrrev_b32 v207, 2, v205\n"
    "v_sub_u32 v207, v206, v207\n"
    "v_lshlrev_b32 v207, 4, v207\n"
    "v_add3_u32 v250, v250, v207, s42\n"
    "s_mul_i32 s39, s39, 13568\n"
    "s_add_u32 s39, s39, 61440\n"
    "s_cmp_eq_u32 s40, 1\n"
    "s_cselect_b32 s42, 2304, 0\n"
    "s_add_u32 s42, s42, s39\n"
    "v_and_b32 v207, 1, v205\n"
    "v_mul_u32_u24 v251, 6784, v207\n"
    "v_lshrrev_b32 v208, 1, v205\n"
    "v_mul_u32_u24 v209, 144, v208\n"
    "v_lshl_add_u32 v209, v206, 4, v209\n"
    "v_add3_u32 v251, v251, v209, s42\n"
    "v_and_b32 v209, 7, v208\n"
    "v_xor_b32 v209, v209, v206\n"
    "v_lshlrev_b32 v240, 7, v208\n"
    "v_lshl_add_u32 v240, v209, 4, v240\n"
    "v_xor_b32 v207, 1, v207\n"
    "v_lshl_add_u32 v240, v207, 3, v240\n"
    "s_lshl_b32 s3, s41, 2\n"
    "s_add_u32 s3, s3, s40\n"
    "s_lshl_b32 s3, s3, 3\n"
    "s_add_u32 s3, s3, 278528\n"
    "s_lshl_b32 s42, s41, 12\n"
    "s_lshl_b32 s39, s40, 11\n"
    "s_add_u32 s42, s42, s39\n"
    "s_add_u32 s42, s42, 2949120\n"
    "s_lshl_b32 s41, s41, 11\n"
    "s_add_u32 s41, s41, 5570560\n"
    "s_cmp_eq_u32 s40, 2\n"
    "s_cselect_b32 s91, s41, s42\n"
    "s_add_u32 s39, s24, s31\n"
    "s_cmp_eq_u32 s39, 8\n"
    "s_cselect_b32 s91, 0x7f000000, s91\n"
    "s_cselect_b32 s3, 0x7f000000, s3\n"
    "s_cselect_b64 vcc, -1, 0\n"
    "v_mov_b32 v207, 61440\n"
    "v_cndmask_b32 v250, v250, v207, vcc\n"
    "v_cndmask_b32 v251, v251, v207, vcc\n"
    "s_mul_i32 s39, s26, 160\n"
    "s_sub_u32 s40, 819200, s39\n"
    "s_add_u32 s20, s72, s39\n"
    "s_addc_u32 s21, s73, 0\n"
    "s_and_b32 s21, s21, 0xffff\n"
    "s_mov_b32 s22, s40\n"
    "s_mov_b32 s23, 0x20000\n"
    "v_exp_f32 v100, v100\n"
    "v_exp_f32 v101, v101\n"
    "v_mfma_f32_32x32x16_bf16 v[160:175], v[10:13], v[120:123], v[160:175]\n"
    "s_add_u32 s38, s90, 18816\n"
    "buffer_load_dwordx4 v[70:73], v247, s[12:15], s38 offen sc1\n"
    "buffer_load_dwordx2 v[74:75], v156, s[12:15], s38 offen sc1\n"
    "buffer_load_dwordx4 v[76:79], v157, s[12:15], s38 offen sc1\n"
    "buffer_load_dwordx4 v[80:83], v157, s[12:15], s38 offen offset:672 sc1\n"
    "v_exp_f32 v102, v102\n"
    "v_cvt_pk_bf16_f32 v116, v100, v101\n"
    "v_exp_f32 v103, v103\n"
    "v_exp_f32 v104, v104\n"
    "s_waitcnt vmcnt(14)\n"
    "v_mfma_f32_32x32x16_bf16 v[84:99], v[28:31], v[192:195], 0\n"
    "v_cvt_pk_bf16_f32 v117, v102, v103\n"
    "v_exp_f32 v105, v105\n"
    "v_exp_f32 v106, v106\n"
    "v_cvt_pk_bf16_f32 v118, v104, v105\n"
    "v_exp_f32 v107, v107\n"
    "v_mfma_f32_32x32x8_bf16 v[84:99], v[32:33], v[196:197], v[84:99]\n"
    "v_exp_f32 v108, v108\n"
    "v_cvt_pk_bf16_f32 v119, v106, v107\n"
    "v_exp_f32 v109, v109\n"
    "v_exp_f32 v110, v110\n"
    "v_mfma_f32_32x32x16_bf16 v[160:175], v[20:23], v[116:119], v[160:175]\n"
    "v_cvt_pk_bf16_f32 v120, v108, v109\n"
    "v_exp_f32 v111, v111\n"
    "v_exp_f32 v112, v112\n"
    "v_cvt_pk_bf16_f32 v121, v110, v111\n"
    "v_exp_f32 v113, v113\n"
    "v_exp_f32 v114, v114\n"
    "v_exp_f32 v115, v115\n"
    "v_cvt_pk_bf16_f32 v122, v112, v113\n"
    "v_cvt_pk_bf16_f32 v123, v114, v115\n"
    "v_exp_f32 v84, v84\n"
    "v_exp_f32 v85, v85\n"
    "v_mfma_f32_32x32x16_bf16 v[160:175], v[24:27], v[120:123], v[160:175]\n"
    "v_exp_f32 v86, v86\n"
    "v_cvt_pk_bf16_f32 v116, v84, v85\n"
    "v_exp_f32 v87, v87\n"
    "v_exp_f32 v88, v88\n"
    "s_waitcnt vmcnt(10)\n"
    "v_mfma_f32_32x32x16_bf16 v[100:115], v[42:45], v[192:195], 0\n"
    "v_cvt_pk_bf16_f32 v117, v86, v87\n"
    "v_exp_f32 v89, v89\n"
    "v_exp_f32 v90, v90\n"
    "v_cvt_pk_bf16_f32 v118, v88, v89\n"
    "v_exp_f32 v91, v91\n"
    "v_mfma_f32_32x32x8_bf16 v[100:115], v[46:47], v[196:197], v[100:115]\n"
    "v_exp_f32 v92, v92\n"
    "v_cvt_pk_bf16_f32 v119, v90, v91\n"
    "v_exp_f32 v93, v93\n"
    "v_exp_f32 v94, v94\n"
    "v_mfma_f32_32x32x16_bf16 v[160:175], v[34:37], v[116:119], v[160:175]\n"
    "v_cvt_pk_bf16_f32 v120, v92, v93\n"
    "v_exp_f32 v95, v95\n"
    "v_exp_f32 v96, v96\n"
    "v_cvt_pk_bf16_f32 v121, v94, v95\n"
    "v_exp_f32 v97, v97\n"
    "v_exp_f32 v98, v98\n"
    "v_exp_f32 v99, v99\n"
    "v_cvt_pk_bf16_f32 v122, v96, v97\n"
    "v_cvt_pk_bf16_f32 v123, v98, v99\n"
    "v_exp_f32 v100, v100\n"
    "v_exp_f32 v101, v101\n"
    "v_mfma_f32_32x32x16_bf16 v[160:175], v[38:41], v[120:123], v[160:175]\n"
    "ds_read_b128 v[198:201], v242 offset:6144\n"
    "ds_read_b64 v[202:203], v243 offset:6176\n"
    "v_exp_f32 v102, v102\n"
    "v_cvt_pk_bf16_f32 v116, v100, v101\n"
    "v_exp_f32 v103, v103\n"
    "v_exp_f32 v104, v104\n"
    "s_waitcnt vmcnt(6)\n"
    "v_mfma_f32_32x32x16_bf16 v[84:99], v[56:59], v[192:195], 0\n"
    "v_cvt_pk_bf16_f32 v117, v102, v103\n"
    "v_exp_f32 v105, v105\n"
    "v_exp_f32 v106, v106\n"
    "v_cvt_pk_bf16_f32 v118, v104, v105\n"
    "v_exp_f32 v107, v107\n"
    "v_mfma_f32_32x32x8_bf16 v[84:99], v[60:61], v[196:197], v[84:99]\n"
    "v_exp_f32 v108, v108\n"
    "v_cvt_pk_bf16_f32 v119, v106, v107\n"
    "v_exp_f32 v109, v109\n"
    "v_exp_f32 v110, v110\n"
    "v_mfma_f32_32x32x16_bf16 v[160:175], v[48:51], v[116:119], v[160:175]\n"
    "v_cvt_pk_bf16_f32 v120, v108, v109\n"
    "v_exp_f32 v111, v111\n"
    "v_exp_f32 v112, v112\n"
    "v_cvt_pk_bf16_f32 v121, v110, v111\n"
    "v_exp_f32 v113, v113\n"
    "v_exp_f32 v114, v114\n"
    "v_exp_f32 v115, v115\n"
    "v_cvt_pk_bf16_f32 v122, v112, v113\n"
    "v_cvt_pk_bf16_f32 v123, v114, v115\n"
    "v_exp_f32 v84, v84\n"
    "v_exp_f32 v85, v85\n"
    "v_mfma_f32_32x32x16_bf16 v[160:175], v[52:55], v[120:123], v[160:175]\n"
    "v_exp_f32 v86, v86\n"
    "v_cvt_pk_bf16_f32 v116, v84, v85\n"
    "v_exp_f32 v87, v87\n"
    "v_exp_f32 v88, v88\n"
    "s_waitcnt vmcnt(2)\n"
    "v_mfma_f32_32x32x16_bf16 v[100:115], v[70:73], v[192:195], 0\n"
    "v_cvt_pk_bf16_f32 v117, v86, v87\n"
    "v_exp_f32 v89, v89\n"
    "v_exp_f32 v90, v90\n"
    "v_cvt_pk_bf16_f32 v118, v88, v89\n"
    "v_exp_f32 v91, v91\n"
    "v_mfma_f32_32x32x8_bf16 v[100:115], v[74:75], v[196:197], v[100:115]\n"
    "v_exp_f32 v92, v92\n"
    "v_cvt_pk_bf16_f32 v119, v90, v91\n"
    "v_exp_f32 v93, v93\n"
    "v_exp_f32 v94, v94\n"
    "v_mfma_f32_32x32x16_bf16 v[160:175], v[62:65], v[116:119], v[160:175]\n"
    "v_cvt_pk_bf16_f32 v120, v92, v93\n"
    "v_exp_f32 v95, v95\n"
    "v_exp_f32 v96, v96\n"
    "v_cvt_pk_bf16_f32 v121, v94, v95\n"
    "v_exp_f32 v97, v97\n"
    "v_exp_f32 v98, v98\n"
    "v_exp_f32 v99, v99\n"
    "v_cvt_pk_bf16_f32 v122, v96, v97\n"
    "v_cvt_pk_bf16_f32 v123, v98, v99\n"
    "ds_read_b32 v249, v245 offset:61436\n"
    "v_exp_f32 v100, v100\n"
    "v_exp_f32 v101, v101\n"
    "v_mfma_f32_32x32x16_bf16 v[160:175], v[66:69], v[120:123], v[160:175]\n"
    "v_exp_f32 v102, v102\n"
    "v_cvt_pk_bf16_f32 v116, v100, v101\n"
    "v_exp_f32 v103, v103\n"
    "v_exp_f32 v104, v104\n"
    "s_waitcnt lgkmcnt(1)\n"
    "v_mfma_f32_32x32x16_bf16 v[84:99], v[128:131], v[198:201], 0\n"
    "v_cvt_pk_bf16_f32 v117, v102, v103\n"
    "v_exp_f32 v105, v105\n"
    "v_exp_f32 v106, v106\n"
    "v_cvt_pk_bf16_f32 v118, v104, v105\n"
    "v_exp_f32 v107, v107\n"
    "v_mfma_f32_32x32x8_bf16 v[84:99], v[132:133], v[202:203], v[84:99]\n"
    "v_exp_f32 v108, v108\n"
    "v_cvt_pk_bf16_f32 v119, v106, v107\n"
    "v_exp_f32 v109, v109\n"
    "v_exp_f32 v110, v110\n"
    "s_waitcnt vmcnt(0)\n"
    "v_mfma_f32_32x32x16_bf16 v[160:175], v[76:79], v[116:119], v[160:175]\n"
    "v_cvt_pk_bf16_f32 v120, v108, v109\n"
    "v_exp_f32 v111, v111\n"
    "v_exp_f32 v112, v112\n"
    "v_cvt_pk_bf16_f32 v121, v110, v111\n"
    "v_exp_f32 v113, v113\n"
    "v_exp_f32 v114, v114\n"
    "v_exp_f32 v115, v115\n"
    "v_cvt_pk_bf16_f32 v122, v112, v113\n"
    "v_cvt_pk_bf16_f32 v123, v114, v115\n"
    "s_waitcnt lgkmcnt(0)\n"
    ".Lkc_85_%=:\n"
    "v_readfirstlane_b32 s47, v249\n"
    "s_cmp_ge_u32 s47, 524288\n"
    "s_cbranch_scc1 .Lkd_86_%=\n"
    "ds_read_b32 v249, v245 offset:61436\n"
    "s_sleep 1\n"
    "s_waitcnt lgkmcnt(0)\n"
    "s_sub_u32 s80, s80, 1\n"
    "s_cbranch_scc0 .Lkc_85_%=\n"
    "s_mov_b32 s80, 0\n"
    ".Lkd_86_%=:\n"
    "ds_read_b128 v[204:207], v245 offset:64\n"
    "ds_read_b128 v[208:211], v245 offset:80\n"
    "ds_read_b128 v[212:215], v245 offset:96\n"
    "ds_read_b32 v249, v245 offset:61244\n"
    "v_exp_f32 v84, v84\n"
    "v_exp_f32 v85, v85\n"
    "v_mfma_f32_32x32x16_bf16 v[160:175], v[80:83], v[120:123], v[160:175]\n"
    "v_exp_f32 v86, v86\n"
    "v_cvt_pk_bf16_f32 v116, v84, v85\n"
    "v_exp_f32 v87, v87\n"
    "v_exp_f32 v88, v88\n"
    "v_mfma_f32_32x32x16_bf16 v[100:115], v[142:145], v[198:201], 0\n"
    "v_cvt_pk_bf16_f32 v117, v86, v87\n"
    "v_exp_f32 v89, v89\n"
    "v_exp_f32 v90, v90\n"
    "v_cvt_pk_bf16_f32 v118, v88, v89\n"
    "v_exp_f32 v91, v91\n"
    "v_mfma_f32_32x32x8_bf16 v[100:115], v[146:147], v[202:203], v[100:115]\n"
    "v_exp_f32 v92, v92\n"
    "v_cvt_pk_bf16_f32 v119, v90, v91\n"
    "v_exp_f32 v93, v93\n"
    "v_exp_f32 v94, v94\n"
    "v_mfma_f32_32x32x16_bf16 v[176:191], v[134:137], v[116:119], 0\n"
    "v_cvt_pk_bf16_f32 v120, v92, v93\n"
    "v_exp_f32 v95, v95\n"
    "v_exp_f32 v96, v96\n"
    "v_cvt_pk_bf16_f32 v121, v94, v95\n"
    "v_exp_f32 v97, v97\n"
    "v_exp_f32 v98, v98\n"
    "v_exp_f32 v99, v99\n"
    "v_cvt_pk_bf16_f32 v122, v96, v97\n"
    "v_cvt_pk_bf16_f32 v123, v98, v99\n"
    "s_setprio 1\n"
    "s_waitcnt lgkmcnt(0)\n"
    ".Lkc_87_%=:\n"
    "v_readfirstlane_b32 s47, v249\n"
    "s_cmp_ge_u32 s47, 262144\n"
    "s_cbranch_scc1 .Lkd_88_%=\n"
    "ds_read_b32 v249, v245 offset:61244\n"
    "s_sleep 1\n"
    "s_waitcnt lgkmcnt(0)\n"
    "s_sub_u32 s80, s80, 1\n"
    "s_cbranch_scc0 .Lkc_87_%=\n"
    "s_mov_b32 s80, 0\n"
    ".Lkd_88_%=:\n"
    "ds_read_b128 v[220:223], v250 offset:2016\n"
    "ds_read_b128 v[224:227], v251 offset:2256\n"
    "s_cmp_eq_u32 s95, 1\n"
    "s_cbranch_scc1 .LkL_89_%=\n"
    "s_mov_b32 s95, 1\n"
    "v_max3_f32 v204, v204, v205, v206\n"
    "v_max3_f32 v208, v208, v209, v210\n"
    "v_max3_u32 v212, v212, v213, v214\n"
    "v_max3_f32 v204, v204, v207, v211\n"
    "v_max_u32 v212, v212, v215\n"
    "v_max_f32 v204, v204, v208\n"
    "v_mul_f32 v204, v204, v212\n"
    "v_cmp_ge_f32 vcc, 0x45800000, v204\n"
    "s_nop 1\n"
    "s_cmp_eq_u64 vcc, -1\n"
    "s_cbranch_scc0 .Lkfall_82_%=\n"
    ".LkL_89_%=:\n"
    "v_exp_f32 v100, v100\n"
    "v_exp_f32 v101, v101\n"
    "v_mfma_f32_32x32x16_bf16 v[176:191], v[138:141], v[120:123], v[176:191]\n"
    "v_exp_f32 v102, v102\n"
    "v_cvt_pk_bf16_f32 v116, v100, v101\n"
    "v_exp_f32 v103, v103\n"
    "v_exp_f32 v104, v104\n"
    "v_mfma_f32_32x32x16_bf16 v[84:99], v[0:3], v[198:201], 0\n"
    "v_cvt_pk_bf16_f32 v117, v102, v103\n"
    "v_exp_f32 v105, v105\n"
    "v_exp_f32 v106, v106\n"
    "v_cvt_pk_bf16_f32 v118, v104, v105\n"
    "v_exp_f32 v107, v107\n"
    "v_mfma_f32_32x32x8_bf16 v[84:99], v[4:5], v[202:203], v[84:99]\n"
    "v_exp_f32 v108, v108\n"
    "v_cvt_pk_bf16_f32 v119, v106, v107\n"
    "v_exp_f32 v109, v109\n"
    "v_exp_f32 v110, v110\n"
    "v_mfma_f32_32x32x16_bf16 v[176:191], v[148:151], v[116:119], v[176:191]\n"
    "v_cvt_pk_bf16_f32 v120, v108, v109\n"
    "v_exp_f32 v111, v111\n"
    "v_exp_f32 v112, v112\n"
    "v_cvt_pk_bf16_f32 v121, v110, v111\n"
    "v_exp_f32 v113, v113\n"
    "v_exp_f32 v114, v114\n"
    "v_exp_f32 v115, v115\n"
    "v_cvt_pk_bf16_f32 v122, v112, v113\n"
    "v_cvt_pk_bf16_f32 v123, v114, v115\n"
    "v_exp_f32 v84, v84\n"
    "v_exp_f32 v85, v85\n"
    "v_mfma_f32_32x32x16_bf16 v[176:191], v[152:155], v[120:123], v[176:191]\n"
    "v_exp_f32 v86, v86\n"
    "v_cvt_pk_bf16_f32 v116, v84, v85\n"
    "v_exp_f32 v87, v87\n"
    "v_exp_f32 v88, v88\n"
    "v_mfma_f32_32x32x16_bf16 v[100:115], v[14:17], v[198:201], 0\n"
    "ds_read_b128 v[228:231], v250 offset:1984\n"
    "ds_read_b128 v[232:235], v251 offset:2224\n"
    "v_cvt_pk_bf16_f32 v117, v86, v87\n"
    "v_exp_f32 v89, v89\n"
    "v_exp_f32 v90, v90\n"
    "v_cvt_pk_bf16_f32 v118, v88, v89\n"
    "v_exp_f32 v91, v91\n"
    "v_mfma_f32_32x32x8_bf16 v[100:115], v[18:19], v[202:203], v[100:115]\n"
    "v_exp_f32 v92, v92\n"
    "v_cvt_pk_bf16_f32 v119, v90, v91\n"
    "v_exp_f32 v93, v93\n"
    "v_exp_f32 v94, v94\n"
    "v_mfma_f32_32x32x16_bf16 v[176:191], v[6:9], v[116:119], v[176:191]\n"
    "v_cvt_pk_bf16_f32 v120, v92, v93\n"
    "v_exp_f32 v95, v95\n"
    "v_exp_f32 v96, v96\n"
    "v_cvt_pk_bf16_f32 v121, v94, v95\n"
    "v_exp_f32 v97, v97\n"
    "s_waitcnt lgkmcnt(2)\n"
    "v_mfma_f32_32x32x16_bf16 v[204:219], v[220:223], v[224:227], 0\n"
    "ds_read_b128 v[220:223], v250 offset:1952\n"
    "ds_read_b128 v[224:227], v251 offset:2192\n"
    "v_exp_f32 v98, v98\n"
    "v_exp_f32 v99, v99\n"
    "s_waitcnt lgkmcnt(2)\n"
    "v_mfma_f32_32x32x16_bf16 v[204:219], v[228:231], v[232:235], v[204:219]\n"
    "v_cvt_pk_bf16_f32 v122, v96, v97\n"
    "v_cvt_pk_bf16_f32 v123, v98, v99\n"
    "v_exp_f32 v100, v100\n"
    "v_exp_f32 v101, v101\n"
    "v_mfma_f32_32x32x16_bf16 v[176:191], v[10:13], v[120:123], v[176:191]\n"
    "v_exp_f32 v102, v102\n"
    "v_cvt_pk_bf16_f32 v116, v100, v101\n"
    "v_exp_f32 v103, v103\n"
    "v_exp_f32 v104, v104\n"
    "v_mfma_f32_32x32x16_bf16 v[84:99], v[28:31], v[198:201], 0\n"
    "ds_read_b128 v[228:231], v250 offset:1920\n"
    "ds_read_b128 v[232:235], v251 offset:2160\n"
    "v_cvt_pk_bf16_f32 v117, v102, v103\n"
    "v_exp_f32 v105, v105\n"
    "v_exp_f32 v106, v106\n"
    "v_cvt_pk_bf16_f32 v118, v104, v105\n"
    "v_exp_f32 v107, v107\n"
    "v_mfma_f32_32x32x8_bf16 v[84:99], v[32:33], v[202:203], v[84:99]\n"
    "v_exp_f32 v108, v108\n"
    "v_cvt_pk_bf16_f32 v119, v106, v107\n"
    "v_exp_f32 v109, v109\n"
    "v_exp_f32 v110, v110\n"
    "v_mfma_f32_32x32x16_bf16 v[176:191], v[20:23], v[116:119], v[176:191]\n"
    "v_cvt_pk_bf16_f32 v120, v108, v109\n"
    "v_exp_f32 v111, v111\n"
    "v_exp_f32 v112, v112\n"
    "v_cvt_pk_bf16_f32 v121, v110, v111\n"
    "v_exp_f32 v113, v113\n"
    "s_waitcnt lgkmcnt(2)\n"
    "v_mfma_f32_32x32x16_bf16 v[204:219], v[220:223], v[224:227], v[204:219]\n"
    "ds_read_b128 v[220:223], v250 offset:1888\n"
    "ds_read_b128 v[224:227], v251 offset:2112\n"
    "v_exp_f32 v114, v114\n"
    "v_exp_f32 v115, v115\n"
    "s_waitcnt lgkmcnt(2)\n"
    "v_mfma_f32_32x32x16_bf16 v[204:219], v[228:231], v[232:235], v[204:219]\n"
    "v_cvt_pk_bf16_f32 v122, v112, v113\n"
    "v_cvt_pk_bf16_f32 v123, v114, v115\n"
    "v_exp_f32 v84, v84\n"
    "v_exp_f32 v85, v85\n"
    "v_mfma_f32_32x32x16_bf16 v[176:191], v[24:27], v[120:123], v[176:191]\n"
    "v_exp_f32 v86, v86\n"
    "v_cvt_pk_bf16_f32 v116, v84, v85\n"
    "v_exp_f32 v87, v87\n"
    "v_exp_f32 v88, v88\n"
    "v_mfma_f32_32x32x16_bf16 v[100:115], v[42:45], v[198:201], 0\n"
    "ds_read_b128 v[228:231], v250 offset:1856\n"
    "ds_read_b128 v[232:235], v251 offset:2080\n"
    "v_cvt_pk_bf16_f32 v117, v86, v87\n"
    "v_exp_f32 v89, v89\n"
    "v_exp_f32 v90, v90\n"
    "v_cvt_pk_bf16_f32 v118, v88, v89\n"
    "v_exp_f32 v91, v91\n"
    "v_mfma_f32_32x32x8_bf16 v[100:115], v[46:47], v[202:203], v[100:115]\n"
    "v_exp_f32 v92, v92\n"
    "v_cvt_pk_bf16_f32 v119, v90, v91\n"
    "v_exp_f32 v93, v93\n"
    "ds_write_b128 v244, v[160:163] offset:0\n"
    "v_exp_f32 v94, v94\n"
    "v_mfma_f32_32x32x16_bf16 v[176:191], v[34:37], v[116:119], v[176:191]\n"
    "v_cvt_pk_bf16_f32 v120, v92, v93\n"
    "v_exp_f32 v95, v95\n"
    "v_exp_f32 v96, v96\n"
    "v_cvt_pk_bf16_f32 v121, v94, v95\n"
    "v_exp_f32 v97, v97\n"
    "s_waitcnt lgkmcnt(3)\n"
    "v_mfma_f32_32x32x16_bf16 v[204:219], v[220:223], v[224:227], v[204:219]\n"
    "ds_read_b128 v[220:223], v250 offset:1824\n"
    "ds_read_b128 v[224:227], v251 offset:2048\n"
    "v_exp_f32 v98, v98\n"
    "v_exp_f32 v99, v99\n"
    "s_waitcnt lgkmcnt(3)\n"
    "v_mfma_f32_32x32x16_bf16 v[204:219], v[228:231], v[232:235], v[204:219]\n"
    "v_cvt_pk_bf16_f32 v122, v96, v97\n"
    "v_cvt_pk_bf16_f32 v123, v98, v99\n"
    "v_exp_f32 v100, v100\n"
    "v_exp_f32 v101, v101\n"
    "v_mfma_f32_32x32x16_bf16 v[176:191], v[38:41], v[120:123], v[176:191]\n"
    "ds_read_b128 v[192:195], v242 offset:48\n"
    "ds_read_b64 v[196:197], v243 offset:80\n"
    "v_exp_f32 v102, v102\n"
    "v_cvt_pk_bf16_f32 v116, v100, v101\n"
    "v_exp_f32 v103, v103\n"
    "v_exp_f32 v104, v104\n"
    "v_mfma_f32_32x32x16_bf16 v[84:99], v[56:59], v[198:201], 0\n"
    "ds_read_b128 v[228:231], v250 offset:1792\n"
    "ds_read_b128 v[232:235], v251 offset:2016\n"
    "v_cvt_pk_bf16_f32 v117, v102, v103\n"
    "v_exp_f32 v105, v105\n"
    "v_exp_f32 v106, v106\n"
    "v_cvt_pk_bf16_f32 v118, v104, v105\n"
    "v_exp_f32 v107, v107\n"
    "v_mfma_f32_32x32x8_bf16 v[84:99], v[60:61], v[202:203], v[84:99]\n"
    "v_exp_f32 v108, v108\n"
    "v_cvt_pk_bf16_f32 v119, v106, v107\n"
    "v_exp_f32 v109, v109\n"
    "ds_write_b128 v244, v[164:167] offset:1024\n"
    "v_exp_f32 v110, v110\n"
    "v_mfma_f32_32x32x16_bf16 v[176:191], v[48:51], v[116:119], v[176:191]\n"
    "v_cvt_pk_bf16_f32 v120, v108, v109\n"
    "v_exp_f32 v111, v111\n"
    "v_exp_f32 v112, v112\n"
    "v_cvt_pk_bf16_f32 v121, v110, v111\n"
    "v_exp_f32 v113, v113\n"
    "s_waitcnt lgkmcnt(5)\n"
    "v_mfma_f32_32x32x16_bf16 v[204:219], v[220:223], v[224:227], v[204:219]\n"
    "ds_read_b128 v[220:223], v250 offset:1760\n"
    "ds_read_b128 v[224:227], v251 offset:1968\n"
    "v_exp_f32 v114, v114\n"
    "v_exp_f32 v115, v115\n"
    "s_waitcnt lgkmcnt(3)\n"
    "v_mfma_f32_32x32x16_bf16 v[204:219], v[228:231], v[232:235], v[204:219]\n"
    "v_cvt_pk_bf16_f32 v122, v112, v113\n"
    "v_cvt_pk_bf16_f32 v123, v114, v115\n"
    "v_exp_f32 v84, v84\n"
    "v_exp_f32 v85, v85\n"
    "v_mfma_f32_32x32x16_bf16 v[176:191], v[52:55], v[120:123], v[176:191]\n"
    "v_exp_f32 v86, v86\n"
    "v_cvt_pk_bf16_f32 v116, v84, v85\n"
    "v_exp_f32 v87, v87\n"
    "v_exp_f32 v88, v88\n"
    "v_mfma_f32_32x32x16_bf16 v[100:115], v[70:73], v[198:201], 0\n"
    "ds_read_b128 v[228:231], v250 offset:1728\n"
    "ds_read_b128 v[232:235], v251 offset:1936\n"
    "v_cvt_pk_bf16_f32 v117, v86, v87\n"
    "v_exp_f32 v89, v89\n"
    "v_exp_f32 v90, v90\n"
    "v_cvt_pk_bf16_f32 v118, v88, v89\n"
    "v_exp_f32 v91, v91\n"
    "v_mfma_f32_32x32x8_bf16 v[100:115], v[74:75], v[202:203], v[100:115]\n"
    "v_exp_f32 v92, v92\n"
    "v_cvt_pk_bf16_f32 v119, v90, v91\n"
    "v_exp_f32 v93, v93\n"
    "ds_write_b128 v244, v[168:171] offset:2048\n"
    "v_exp_f32 v94, v94\n"
    "v_mfma_f32_32x32x16_bf16 v[176:191], v[62:65], v[116:119], v[176:191]\n"
    "v_cvt_pk_bf16_f32 v120, v92, v93\n"
    "v_exp_f32 v95, v95\n"
    "v_exp_f32 v96, v96\n"
    "v_cvt_pk_bf16_f32 v121, v94, v95\n"
    "v_exp_f32 v97, v97\n"
    "s_waitcnt lgkmcnt(3)\n"
    "v_mfma_f32_32x32x16_bf16 v[204:219], v[220:223], v[224:227], v[204:219]\n"
    "ds_read_b128 v[220:223], v250 offset:1696\n"
    "ds_read_b128 v[224:227], v251 offset:1904\n"
    "v_exp_f32 v98, v98\n"
    "v_exp_f32 v99, v99\n"
    "s_waitcnt lgkmcnt(3)\n"
    "v_mfma_f32_32x32x16_bf16 v[204:219], v[228:231], v[232:235], v[204:219]\n"
    "v_cvt_pk_bf16_f32 v122, v96, v97\n"
    "v_cvt_pk_bf16_f32 v123, v98, v99\n"
    "v_exp_f32 v100, v100\n"
    "v_exp_f32 v101, v101\n"
    "v_mfma_f32_32x32x16_bf16 v[176:191], v[66:69], v[120:123], v[176:191]\n"
    "v_exp_f32 v102, v102\n"
    "v_cvt_pk_bf16_f32 v116, v100, v101\n"
    "v_exp_f32 v103, v103\n"
    "v_exp_f32 v104, v104\n"
    "v_mfma_f32_32x32x16_bf16 v[84:99], v[128:131], v[192:195], 0\n"
    "ds_read_b128 v[228:231], v250 offset:1664\n"
    "ds_read_b128 v[232:235], v251 offset:1872\n"
    "v_cvt_pk_bf16_f32 v117, v102, v103\n"
    "v_exp_f32 v105, v105\n"
    "v_exp_f32 v106, v106\n"
    "v_cvt_pk_bf16_f32 v118, v104, v105\n"
    "v_exp_f32 v107, v107\n"
    "v_mfma_f32_32x32x8_bf16 v[84:99], v[132:133], v[196:197], v[84:99]\n"
    "v_exp_f32 v108, v108\n"
    "v_cvt_pk_bf16_f32 v119, v106, v107\n"
    "v_exp_f32 v109, v109\n"
    "v_exp_f32 v110, v110\n"
    "v_mfma_f32_32x32x16_bf16 v[176:191], v[76:79], v[116:119], v[176:191]\n"
    "v_cvt_pk_bf16_f32 v120, v108, v109\n"
    "v_exp_f32 v111, v111\n"
    "v_exp_f32 v112, v112\n"
    "v_cvt_pk_bf16_f32 v121, v110, v111\n"
    "v_exp_f32 v113, v113\n"
    "s_waitcnt lgkmcnt(2)\n"
    "v_mfma_f32_32x32x16_bf16 v[204:219], v[220:223], v[224:227], v[204:219]\n"
    "ds_read_b128 v[220:223], v250 offset:1632\n"
    "ds_read_b128 v[224:227], v251 offset:1824\n"
    "v_exp_f32 v114, v114\n"
    "v_exp_f32 v115, v115\n"
    "s_waitcnt lgkmcnt(2)\n"
    "v_mfma_f32_32x32x16_bf16 v[204:219], v[228:231], v[232:235], v[204:219]\n"
    "v_cvt_pk_bf16_f32 v122, v112, v113\n"
    "v_cvt_pk_bf16_f32 v123, v114, v115\n"
    "s_setprio 0\n"
    "v_exp_f32 v84, v84\n"
    "v_exp_f32 v85, v85\n"
    "v_mfma_f32_32x32x16_bf16 v[176:191], v[80:83], v[120:123], v[176:191]\n"
    "v_exp_f32 v86, v86\n"
    "v_cvt_pk_bf16_f32 v116, v84, v85\n"
    "v_exp_f32 v87, v87\n"
    "v_exp_f32 v88, v88\n"
    "v_mfma_f32_32x32x16_bf16 v[100:115], v[142:145], v[192:195], 0\n"
    "ds_read_b128 v[228:231], v250 offset:1600\n"
    "ds_read_b128 v[232:235], v251 offset:1792\n"
    "v_cvt_pk_bf16_f32 v117, v86, v87\n"
    "v_exp_f32 v89, v89\n"
    "v_exp_f32 v90, v90\n"
    "v_cvt_pk_bf16_f32 v118, v88, v89\n"
    "v_exp_f32 v91, v91\n"
    "v_mfma_f32_32x32x8_bf16 v[100:115], v[146:147], v[196:197], v[100:115]\n"
    "v_exp_f32 v92, v92\n"
    "v_cvt_pk_bf16_f32 v119, v90, v91\n"
    "v_exp_f32 v93, v93\n"
    "ds_write_b128 v244, v[176:179] offset:24576\n"
    "v_exp_f32 v94, v94\n"
    "v_mfma_f32_32x32x16_bf16 v[160:175], v[134:137], v[116:119], 0\n"
    "v_cvt_pk_bf16_f32 v120, v92, v93\n"
    "v_exp_f32 v95, v95\n"
    "v_exp_f32 v96, v96\n"
    "v_cvt_pk_bf16_f32 v121, v94, v95\n"
    "v_exp_f32 v97, v97\n"
    "s_waitcnt lgkmcnt(3)\n"
    "v_mfma_f32_32x32x16_bf16 v[204:219], v[220:223], v[224:227], v[204:219]\n"
    "ds_read_b128 v[220:223], v250 offset:1568\n"
    "ds_read_b128 v[224:227], v251 offset:1760\n"
    "v_exp_f32 v98, v98\n"
    "v_exp_f32 v99, v99\n"
    "s_waitcnt lgkmcnt(3)\n"
    "v_mfma_f32_32x32x16_bf16 v[204:219], v[228:231], v[232:235], v[204:219]\n"
    "v_cvt_pk_bf16_f32 v122, v96, v97\n"
    "v_cvt_pk_bf16_f32 v123, v98, v99\n"
    "v_exp_f32 v100, v100\n"
    "v_exp_f32 v101, v101\n"
    "v_mfma_f32_32x32x16_bf16 v[160:175], v[138:141], v[120:123], v[160:175]\n"
    "v_exp_f32 v102, v102\n"
    "v_cvt_pk_bf16_f32 v116, v100, v101\n"
    "v_exp_f32 v103, v103\n"
    "v_exp_f32 v104, v104\n"
    "v_mfma_f32_32x32x16_bf16 v[84:99], v[0:3], v[192:195], 0\n"
    "ds_read_b128 v[228:231], v250 offset:1536\n"
    "ds_read_b128 v[232:235], v251 offset:1728\n"
    "v_cvt_pk_bf16_f32 v117, v102, v103\n"
    "v_exp_f32 v105, v105\n"
    "v_exp_f32 v106, v106\n"
    "v_cvt_pk_bf16_f32 v118, v104, v105\n"
    "v_exp_f32 v107, v107\n"
    "v_mfma_f32_32x32x8_bf16 v[84:99], v[4:5], v[196:197], v[84:99]\n"
    "v_exp_f32 v108, v108\n"
    "v_cvt_pk_bf16_f32 v119, v106, v107\n"
    "v_exp_f32 v109, v109\n"
    "ds_write_b128 v244, v[180:183] offset:25600\n"
    "v_exp_f32 v110, v110\n"
    "v_mfma_f32_32x32x16_bf16 v[160:175], v[148:151], v[116:119], v[160:175]\n"
    "v_cvt_pk_bf16_f32 v120, v108, v109\n"
    "v_exp_f32 v111, v111\n"
    "v_exp_f32 v112, v112\n"
    "v_cvt_pk_bf16_f32 v121, v110, v111\n"
    "v_exp_f32 v113, v113\n"
    "s_waitcnt lgkmcnt(3)\n"
    "v_mfma_f32_32x32x16_bf16 v[204:219], v[220:223], v[224:227], v[204:219]\n"
    "ds_read_b128 v[220:223], v250 offset:1504\n"
    "ds_read_b128 v[224:227], v251 offset:1680\n"
    "v_exp_f32 v114, v114\n"
    "v_exp_f32 v115, v115\n"
    "s_waitcnt lgkmcnt(3)\n"
    "v_mfma_f32_32x32x16_bf16 v[204:219], v[228:231], v[232:235], v[204:219]\n"
    "v_cvt_pk_bf16_f32 v122, v112, v113\n"
    "v_cvt_pk_bf16_f32 v123, v114, v115\n"
    "v_exp_f32 v84, v84\n"
    "v_exp_f32 v85, v85\n"
    "v_mfma_f32_32x32x16_bf16 v[160:175], v[152:155], v[120:123], v[160:175]\n"
    "v_exp_f32 v86, v86\n"
    "v_cvt_pk_bf16_f32 v116, v84, v85\n"
    "v_exp_f32 v87, v87\n"
    "v_exp_f32 v88, v88\n"
    "v_mfma_f32_32x32x16_bf16 v[100:115], v[14:17], v[192:195], 0\n"
    "ds_read_b128 v[228:231], v250 offset:1472\n"
    "ds_read_b128 v[232:235], v251 offset:1648\n"
    "v_cvt_pk_bf16_f32 v117, v86, v87\n"
    "v_exp_f32 v89, v89\n"
    "v_exp_f32 v90, v90\n"
    "v_cvt_pk_bf16_f32 v118, v88, v89\n"
    "v_exp_f32 v91, v91\n"
    "v_mfma_f32_32x32x8_bf16 v[100:115], v[18:19], v[196:197], v[100:115]\n"
    "v_exp_f32 v92, v92\n"
    "v_cvt_pk_bf16_f32 v119, v90, v91\n"
    "v_exp_f32 v93, v93\n"
    "ds_write_b128 v244, v[184:187] offset:26624\n"
    "v_exp_f32 v94, v94\n"
    "v_mfma_f32_32x32x16_bf16 v[160:175], v[6:9], v[116:119], v[160:175]\n"
    "v_cvt_pk_bf16_f32 v120, v92, v93\n"
    "v_exp_f32 v95, v95\n"
    "v_exp_f32 v96, v96\n"
    "v_cvt_pk_bf16_f32 v121, v94, v95\n"
    "v_exp_f32 v97, v97\n"
    "s_waitcnt lgkmcnt(3)\n"
    "v_mfma_f32_32x32x16_bf16 v[204:219], v[220:223], v[224:227], v[204:219]\n"
    "ds_read_b128 v[220:223], v250 offset:1440\n"
    "ds_read_b128 v[224:227], v251 offset:1616\n"
    "v_exp_f32 v98, v98\n"
    "v_exp_f32 v99, v99\n"
    "s_waitcnt lgkmcnt(3)\n"
    "v_mfma_f32_32x32x16_bf16 v[204:219], v[228:231], v[232:235], v[204:219]\n"
    "v_cvt_pk_bf16_f32 v122, v96, v97\n"
    "v_cvt_pk_bf16_f32 v123, v98, v99\n"
    "s_waitcnt lgkmcnt(2)\n"
    "v_mov_b32 v252, 0x10000\n"
    "ds_add_u32 v254, v252 offset:61052\n"
    "v_exp_f32 v100, v100\n"
    "v_exp_f32 v101, v101\n"
    "v_mfma_f32_32x32x16_bf16 v[160:175], v[10:13], v[120:123], v[160:175]\n"
    "v_exp_f32 v102, v102\n"
    "v_cvt_pk_bf16_f32 v116, v100, v101\n"
    "v_exp_f32 v103, v103\n"
    "v_exp_f32 v104, v104\n"
    "v_mfma_f32_32x32x16_bf16 v[84:99], v[28:31], v[192:195], 0\n"
    "ds_read_b128 v[228:231], v250 offset:1408\n"
    "ds_read_b128 v[232:235], v251 offset:1584\n"
    "v_cvt_pk_bf16_f32 v117, v102, v103\n"
    "v_exp_f32 v105, v105\n"
    "v_exp_f32 v106, v106\n"
    "v_cvt_pk_bf16_f32 v118, v104, v105\n"
    "v_exp_f32 v107, v107\n"
    "v_mfma_f32_32x32x8_bf16 v[84:99], v[32:33], v[196:197], v[84:99]\n"
    "v_exp_f32 v108, v108\n"
    "v_cvt_pk_bf16_f32 v119, v106, v107\n"
    "v_exp_f32 v109, v109\n"
    "v_exp_f32 v110, v110\n"
    "v_mfma_f32_32x32x16_bf16 v[160:175], v[20:23], v[116:119], v[160:175]\n"
    "v_cvt_pk_bf16_f32 v120, v108, v109\n"
    "v_exp_f32 v111, v111\n"
    "v_exp_f32 v112, v112\n"
    "v_cvt_pk_bf16_f32 v121, v110, v111\n"
    "v_exp_f32 v113, v113\n"
    "s_waitcnt lgkmcnt(3)\n"
    "v_mfma_f32_32x32x16_bf16 v[204:219], v[220:223], v[224:227], v[204:219]\n"
    "ds_read_b128 v[220:223], v250 offset:1376\n"
    "ds_read_b128 v[224:227], v251 offset:1536\n"
    "v_exp_f32 v114, v114\n"
    "v_exp_f32 v115, v115\n"
    "s_waitcnt lgkmcnt(2)\n"
    "v_mfma_f32_32x32x16_bf16 v[204:219], v[228:231], v[232:235], v[204:219]\n"
    "v_cvt_pk_bf16_f32 v122, v112, v113\n"
    "v_cvt_pk_bf16_f32 v123, v114, v115\n"
    "ds_read_b32 v249, v245 offset:61052\n"
    "v_exp_f32 v84, v84\n"
    "v_exp_f32 v85, v85\n"
    "v_mfma_f32_32x32x16_bf16 v[160:175], v[24:27], v[120:123], v[160:175]\n"
    "v_exp_f32 v86, v86\n"
    "v_cvt_pk_bf16_f32 v116, v84, v85\n"
    "v_exp_f32 v87, v87\n"
    "v_exp_f32 v88, v88\n"
    "v_mfma_f32_32x32x16_bf16 v[100:115], v[42:45], v[192:195], 0\n"
    "ds_read_b128 v[228:231], v250 offset:1344\n"
    "ds_read_b128 v[232:235], v251 offset:1504\n"
    "v_cvt_pk_bf16_f32 v117, v86, v87\n"
    "v_exp_f32 v89, v89\n"
    "v_exp_f32 v90, v90\n"
    "v_cvt_pk_bf16_f32 v118, v88, v89\n"
    "v_exp_f32 v91, v91\n"
    "v_mfma_f32_32x32x8_bf16 v[100:115], v[46:47], v[196:197], v[100:115]\n"
    "v_exp_f32 v92, v92\n"
    "v_cvt_pk_bf16_f32 v119, v90, v91\n"
    "v_exp_f32 v93, v93\n"
    "v_exp_f32 v94, v94\n"
    "v_mfma_f32_32x32x16_bf16 v[160:175], v[34:37], v[116:119], v[160:175]\n"
    "v_cvt_pk_bf16_f32 v120, v92, v93\n"
    "v_exp_f32 v95, v95\n"
    "v_exp_f32 v96, v96\n"
    "v_cvt_pk_bf16_f32 v121, v94, v95\n"
    "v_exp_f32 v97, v97\n"
    "s_waitcnt lgkmcnt(3)\n"
    "v_mfma_f32_32x32x16_bf16 v[204:219], v[220:223], v[224:227], v[204:219]\n"
    "ds_read_b128 v[220:223], v250 offset:1312\n"
    "ds_read_b128 v[224:227], v251 offset:1472\n"
    "v_exp_f32 v98, v98\n"
    "v_exp_f32 v99, v99\n"
    "s_waitcnt lgkmcnt(2)\n"
    "v_mfma_f32_32x32x16_bf16 v[204:219], v[228:231], v[232:235], v[204:219]\n"
    "v_cvt_pk_bf16_f32 v122, v96, v97\n"
    "v_cvt_pk_bf16_f32 v123, v98, v99\n"
    ".Lkc_90_%=:\n"
    "v_readfirstlane_b32 s47, v249\n"
    "s_cmp_ge_u32 s47, 524288\n"
    "s_cbranch_scc1 .Lkd_91_%=\n"
    "ds_read_b32 v249, v245 offset:61052\n"
    "s_sleep 1\n"
    "s_waitcnt lgkmcnt(0)\n"
    "s_sub_u32 s80, s80, 1\n"
    "s_cbranch_scc0 .Lkc_90_%=\n"
    "s_mov_b32 s80, 0\n"
    ".Lkd_91_%=:\n"
    "s_setprio 3\n"
    "ds_read_b128 v[176:179], v246 offset:0\n"
    "ds_read_b128 v[180:183], v246 offset:3072\n"
    "ds_read_b128 v[184:187], v246 offset:6144\n"
    "ds_read_b128 v[188:191], v246 offset:9216\n"
    "ds_read_b128 v[124:127], v246 offset:12288\n"
    "ds_read_b128 v[156:159], v246 offset:15360\n"
    "ds_read_b128 v[236:239], v246 offset:18432\n"
    "v_exp_f32 v100, v100\n"
    "v_exp_f32 v101, v101\n"
    "v_mfma_f32_32x32x16_bf16 v[160:175], v[38:41], v[120:123], v[160:175]\n"
    "ds_read_b128 v[198:201], v242 offset:6192\n"
    "ds_read_b64 v[202:203], v243 offset:6224\n"
    "v_exp_f32 v102, v102\n"
    "v_cvt_pk_bf16_f32 v116, v100, v101\n"
    "v_exp_f32 v103, v103\n"
    "v_exp_f32 v104, v104\n"
    "v_mfma_f32_32x32x16_bf16 v[84:99], v[56:59], v[192:195], 0\n"
    "ds_read_b128 v[228:231], v250 offset:1280\n"
    "ds_read_b128 v[232:235], v251 offset:1440\n"
    "v_cvt_pk_bf16_f32 v117, v102, v103\n"
    "v_exp_f32 v105, v105\n"
    "v_exp_f32 v106, v106\n"
    "v_cvt_pk_bf16_f32 v118, v104, v105\n"
    "v_exp_f32 v107, v107\n"
    "v_mfma_f32_32x32x8_bf16 v[84:99], v[60:61], v[196:197], v[84:99]\n"
    "v_exp_f32 v108, v108\n"
    "v_cvt_pk_bf16_f32 v119, v106, v107\n"
    "v_exp_f32 v109, v109\n"
    "v_exp_f32 v110, v110\n"
    "v_mfma_f32_32x32x16_bf16 v[160:175], v[48:51], v[116:119], v[160:175]\n"
    "v_cvt_pk_bf16_f32 v120, v108, v109\n"
    "v_exp_f32 v111, v111\n"
    "v_exp_f32 v112, v112\n"
    "v_cvt_pk_bf16_f32 v121, v110, v111\n"
    "v_exp_f32 v113, v113\n"
    "s_waitcnt lgkmcnt(11)\n"
    "v_mfma_f32_32x32x16_bf16 v[204:219], v[220:223], v[224:227], v[204:219]\n"
    "ds_read_b128 v[220:223], v250 offset:1248\n"
    "ds_read_b128 v[224:227], v251 offset:1392\n"
    "v_exp_f32 v114, v114\n"
    "v_exp_f32 v115, v115\n"
    "s_waitcnt lgkmcnt(2)\n"
    "v_mfma_f32_32x32x16_bf16 v[204:219], v[228:231], v[232:235], v[204:219]\n"
    "v_cvt_pk_bf16_f32 v122, v112, v113\n"
    "v_cvt_pk_bf16_f32 v123, v114, v115\n"
    "v_pk_add_f32 v[176:177], v[176:177], v[180:181]\n"
    "v_pk_add_f32 v[178:179], v[178:179], v[182:183]\n"
    "ds_read_b128 v[180:183], v246 offset:21504\n"
    "v_pk_add_f32 v[184:185], v[184:185], v[188:189]\n"
    "v_pk_add_f32 v[186:187], v[186:187], v[190:191]\n"
    "v_pk_add_f32 v[124:125], v[124:125], v[156:157]\n"
    "v_pk_add_f32 v[126:127], v[126:127], v[158:159]\n"
    "v_pk_add_f32 v[176:177], v[176:177], v[184:185]\n"
    "v_pk_add_f32 v[178:179], v[178:179], v[186:187]\n"
    "v_exp_f32 v84, v84\n"
    "v_exp_f32 v85, v85\n"
    "v_mfma_f32_32x32x16_bf16 v[160:175], v[52:55], v[120:123], v[160:175]\n"
    "v_exp_f32 v86, v86\n"
    "v_cvt_pk_bf16_f32 v116, v84, v85\n"
    "v_exp_f32 v87, v87\n"
    "v_exp_f32 v88, v88\n"
    "v_mfma_f32_32x32x16_bf16 v[100:115], v[70:73], v[192:195], 0\n"
    "ds_read_b128 v[228:231], v250 offset:1216\n"
    "ds_read_b128 v[232:235], v251 offset:1360\n"
    "v_cvt_pk_bf16_f32 v117, v86, v87\n"
    "v_exp_f32 v89, v89\n"
    "v_exp_f32 v90, v90\n"
    "v_cvt_pk_bf16_f32 v118, v88, v89\n"
    "v_exp_f32 v91, v91\n"
    "v_mfma_f32_32x32x8_bf16 v[100:115], v[74:75], v[196:197], v[100:115]\n"
    "v_exp_f32 v92, v92\n"
    "v_cvt_pk_bf16_f32 v119, v90, v91\n"
    "v_exp_f32 v93, v93\n"
    "v_exp_f32 v94, v94\n"
    "v_mfma_f32_32x32x16_bf16 v[160:175], v[62:65], v[116:119], v[160:175]\n"
    "v_cvt_pk_bf16_f32 v120, v92, v93\n"
    "v_exp_f32 v95, v95\n"
    "v_exp_f32 v96, v96\n"
    "v_cvt_pk_bf16_f32 v121, v94, v95\n"
    "v_exp_f32 v97, v97\n"
    "s_waitcnt lgkmcnt(3)\n"
    "v_mfma_f32_32x32x16_bf16 v[204:219], v[220:223], v[224:227], v[204:219]\n"
    "ds_read_b128 v[220:223], v250 offset:1184\n"
    "ds_read_b128 v[224:227], v251 offset:1328\n"
    "v_exp_f32 v98, v98\n"
    "v_exp_f32 v99, v99\n"
    "s_waitcnt lgkmcnt(2)\n"
    "v_mfma_f32_32x32x16_bf16 v[204:219], v[228:231], v[232:235], v[204:219]\n"
    "v_cvt_pk_bf16_f32 v122, v96, v97\n"
    "v_cvt_pk_bf16_f32 v123, v98, v99\n"
    "v_pk_add_f32 v[236:237], v[236:237], v[180:181]\n"
    "v_pk_add_f32 v[238:239], v[238:239], v[182:183]\n"
    "v_pk_add_f32 v[176:177], v[176:177], v[124:125]\n"
    "v_pk_add_f32 v[178:179], v[178:179], v[126:127]\n"
    "v_pk_add_f32 v[176:177], v[176:177], v[236:237]\n"
    "v_pk_add_f32 v[178:179], v[178:179], v[238:239]\n"
    "v_bfe_u32 v180, v247, 2, 5\n"
    "v_add_u32 v180, 160, v180\n"
    "ds_bpermute_b32 v180, v180, v176\n"
    "v_mov_b32 v252, 0x10000\n"
    "ds_add_u32 v254, v252 offset:61436\n"
    "v_exp_f32 v100, v100\n"
    "v_exp_f32 v101, v101\n"
    "v_mfma_f32_32x32x16_bf16 v[160:175], v[66:69], v[120:123], v[160:175]\n"
    "v_exp_f32 v102, v102\n"
    "v_cvt_pk_bf16_f32 v116, v100, v101\n"
    "v_exp_f32 v103, v103\n"
    "v_exp_f32 v104, v104\n"
    "v_mfma_f32_32x32x16_bf16 v[84:99], v[128:131], v[198:201], 0\n"
    "ds_read_b128 v[228:231], v250 offset:1152\n"
    "ds_read_b128 v[232:235], v251 offset:1296\n"
    "v_cvt_pk_bf16_f32 v117, v102, v103\n"
    "v_exp_f32 v105, v105\n"
    "v_exp_f32 v106, v106\n"
    "v_cvt_pk_bf16_f32 v118, v104, v105\n"
    "v_exp_f32 v107, v107\n"
    "v_mfma_f32_32x32x8_bf16 v[84:99], v[132:133], v[202:203], v[84:99]\n"
    "v_exp_f32 v108, v108\n"
    "v_cvt_pk_bf16_f32 v119, v106, v107\n"
    "v_exp_f32 v109, v109\n"
    "v_exp_f32 v110, v110\n"
    "v_mfma_f32_32x32x16_bf16 v[160:175], v[76:79], v[116:119], v[160:175]\n"
    "v_cvt_pk_bf16_f32 v120, v108, v109\n"
    "v_exp_f32 v111, v111\n"
    "v_exp_f32 v112, v112\n"
    "v_cvt_pk_bf16_f32 v121, v110, v111\n"
    "v_exp_f32 v113, v113\n"
    "s_waitcnt lgkmcnt(4)\n"
    "v_mfma_f32_32x32x16_bf16 v[204:219], v[220:223], v[224:227], v[204:219]\n"
    "ds_read_b128 v[220:223], v250 offset:1120\n"
    "ds_read_b128 v[224:227], v251 offset:1248\n"
    "v_exp_f32 v114, v114\n"
    "v_exp_f32 v115, v115\n"
    "s_waitcnt lgkmcnt(2)\n"
    "v_mfma_f32_32x32x16_bf16 v[204:219], v[228:231], v[232:235], v[204:219]\n"
    "v_cvt_pk_bf16_f32 v122, v112, v113\n"
    "v_cvt_pk_bf16_f32 v123, v114, v115\n"
    "v_rcp_f32 v180, v180\n"
    "s_add_u32 s38, s92, 0\n"
    "v_cndmask_b32 v180, v180, 0, s[96:97]\n"
    "v_pk_mul_f32 v[176:177], v[176:177], v[180:181] op_sel_hi:[1,0]\n"
    "v_pk_mul_f32 v[178:179], v[178:179], v[180:181] op_sel_hi:[1,0]\n"
    "v_cvt_pk_bf16_f32 v124, v176, v177\n"
    "v_cvt_pk_bf16_f32 v125, v178, v179\n"
    "buffer_store_dwordx2 v[124:125], v241, s[12:15], s38 offen sc1\n"
    "v_exp_f32 v84, v84\n"
    "v_exp_f32 v85, v85\n"
    "v_mfma_f32_32x32x16_bf16 v[160:175], v[80:83], v[120:123], v[160:175]\n"
    "v_exp_f32 v86, v86\n"
    "v_cvt_pk_bf16_f32 v116, v84, v85\n"
    "v_exp_f32 v87, v87\n"
    "v_exp_f32 v88, v88\n"
    "v_mfma_f32_32x32x16_bf16 v[100:115], v[142:145], v[198:201], 0\n"
    "ds_read_b128 v[228:231], v250 offset:1088\n"
    "ds_read_b128 v[232:235], v251 offset:1216\n"
    "v_cvt_pk_bf16_f32 v117, v86, v87\n"
    "v_exp_f32 v89, v89\n"
    "v_exp_f32 v90, v90\n"
    "v_cvt_pk_bf16_f32 v118, v88, v89\n"
    "v_exp_f32 v91, v91\n"
    "v_mfma_f32_32x32x8_bf16 v[100:115], v[146:147], v[202:203], v[100:115]\n"
    "v_exp_f32 v92, v92\n"
    "v_cvt_pk_bf16_f32 v119, v90, v91\n"
    "v_exp_f32 v93, v93\n"
    "v_exp_f32 v94, v94\n"
    "v_mfma_f32_32x32x16_bf16 v[176:191], v[134:137], v[116:119], 0\n"
    "v_cvt_pk_bf16_f32 v120, v92, v93\n"
    "v_exp_f32 v95, v95\n"
    "v_exp_f32 v96, v96\n"
    "v_cvt_pk_bf16_f32 v121, v94, v95\n"
    "v_exp_f32 v97, v97\n"
    "s_waitcnt lgkmcnt(2)\n"
    "v_mfma_f32_32x32x16_bf16 v[204:219], v[220:223], v[224:227], v[204:219]\n"
    "ds_read_b128 v[220:223], v250 offset:1056\n"
    "ds_read_b128 v[224:227], v251 offset:1184\n"
    "v_exp_f32 v98, v98\n"
    "v_exp_f32 v99, v99\n"
    "s_waitcnt lgkmcnt(2)\n"
    "v_mfma_f32_32x32x16_bf16 v[204:219], v[228:231], v[232:235], v[204:219]\n"
    "v_cvt_pk_bf16_f32 v122, v96, v97\n"
    "v_cvt_pk_bf16_f32 v123, v98, v99\n"
    "v_exp_f32 v100, v100\n"
    "v_exp_f32 v101, v101\n"
    "v_mfma_f32_32x32x16_bf16 v[176:191], v[138:141], v[120:123], v[176:191]\n"
    "v_exp_f32 v102, v102\n"
    "v_cvt_pk_bf16_f32 v116, v100, v101\n"
    "v_exp_f32 v103, v103\n"
    "v_exp_f32 v104, v104\n"
    "v_mfma_f32_32x32x16_bf16 v[84:99], v[0:3], v[198:201], 0\n"
    "ds_read_b128 v[228:231], v250 offset:1024\n"
    "ds_read_b128 v[232:235], v251 offset:1152\n"
    "v_cvt_pk_bf16_f32 v117, v102, v103\n"
    "v_exp_f32 v105, v105\n"
    "v_exp_f32 v106, v106\n"
    "v_cvt_pk_bf16_f32 v118, v104, v105\n"
    "v_exp_f32 v107, v107\n"
    "v_mfma_f32_32x32x8_bf16 v[84:99], v[4:5], v[202:203], v[84:99]\n"
    "v_exp_f32 v108, v108\n"
    "v_cvt_pk_bf16_f32 v119, v106, v107\n"
    "v_exp_f32 v109, v109\n"
    "v_exp_f32 v110, v110\n"
    "v_mfma_f32_32x32x16_bf16 v[176:191], v[148:151], v[116:119], v[176:191]\n"
    "v_cvt_pk_bf16_f32 v120, v108, v109\n"
    "v_exp_f32 v111, v111\n"
    "v_exp_f32 v112, v112\n"
    "v_cvt_pk_bf16_f32 v121, v110, v111\n"
    "v_exp_f32 v113, v113\n"
    "s_waitcnt lgkmcnt(2)\n"
    "v_mfma_f32_32x32x16_bf16 v[204:219], v[220:223], v[224:227], v[204:219]\n"
    "ds_read_b128 v[220:223], v250 offset:992\n"
    "ds_read_b128 v[224:227], v251 offset:1104\n"
    "v_exp_f32 v114, v114\n"
    "v_exp_f32 v115, v115\n"
    "s_waitcnt lgkmcnt(2)\n"
    "v_mfma_f32_32x32x16_bf16 v[204:219], v[228:231], v[232:235], v[204:219]\n"
    "v_cvt_pk_bf16_f32 v122, v112, v113\n"
    "v_cvt_pk_bf16_f32 v123, v114, v115\n"
    "v_exp_f32 v84, v84\n"
    "v_exp_f32 v85, v85\n"
    "v_mfma_f32_32x32x16_bf16 v[176:191], v[152:155], v[120:123], v[176:191]\n"
    "v_exp_f32 v86, v86\n"
    "v_cvt_pk_bf16_f32 v116, v84, v85\n"
    "v_exp_f32 v87, v87\n"
    "v_exp_f32 v88, v88\n"
    "v_mfma_f32_32x32x16_bf16 v[100:115], v[14:17], v[198:201], 0\n"
    "ds_read_b128 v[228:231], v250 offset:960\n"
    "ds_read_b128 v[232:235], v251 offset:1072\n"
    "v_cvt_pk_bf16_f32 v117, v86, v87\n"
    "v_exp_f32 v89, v89\n"
    "v_exp_f32 v90, v90\n"
    "v_cvt_pk_bf16_f32 v118, v88, v89\n"
    "v_exp_f32 v91, v91\n"
    "v_mfma_f32_32x32x8_bf16 v[100:115], v[18:19], v[202:203], v[100:115]\n"
    "v_exp_f32 v92, v92\n"
    "v_cvt_pk_bf16_f32 v119, v90, v91\n"
    "v_exp_f32 v93, v93\n"
    "v_exp_f32 v94, v94\n"
    "v_mfma_f32_32x32x16_bf16 v[176:191], v[6:9], v[116:119], v[176:191]\n"
    "v_cvt_pk_bf16_f32 v120, v92, v93\n"
    "v_exp_f32 v95, v95\n"
    "v_exp_f32 v96, v96\n"
    "v_cvt_pk_bf16_f32 v121, v94, v95\n"
    "v_exp_f32 v97, v97\n"
    "s_waitcnt lgkmcnt(2)\n"
    "v_mfma_f32_32x32x16_bf16 v[204:219], v[220:223], v[224:227], v[204:219]\n"
    "ds_read_b128 v[220:223], v250 offset:928\n"
    "ds_read_b128 v[224:227], v251 offset:1040\n"
    "v_exp_f32 v98, v98\n"
    "v_exp_f32 v99, v99\n"
    "s_waitcnt lgkmcnt(2)\n"
    "v_mfma_f32_32x32x16_bf16 v[204:219], v[228:231], v[232:235], v[204:219]\n"
    "v_cvt_pk_bf16_f32 v122, v96, v97\n"
    "v_cvt_pk_bf16_f32 v123, v98, v99\n"
    "s_setprio 2\n"
    "s_waitcnt vmcnt(0)\n"
    "s_add_u32 s38, s93, 0\n"
    "v_mov_b32 v252, s34\n"
    "v_mov_b32 v253, s35\n"
    "buffer_store_dwordx2 v[252:253], v254, s[12:15], s38 offen sc1\n"
    "ds_read_b32 v249, v245 offset:61436\n"
    "v_exp_f32 v100, v100\n"
    "v_exp_f32 v101, v101\n"
    "v_mfma_f32_32x32x16_bf16 v[176:191], v[10:13], v[120:123], v[176:191]\n"
    "v_exp_f32 v102, v102\n"
    "v_cvt_pk_bf16_f32 v116, v100, v101\n"
    "v_exp_f32 v103, v103\n"
    "v_exp_f32 v104, v104\n"
    "v_mfma_f32_32x32x16_bf16 v[84:99], v[28:31], v[198:201], 0\n"
    "ds_read_b128 v[228:231], v250 offset:896\n"
    "ds_read_b128 v[232:235], v251 offset:1008\n"
    "v_cvt_pk_bf16_f32 v117, v102, v103\n"
    "v_exp_f32 v105, v105\n"
    "v_exp_f32 v106, v106\n"
    "v_cvt_pk_bf16_f32 v118, v104, v105\n"
    "v_exp_f32 v107, v107\n"
    "v_mfma_f32_32x32x8_bf16 v[84:99], v[32:33], v[202:203], v[84:99]\n"
    "v_exp_f32 v108, v108\n"
    "v_cvt_pk_bf16_f32 v119, v106, v107\n"
    "v_exp_f32 v109, v109\n"
    "v_exp_f32 v110, v110\n"
    "v_mfma_f32_32x32x16_bf16 v[176:191], v[20:23], v[116:119], v[176:191]\n"
    "v_cvt_pk_bf16_f32 v120, v108, v109\n"
    "v_exp_f32 v111, v111\n"
    "v_exp_f32 v112, v112\n"
    "v_cvt_pk_bf16_f32 v121, v110, v111\n"
    "v_exp_f32 v113, v113\n"
    "s_waitcnt lgkmcnt(3)\n"
    "v_mfma_f32_32x32x16_bf16 v[204:219], v[220:223], v[224:227], v[204:219]\n"
    "ds_read_b128 v[220:223], v250 offset:864\n"
    "ds_read_b128 v[224:227], v251 offset:960\n"
    "v_exp_f32 v114, v114\n"
    "v_exp_f32 v115, v115\n"
    "s_waitcnt lgkmcnt(2)\n"
    "v_mfma_f32_32x32x16_bf16 v[204:219], v[228:231], v[232:235], v[204:219]\n"
    "v_cvt_pk_bf16_f32 v122, v112, v113\n"
    "v_cvt_pk_bf16_f32 v123, v114, v115\n"
    ".Lkc_92_%=:\n"
    "v_readfirstlane_b32 s47, v249\n"
    "s_cmp_ge_u32 s47, 1048576\n"
    "s_cbranch_scc1 .Lkd_93_%=\n"
    "ds_read_b32 v249, v245 offset:61436\n"
    "s_sleep 1\n"
    "s_waitcnt lgkmcnt(0)\n"
    "s_sub_u32 s80, s80, 1\n"
    "s_cbranch_scc0 .Lkc_92_%=\n"
    "s_mov_b32 s80, 0\n"
    ".Lkd_93_%=:\n"
    "v_exp_f32 v84, v84\n"
    "v_exp_f32 v85, v85\n"
    "v_mfma_f32_32x32x16_bf16 v[176:191], v[24:27], v[120:123], v[176:191]\n"
    "v_exp_f32 v86, v86\n"
    "v_cvt_pk_bf16_f32 v116, v84, v85\n"
    "v_exp_f32 v87, v87\n"
    "v_exp_f32 v88, v88\n"
    "v_mfma_f32_32x32x16_bf16 v[100:115], v[42:45], v[198:201], 0\n"
    "ds_read_b128 v[228:231], v250 offset:832\n"
    "ds_read_b128 v[232:235], v251 offset:928\n"
    "v_cvt_pk_bf16_f32 v117, v86, v87\n"
    "v_exp_f32 v89, v89\n"
    "v_exp_f32 v90, v90\n"
    "v_cvt_pk_bf16_f32 v118, v88, v89\n"
    "v_exp_f32 v91, v91\n"
    "v_mfma_f32_32x32x8_bf16 v[100:115], v[46:47], v[202:203], v[100:115]\n"
    "v_exp_f32 v92, v92\n"
    "v_cvt_pk_bf16_f32 v119, v90, v91\n"
    "v_exp_f32 v93, v93\n"
    "ds_write_b128 v244, v[160:163] offset:0\n"
    "v_exp_f32 v94, v94\n"
    "v_mfma_f32_32x32x16_bf16 v[176:191], v[34:37], v[116:119], v[176:191]\n"
    "v_cvt_pk_bf16_f32 v120, v92, v93\n"
    "v_exp_f32 v95, v95\n"
    "v_exp_f32 v96, v96\n"
    "v_cvt_pk_bf16_f32 v121, v94, v95\n"
    "v_exp_f32 v97, v97\n"
    "s_waitcnt lgkmcnt(3)\n"
    "v_mfma_f32_32x32x16_bf16 v[204:219], v[220:223], v[224:227], v[204:219]\n"
    "ds_read_b128 v[220:223], v250 offset:800\n"
    "ds_read_b128 v[224:227], v251 offset:896\n"
    "v_exp_f32 v98, v98\n"
    "v_exp_f32 v99, v99\n"
    "s_waitcnt lgkmcnt(3)\n"
    "v_mfma_f32_32x32x16_bf16 v[204:219], v[228:231], v[232:235], v[204:219]\n"
    "v_cvt_pk_bf16_f32 v122, v96, v97\n"
    "v_cvt_pk_bf16_f32 v123, v98, v99\n"
    "v_exp_f32 v100, v100\n"
    "v_exp_f32 v101, v101\n"
    "v_mfma_f32_32x32x16_bf16 v[176:191], v[38:41], v[120:123], v[176:191]\n"
    "ds_read_b128 v[192:195], v242 offset:96\n"
    "ds_read_b64 v[196:197], v243 offset:128\n"
    "v_exp_f32 v102, v102\n"
    "v_cvt_pk_bf16_f32 v116, v100, v101\n"
    "v_exp_f32 v103, v103\n"
    "v_exp_f32 v104, v104\n"
    "v_mfma_f32_32x32x16_bf16 v[84:99], v[56:59], v[198:201], 0\n"
    "ds_read_b128 v[228:231], v250 offset:768\n"
    "ds_read_b128 v[232:235], v251 offset:864\n"
    "v_cvt_pk_bf16_f32 v117, v102, v103\n"
    "v_exp_f32 v105, v105\n"
    "v_exp_f32 v106, v106\n"
    "v_cvt_pk_bf16_f32 v118, v104, v105\n"
    "v_exp_f32 v107, v107\n"
    "v_mfma_f32_32x32x8_bf16 v[84:99], v[60:61], v[202:203], v[84:99]\n"
    "v_exp_f32 v108, v108\n"
    "v_cvt_pk_bf16_f32 v119, v106, v107\n"
    "v_exp_f32 v109, v109\n"
    "ds_write_b128 v244, v[164:167] offset:1024\n"
    "v_exp_f32 v110, v110\n"
    "v_mfma_f32_32x32x16_bf16 v[176:191], v[48:51], v[116:119], v[176:191]\n"
    "v_cvt_pk_bf16_f32 v120, v108, v109\n"
    "v_exp_f32 v111, v111\n"
    "v_exp_f32 v112, v112\n"
    "v_cvt_pk_bf16_f32 v121, v110, v111\n"
    "v_exp_f32 v113, v113\n"
    "s_waitcnt lgkmcnt(5)\n"
    "v_mfma_f32_32x32x16_bf16 v[204:219], v[220:223], v[224:227], v[204:219]\n"
    "ds_read_b128 v[220:223], v250 offset:736\n"
    "ds_read_b128 v[224:227], v251 offset:816\n"
    "v_exp_f32 v114, v114\n"
    "v_exp_f32 v115, v115\n"
    "s_waitcnt lgkmcnt(3)\n"
    "v_mfma_f32_32x32x16_bf16 v[204:219], v[228:231], v[232:235], v[204:219]\n"
    "v_cvt_pk_bf16_f32 v122, v112, v113\n"
    "v_cvt_pk_bf16_f32 v123, v114, v115\n"
    "v_exp_f32 v84, v84\n"
    "v_exp_f32 v85, v85\n"
    "v_mfma_f32_32x32x16_bf16 v[176:191], v[52:55], v[120:123], v[176:191]\n"
    "v_exp_f32 v86, v86\n"
    "v_cvt_pk_bf16_f32 v116, v84, v85\n"
    "v_exp_f32 v87, v87\n"
    "v_exp_f32 v88, v88\n"
    "v_mfma_f32_32x32x16_bf16 v[100:115], v[70:73], v[198:201], 0\n"
    "ds_read_b128 v[228:231], v250 offset:704\n"
    "ds_read_b128 v[232:235], v251 offset:784\n"
    "v_cvt_pk_bf16_f32 v117, v86, v87\n"
    "v_exp_f32 v89, v89\n"
    "v_exp_f32 v90, v90\n"
    "v_cvt_pk_bf16_f32 v118, v88, v89\n"
    "v_exp_f32 v91, v91\n"
    "v_mfma_f32_32x32x8_bf16 v[100:115], v[74:75], v[202:203], v[100:115]\n"
    "v_exp_f32 v92, v92\n"
    "v_cvt_pk_bf16_f32 v119, v90, v91\n"
    "v_exp_f32 v93, v93\n"
    "ds_write_b128 v244, v[168:171] offset:2048\n"
    "v_exp_f32 v94, v94\n"
    "v_mfma_f32_32x32x16_bf16 v[176:191], v[62:65], v[116:119], v[176:191]\n"
    "v_cvt_pk_bf16_f32 v120, v92, v93\n"
    "v_exp_f32 v95, v95\n"
    "v_exp_f32 v96, v96\n"
    "v_cvt_pk_bf16_f32 v121, v94, v95\n"
    "v_exp_f32 v97, v97\n"
    "s_waitcnt lgkmcnt(3)\n"
    "v_mfma_f32_32x32x16_bf16 v[204:219], v[220:223], v[224:227], v[204:219]\n"
    "ds_read_b128 v[220:223], v250 offset:672\n"
    "ds_read_b128 v[224:227], v251 offset:752\n"
    "v_exp_f32 v98, v98\n"
    "v_exp_f32 v99, v99\n"
    "s_waitcnt lgkmcnt(3)\n"
    "v_mfma_f32_32x32x16_bf16 v[204:219], v[228:231], v[232:235], v[204:219]\n"
    "v_cvt_pk_bf16_f32 v122, v96, v97\n"
    "v_cvt_pk_bf16_f32 v123, v98, v99\n"
    "v_exp_f32 v100, v100\n"
    "v_exp_f32 v101, v101\n"
    "v_mfma_f32_32x32x16_bf16 v[176:191], v[66:69], v[120:123], v[176:191]\n"
    "v_exp_f32 v102, v102\n"
    "v_cvt_pk_bf16_f32 v116, v100, v101\n"
    "v_exp_f32 v103, v103\n"
    "v_exp_f32 v104, v104\n"
    "v_mfma_f32_32x32x16_bf16 v[84:99], v[128:131], v[192:195], 0\n"
    "ds_read_b128 v[228:231], v250 offset:640\n"
    "ds_read_b128 v[232:235], v251 offset:720\n"
    "v_cvt_pk_bf16_f32 v117, v102, v103\n"
    "v_exp_f32 v105, v105\n"
    "v_exp_f32 v106, v106\n"
    "v_cvt_pk_bf16_f32 v118, v104, v105\n"
    "v_exp_f32 v107, v107\n"
    "v_mfma_f32_32x32x8_bf16 v[84:99], v[132:133], v[196:197], v[84:99]\n"
    "v_exp_f32 v108, v108\n"
    "v_cvt_pk_bf16_f32 v119, v106, v107\n"
    "v_exp_f32 v109, v109\n"
    "v_exp_f32 v110, v110\n"
    "v_mfma_f32_32x32x16_bf16 v[176:191], v[76:79], v[116:119], v[176:191]\n"
    "v_cvt_pk_bf16_f32 v120, v108, v109\n"
    "v_exp_f32 v111, v111\n"
    "v_exp_f32 v112, v112\n"
    "v_cvt_pk_bf16_f32 v121, v110, v111\n"
    "v_exp_f32 v113, v113\n"
    "s_waitcnt lgkmcnt(2)\n"
    "v_mfma_f32_32x32x16_bf16 v[204:219], v[220:223], v[224:227], v[204:219]\n"
    "ds_read_b128 v[220:223], v250 offset:608\n"
    "ds_read_b128 v[224:227], v251 offset:672\n"
    "v_exp_f32 v114, v114\n"
    "v_exp_f32 v115, v115\n"
    "s_waitcnt lgkmcnt(2)\n"
    "v_mfma_f32_32x32x16_bf16 v[204:219], v[228:231], v[232:235], v[204:219]\n"
    "v_cvt_pk_bf16_f32 v122, v112, v113\n"
    "v_cvt_pk_bf16_f32 v123, v114, v115\n"
    "s_setprio 1\n"
    "v_exp_f32 v84, v84\n"
    "v_exp_f32 v85, v85\n"
    "v_mfma_f32_32x32x16_bf16 v[176:191], v[80:83], v[120:123], v[176:191]\n"
    "v_exp_f32 v86, v86\n"
    "v_cvt_pk_bf16_f32 v116, v84, v85\n"
    "v_exp_f32 v87, v87\n"
    "v_exp_f32 v88, v88\n"
    "v_mfma_f32_32x32x16_bf16 v[100:115], v[142:145], v[192:195], 0\n"
    "ds_read_b128 v[228:231], v250 offset:576\n"
    "ds_read_b128 v[232:235], v251 offset:640\n"
    "v_cvt_pk_bf16_f32 v117, v86, v87\n"
    "v_exp_f32 v89, v89\n"
    "v_exp_f32 v90, v90\n"
    "v_cvt_pk_bf16_f32 v118, v88, v89\n"
    "v_exp_f32 v91, v91\n"
    "v_mfma_f32_32x32x8_bf16 v[100:115], v[146:147], v[196:197], v[100:115]\n"
    "v_exp_f32 v92, v92\n"
    "v_cvt_pk_bf16_f32 v119, v90, v91\n"
    "v_exp_f32 v93, v93\n"
    "ds_write_b128 v244, v[176:179] offset:24576\n"
    "v_exp_f32 v94, v94\n"
    "v_mfma_f32_32x32x16_bf16 v[160:175], v[134:137], v[116:119], 0\n"
    "v_cvt_pk_bf16_f32 v120, v92, v93\n"
    "v_exp_f32 v95, v95\n"
    "v_exp_f32 v96, v96\n"
    "v_cvt_pk_bf16_f32 v121, v94, v95\n"
    "v_exp_f32 v97, v97\n"
    "s_waitcnt lgkmcnt(3)\n"
    "v_mfma_f32_32x32x16_bf16 v[204:219], v[220:223], v[224:227], v[204:219]\n"
    "ds_read_b128 v[220:223], v250 offset:544\n"
    "ds_read_b128 v[224:227], v251 offset:608\n"
    "v_exp_f32 v98, v98\n"
    "v_exp_f32 v99, v99\n"
    "s_waitcnt lgkmcnt(3)\n"
    "v_mfma_f32_32x32x16_bf16 v[204:219], v[228:231], v[232:235], v[204:219]\n"
    "v_cvt_pk_bf16_f32 v122, v96, v97\n"
    "v_cvt_pk_bf16_f32 v123, v98, v99\n"
    "v_exp_f32 v100, v100\n"
    "v_exp_f32 v101, v101\n"
    "v_mfma_f32_32x32x16_bf16 v[160:175], v[138:141], v[120:123], v[160:175]\n"
    "v_exp_f32 v102, v102\n"
    "v_cvt_pk_bf16_f32 v116, v100, v101\n"
    "v_exp_f32 v103, v103\n"
    "v_exp_f32 v104, v104\n"
    "v_mfma_f32_32x32x16_bf16 v[84:99], v[0:3], v[192:195], 0\n"
    "ds_read_b128 v[228:231], v250 offset:512\n"
    "ds_read_b128 v[232:235], v251 offset:576\n"
    "v_cvt_pk_bf16_f32 v117, v102, v103\n"
    "v_exp_f32 v105, v105\n"
    "v_exp_f32 v106, v106\n"
    "v_cvt_pk_bf16_f32 v118, v104, v105\n"
    "v_exp_f32 v107, v107\n"
    "v_mfma_f32_32x32x8_bf16 v[84:99], v[4:5], v[196:197], v[84:99]\n"
    "v_exp_f32 v108, v108\n"
    "v_cvt_pk_bf16_f32 v119, v106, v107\n"
    "v_exp_f32 v109, v109\n"
    "ds_write_b128 v244, v[180:183] offset:25600\n"
    "v_exp_f32 v110, v110\n"
    "v_mfma_f32_32x32x16_bf16 v[160:175], v[148:151], v[116:119], v[160:175]\n"
    "v_cvt_pk_bf16_f32 v120, v108, v109\n"
    "v_exp_f32 v111, v111\n"
    "v_exp_f32 v112, v112\n"
    "v_cvt_pk_bf16_f32 v121, v110, v111\n"
    "v_exp_f32 v113, v113\n"
    "s_waitcnt lgkmcnt(3)\n"
    "v_mfma_f32_32x32x16_bf16 v[204:219], v[220:223], v[224:227], v[204:219]\n"
    "ds_read_b128 v[220:223], v250 offset:480\n"
    "ds_read_b128 v[224:227], v251 offset:528\n"
    "v_exp_f32 v114, v114\n"
    "v_exp_f32 v115, v115\n"
    "s_waitcnt lgkmcnt(3)\n"
    "v_mfma_f32_32x32x16_bf16 v[204:219], v[228:231], v[232:235], v[204:219]\n"
    "v_cvt_pk_bf16_f32 v122, v112, v113\n"
    "v_cvt_pk_bf16_f32 v123, v114, v115\n"
    "v_exp_f32 v84, v84\n"
    "v_exp_f32 v85, v85\n"
    "v_mfma_f32_32x32x16_bf16 v[160:175], v[152:155], v[120:123], v[160:175]\n"
    "v_exp_f32 v86, v86\n"
    "v_cvt_pk_bf16_f32 v116, v84, v85\n"
    "v_exp_f32 v87, v87\n"
    "v_exp_f32 v88, v88\n"
    "v_mfma_f32_32x32x16_bf16 v[100:115], v[14:17], v[192:195], 0\n"
    "ds_read_b128 v[228:231], v250 offset:448\n"
    "ds_read_b128 v[232:235], v251 offset:496\n"
    "v_cvt_pk_bf16_f32 v117, v86, v87\n"
    "v_exp_f32 v89, v89\n"
    "v_exp_f32 v90, v90\n"
    "v_cvt_pk_bf16_f32 v118, v88, v89\n"
    "v_exp_f32 v91, v91\n"
    "v_mfma_f32_32x32x8_bf16 v[100:115], v[18:19], v[196:197], v[100:115]\n"
    "v_exp_f32 v92, v92\n"
    "v_cvt_pk_bf16_f32 v119, v90, v91\n"
    "v_exp_f32 v93, v93\n"
    "ds_write_b128 v244, v[184:187] offset:26624\n"
    "v_exp_f32 v94, v94\n"
    "v_mfma_f32_32x32x16_bf16 v[160:175], v[6:9], v[116:119], v[160:175]\n"
    "v_cvt_pk_bf16_f32 v120, v92, v93\n"
    "v_exp_f32 v95, v95\n"
    "v_exp_f32 v96, v96\n"
    "v_cvt_pk_bf16_f32 v121, v94, v95\n"
    "v_exp_f32 v97, v97\n"
    "s_waitcnt lgkmcnt(3)\n"
    "v_mfma_f32_32x32x16_bf16 v[204:219], v[220:223], v[224:227], v[204:219]\n"
    "ds_read_b128 v[220:223], v250 offset:416\n"
    "ds_read_b128 v[224:227], v251 offset:464\n"
    "v_exp_f32 v98, v98\n"
    "v_exp_f32 v99, v99\n"
    "s_waitcnt lgkmcnt(3)\n"
    "v_mfma_f32_32x32x16_bf16 v[204:219], v[228:231], v[232:235], v[204:219]\n"
    "v_cvt_pk_bf16_f32 v122, v96, v97\n"
    "v_cvt_pk_bf16_f32 v123, v98, v99\n"
    "s_setprio 0\n"
    "s_waitcnt lgkmcnt(2)\n"
    "v_mov_b32 v252, 0x10000\n"
    "ds_add_u32 v254, v252 offset:61052\n"
    "v_exp_f32 v100, v100\n"
    "v_exp_f32 v101, v101\n"
    "v_mfma_f32_32x32x16_bf16 v[160:175], v[10:13], v[120:123], v[160:175]\n"
    "v_exp_f32 v102, v102\n"
    "v_cvt_pk_bf16_f32 v116, v100, v101\n"
    "v_exp_f32 v103, v103\n"
    "v_exp_f32 v104, v104\n"
    "v_mfma_f32_32x32x16_bf16 v[84:99], v[28:31], v[192:195], 0\n"
    "ds_read_b128 v[228:231], v250 offset:384\n"
    "ds_read_b128 v[232:235], v251 offset:432\n"
    "v_cvt_pk_bf16_f32 v117, v102, v103\n"
    "v_exp_f32 v105, v105\n"
    "v_exp_f32 v106, v106\n"
    "v_cvt_pk_bf16_f32 v118, v104, v105\n"
    "v_exp_f32 v107, v107\n"
    "v_mfma_f32_32x32x8_bf16 v[84:99], v[32:33], v[196:197], v[84:99]\n"
    "v_exp_f32 v108, v108\n"
    "v_cvt_pk_bf16_f32 v119, v106, v107\n"
    "v_exp_f32 v109, v109\n"
    "v_exp_f32 v110, v110\n"
    "v_mfma_f32_32x32x16_bf16 v[160:175], v[20:23], v[116:119], v[160:175]\n"
    "v_cvt_pk_bf16_f32 v120, v108, v109\n"
    "v_exp_f32 v111, v111\n"
    "v_exp_f32 v112, v112\n"
    "v_cvt_pk_bf16_f32 v121, v110, v111\n"
    "v_exp_f32 v113, v113\n"
    "s_waitcnt lgkmcnt(3)\n"
    "v_mfma_f32_32x32x16_bf16 v[204:219], v[220:223], v[224:227], v[204:219]\n"
    "ds_read_b128 v[220:223], v250 offset:352\n"
    "ds_read_b128 v[224:227], v251 offset:384\n"
    "v_exp_f32 v114, v114\n"
    "v_exp_f32 v115, v115\n"
    "s_waitcnt lgkmcnt(2)\n"
    "v_mfma_f32_32x32x16_bf16 v[204:219], v[228:231], v[232:235], v[204:219]\n"
    "v_cvt_pk_bf16_f32 v122, v112, v113\n"
    "v_cvt_pk_bf16_f32 v123, v114, v115\n"
    "ds_read_b32 v249, v245 offset:61052\n"
    "v_exp_f32 v84, v84\n"
    "v_exp_f32 v85, v85\n"
    "v_mfma_f32_32x32x16_bf16 v[160:175], v[24:27], v[120:123], v[160:175]\n"
    "v_exp_f32 v86, v86\n"
    "v_cvt_pk_bf16_f32 v116, v84, v85\n"
    "v_exp_f32 v87, v87\n"
    "v_exp_f32 v88, v88\n"
    "v_mfma_f32_32x32x16_bf16 v[100:115], v[42:45], v[192:195], 0\n"
    "ds_read_b128 v[228:231], v250 offset:320\n"
    "ds_read_b128 v[232:235], v251 offset:352\n"
    "v_cvt_pk_bf16_f32 v117, v86, v87\n"
    "v_exp_f32 v89, v89\n"
    "v_exp_f32 v90, v90\n"
    "v_cvt_pk_bf16_f32 v118, v88, v89\n"
    "v_exp_f32 v91, v91\n"
    "v_mfma_f32_32x32x8_bf16 v[100:115], v[46:47], v[196:197], v[100:115]\n"
    "v_exp_f32 v92, v92\n"
    "v_cvt_pk_bf16_f32 v119, v90, v91\n"
    "v_exp_f32 v93, v93\n"
    "v_exp_f32 v94, v94\n"
    "v_mfma_f32_32x32x16_bf16 v[160:175], v[34:37], v[116:119], v[160:175]\n"
    "v_cvt_pk_bf16_f32 v120, v92, v93\n"
    "v_exp_f32 v95, v95\n"
    "v_exp_f32 v96, v96\n"
    "v_cvt_pk_bf16_f32 v121, v94, v95\n"
    "v_exp_f32 v97, v97\n"
    "s_waitcnt lgkmcnt(3)\n"
    "v_mfma_f32_32x32x16_bf16 v[204:219], v[220:223], v[224:227], v[204:219]\n"
    "ds_read_b128 v[220:223], v250 offset:288\n"
    "ds_read_b128 v[224:227], v251 offset:320\n"
    "v_exp_f32 v98, v98\n"
    "v_exp_f32 v99, v99\n"
    "s_waitcnt lgkmcnt(2)\n"
    "v_mfma_f32_32x32x16_bf16 v[204:219], v[228:231], v[232:235], v[204:219]\n"
    "v_cvt_pk_bf16_f32 v122, v96, v97\n"
    "v_cvt_pk_bf16_f32 v123, v98, v99\n"
    ".Lkc_94_%=:\n"
    "v_readfirstlane_b32 s47, v249\n"
    "s_cmp_ge_u32 s47, 1048576\n"
    "s_cbranch_scc1 .Lkd_95_%=\n"
    "ds_read_b32 v249, v245 offset:61052\n"
    "s_sleep 1\n"
    "s_waitcnt lgkmcnt(0)\n"
    "s_sub_u32 s80, s80, 1\n"
    "s_cbranch_scc0 .Lkc_94_%=\n"
    "s_mov_b32 s80, 0\n"
    ".Lkd_95_%=:\n"
    "s_setprio 3\n"
    "ds_read_b128 v[176:179], v246 offset:0\n"
    "ds_read_b128 v[180:183], v246 offset:3072\n"
    "ds_read_b128 v[184:187], v246 offset:6144\n"
    "ds_read_b128 v[188:191], v246 offset:9216\n"
    "ds_read_b128 v[124:127], v246 offset:12288\n"
    "ds_read_b128 v[156:159], v246 offset:15360\n"
    "ds_read_b128 v[236:239], v246 offset:18432\n"
    "v_exp_f32 v100, v100\n"
    "v_exp_f32 v101, v101\n"
    "v_mfma_f32_32x32x16_bf16 v[160:175], v[38:41], v[120:123], v[160:175]\n"
    "ds_read_b128 v[198:201], v242 offset:6240\n"
    "ds_read_b64 v[202:203], v243 offset:6272\n"
    "v_exp_f32 v102, v102\n"
    "v_cvt_pk_bf16_f32 v116, v100, v101\n"
    "v_exp_f32 v103, v103\n"
    "v_exp_f32 v104, v104\n"
    "v_mfma_f32_32x32x16_bf16 v[84:99], v[56:59], v[192:195], 0\n"
    "ds_read_b128 v[228:231], v250 offset:256\n"
    "ds_read_b128 v[232:235], v251 offset:288\n"
    "v_cvt_pk_bf16_f32 v117, v102, v103\n"
    "v_exp_f32 v105, v105\n"
    "v_exp_f32 v106, v106\n"
    "v_cvt_pk_bf16_f32 v118, v104, v105\n"
    "v_exp_f32 v107, v107\n"
    "v_mfma_f32_32x32x8_bf16 v[84:99], v[60:61], v[196:197], v[84:99]\n"
    "v_exp_f32 v108, v108\n"
    "v_cvt_pk_bf16_f32 v119, v106, v107\n"
    "v_exp_f32 v109, v109\n"
    "v_exp_f32 v110, v110\n"
    "v_mfma_f32_32x32x16_bf16 v[160:175], v[48:51], v[116:119], v[160:175]\n"
    "v_cvt_pk_bf16_f32 v120, v108, v109\n"
    "v_exp_f32 v111, v111\n"
    "v_exp_f32 v112, v112\n"
    "v_cvt_pk_bf16_f32 v121, v110, v111\n"
    "v_exp_f32 v113, v113\n"
    "s_waitcnt lgkmcnt(11)\n"
    "v_mfma_f32_32x32x16_bf16 v[204:219], v[220:223], v[224:227], v[204:219]\n"
    "ds_read_b128 v[220:223], v250 offset:224\n"
    "ds_read_b128 v[224:227], v251 offset:240\n"
    "v_exp_f32 v114, v114\n"
    "v_exp_f32 v115, v115\n"
    "s_waitcnt lgkmcnt(2)\n"
    "v_mfma_f32_32x32x16_bf16 v[204:219], v[228:231], v[232:235], v[204:219]\n"
    "v_cvt_pk_bf16_f32 v122, v112, v113\n"
    "v_cvt_pk_bf16_f32 v123, v114, v115\n"
    "v_pk_add_f32 v[176:177], v[176:177], v[180:181]\n"
    "v_pk_add_f32 v[178:179], v[178:179], v[182:183]\n"
    "ds_read_b128 v[180:183], v246 offset:21504\n"
    "v_pk_add_f32 v[184:185], v[184:185], v[188:189]\n"
    "v_pk_add_f32 v[186:187], v[186:187], v[190:191]\n"
    "v_pk_add_f32 v[124:125], v[124:125], v[156:157]\n"
    "v_pk_add_f32 v[126:127], v[126:127], v[158:159]\n"
    "v_pk_add_f32 v[176:177], v[176:177], v[184:185]\n"
    "v_pk_add_f32 v[178:179], v[178:179], v[186:187]\n"
    "v_exp_f32 v84, v84\n"
    "v_exp_f32 v85, v85\n"
    "v_mfma_f32_32x32x16_bf16 v[160:175], v[52:55], v[120:123], v[160:175]\n"
    "v_exp_f32 v86, v86\n"
    "v_cvt_pk_bf16_f32 v116, v84, v85\n"
    "v_exp_f32 v87, v87\n"
    "v_exp_f32 v88, v88\n"
    "v_mfma_f32_32x32x16_bf16 v[100:115], v[70:73], v[192:195], 0\n"
    "ds_read_b128 v[228:231], v250 offset:192\n"
    "ds_read_b128 v[232:235], v251 offset:208\n"
    "v_cvt_pk_bf16_f32 v117, v86, v87\n"
    "v_exp_f32 v89, v89\n"
    "v_exp_f32 v90, v90\n"
    "v_cvt_pk_bf16_f32 v118, v88, v89\n"
    "v_exp_f32 v91, v91\n"
    "v_mfma_f32_32x32x8_bf16 v[100:115], v[74:75], v[196:197], v[100:115]\n"
    "v_exp_f32 v92, v92\n"
    "v_cvt_pk_bf16_f32 v119, v90, v91\n"
    "v_exp_f32 v93, v93\n"
    "v_exp_f32 v94, v94\n"
    "v_mfma_f32_32x32x16_bf16 v[160:175], v[62:65], v[116:119], v[160:175]\n"
    "v_cvt_pk_bf16_f32 v120, v92, v93\n"
    "v_exp_f32 v95, v95\n"
    "v_exp_f32 v96, v96\n"
    "v_cvt_pk_bf16_f32 v121, v94, v95\n"
    "v_exp_f32 v97, v97\n"
    "s_waitcnt lgkmcnt(3)\n"
    "v_mfma_f32_32x32x16_bf16 v[204:219], v[220:223], v[224:227], v[204:219]\n"
    "ds_read_b128 v[220:223], v250 offset:160\n"
    "ds_read_b128 v[224:227], v251 offset:176\n"
    "v_exp_f32 v98, v98\n"
    "v_exp_f32 v99, v99\n"
    "s_waitcnt lgkmcnt(2)\n"
    "v_mfma_f32_32x32x16_bf16 v[204:219], v[228:231], v[232:235], v[204:219]\n"
    "v_cvt_pk_bf16_f32 v122, v96, v97\n"
    "v_cvt_pk_bf16_f32 v123, v98, v99\n"
    "v_pk_add_f32 v[236:237], v[236:237], v[180:181]\n"
    "v_pk_add_f32 v[238:239], v[238:239], v[182:183]\n"
    "v_pk_add_f32 v[176:177], v[176:177], v[124:125]\n"
    "v_pk_add_f32 v[178:179], v[178:179], v[126:127]\n"
    "v_pk_add_f32 v[176:177], v[176:177], v[236:237]\n"
    "v_pk_add_f32 v[178:179], v[178:179], v[238:239]\n"
    "v_bfe_u32 v180, v247, 2, 5\n"
    "v_add_u32 v180, 160, v180\n"
    "ds_bpermute_b32 v180, v180, v176\n"
    "v_mov_b32 v252, 0x10000\n"
    "ds_add_u32 v254, v252 offset:61436\n"
    "v_exp_f32 v100, v100\n"
    "v_exp_f32 v101, v101\n"
    "v_mfma_f32_32x32x16_bf16 v[160:175], v[66:69], v[120:123], v[160:175]\n"
    "v_exp_f32 v102, v102\n"
    "v_cvt_pk_bf16_f32 v116, v100, v101\n"
    "v_exp_f32 v103, v103\n"
    "v_exp_f32 v104, v104\n"
    "v_mfma_f32_32x32x16_bf16 v[84:99], v[128:131], v[198:201], 0\n"
    "ds_read_b128 v[228:231], v250 offset:128\n"
    "ds_read_b128 v[232:235], v251 offset:144\n"
    "v_cvt_pk_bf16_f32 v117, v102, v103\n"
    "v_exp_f32 v105, v105\n"
    "v_exp_f32 v106, v106\n"
    "v_cvt_pk_bf16_f32 v118, v104, v105\n"
    "v_exp_f32 v107, v107\n"
    "v_mfma_f32_32x32x8_bf16 v[84:99], v[132:133], v[202:203], v[84:99]\n"
    "v_exp_f32 v108, v108\n"
    "v_cvt_pk_bf16_f32 v119, v106, v107\n"
    "v_exp_f32 v109, v109\n"
    "v_exp_f32 v110, v110\n"
    "v_mfma_f32_32x32x16_bf16 v[160:175], v[76:79], v[116:119], v[160:175]\n"
    "v_cvt_pk_bf16_f32 v120, v108, v109\n"
    "v_exp_f32 v111, v111\n"
    "v_exp_f32 v112, v112\n"
    "v_cvt_pk_bf16_f32 v121, v110, v111\n"
    "v_exp_f32 v113, v113\n"
    "s_waitcnt lgkmcnt(4)\n"
    "v_mfma_f32_32x32x16_bf16 v[204:219], v[220:223], v[224:227], v[204:219]\n"
    "ds_read_b128 v[220:223], v250 offset:96\n"
    "ds_read_b128 v[224:227], v251 offset:96\n"
    "v_exp_f32 v114, v114\n"
    "v_exp_f32 v115, v115\n"
    "s_waitcnt lgkmcnt(2)\n"
    "v_mfma_f32_32x32x16_bf16 v[204:219], v[228:231], v[232:235], v[204:219]\n"
    "v_cvt_pk_bf16_f32 v122, v112, v113\n"
    "v_cvt_pk_bf16_f32 v123, v114, v115\n"
    "v_rcp_f32 v180, v180\n"
    "s_add_u32 s38, s92, 24576\n"
    "v_cndmask_b32 v180, v180, 0, s[96:97]\n"
    "v_pk_mul_f32 v[176:177], v[176:177], v[180:181] op_sel_hi:[1,0]\n"
    "v_pk_mul_f32 v[178:179], v[178:179], v[180:181] op_sel_hi:[1,0]\n"
    "v_cvt_pk_bf16_f32 v124, v176, v177\n"
    "v_cvt_pk_bf16_f32 v125, v178, v179\n"
    "buffer_store_dwordx2 v[124:125], v241, s[12:15], s38 offen sc1\n"
    "v_exp_f32 v84, v84\n"
    "v_exp_f32 v85, v85\n"
    "v_mfma_f32_32x32x16_bf16 v[160:175], v[80:83], v[120:123], v[160:175]\n"
    "v_exp_f32 v86, v86\n"
    "v_cvt_pk_bf16_f32 v116, v84, v85\n"
    "v_exp_f32 v87, v87\n"
    "v_exp_f32 v88, v88\n"
    "v_mfma_f32_32x32x16_bf16 v[100:115], v[142:145], v[198:201], 0\n"
    "ds_read_b128 v[228:231], v250 offset:64\n"
    "ds_read_b128 v[232:235], v251 offset:64\n"
    "v_cvt_pk_bf16_f32 v117, v86, v87\n"
    "v_exp_f32 v89, v89\n"
    "v_exp_f32 v90, v90\n"
    "v_cvt_pk_bf16_f32 v118, v88, v89\n"
    "v_exp_f32 v91, v91\n"
    "v_mfma_f32_32x32x8_bf16 v[100:115], v[146:147], v[202:203], v[100:115]\n"
    "v_exp_f32 v92, v92\n"
    "v_cvt_pk_bf16_f32 v119, v90, v91\n"
    "v_exp_f32 v93, v93\n"
    "v_exp_f32 v94, v94\n"
    "v_mfma_f32_32x32x16_bf16 v[176:191], v[134:137], v[116:119], 0\n"
    "v_cvt_pk_bf16_f32 v120, v92, v93\n"
    "v_exp_f32 v95, v95\n"
    "v_exp_f32 v96, v96\n"
    "v_cvt_pk_bf16_f32 v121, v94, v95\n"
    "v_exp_f32 v97, v97\n"
    "s_waitcnt lgkmcnt(2)\n"
    "v_mfma_f32_32x32x16_bf16 v[204:219], v[220:223], v[224:227], v[204:219]\n"
    "ds_read_b128 v[220:223], v250 offset:32\n"
    "ds_read_b128 v[224:227], v251 offset:32\n"
    "v_exp_f32 v98, v98\n"
    "v_exp_f32 v99, v99\n"
    "s_waitcnt lgkmcnt(2)\n"
    "v_mfma_f32_32x32x16_bf16 v[204:219], v[228:231], v[232:235], v[204:219]\n"
    "v_cvt_pk_bf16_f32 v122, v96, v97\n"
    "v_cvt_pk_bf16_f32 v123, v98, v99\n"
    "v_exp_f32 v100, v100\n"
    "v_exp_f32 v101, v101\n"
    "v_mfma_f32_32x32x16_bf16 v[176:191], v[138:141], v[120:123], v[176:191]\n"
    "v_exp_f32 v102, v102\n"
    "v_cvt_pk_bf16_f32 v116, v100, v101\n"
    "v_exp_f32 v103, v103\n"
    "v_exp_f32 v104, v104\n"
    "v_mfma_f32_32x32x16_bf16 v[84:99], v[0:3], v[198:201], 0\n"
    "ds_read_b128 v[228:231], v250 offset:0\n"
    "ds_read_b128 v[232:235], v251 offset:0\n"
    "v_cvt_pk_bf16_f32 v117, v102, v103\n"
    "v_exp_f32 v105, v105\n"
    "v_exp_f32 v106, v106\n"
    "v_cvt_pk_bf16_f32 v118, v104, v105\n"
    "v_exp_f32 v107, v107\n"
    "v_mfma_f32_32x32x8_bf16 v[84:99], v[4:5], v[202:203], v[84:99]\n"
    "v_exp_f32 v108, v108\n"
    "v_cvt_pk_bf16_f32 v119, v106, v107\n"
    "v_exp_f32 v109, v109\n"
    "v_exp_f32 v110, v110\n"
    "v_mfma_f32_32x32x16_bf16 v[176:191], v[148:151], v[116:119], v[176:191]\n"
    "v_cvt_pk_bf16_f32 v120, v108, v109\n"
    "v_exp_f32 v111, v111\n"
    "v_exp_f32 v112, v112\n"
    "v_cvt_pk_bf16_f32 v121, v110, v111\n"
    "v_exp_f32 v113, v113\n"
    "s_waitcnt lgkmcnt(2)\n"
    "v_mfma_f32_32x32x16_bf16 v[204:219], v[220:223], v[224:227], v[204:219]\n"
    "v_exp_f32 v114, v114\n"
    "v_exp_f32 v115, v115\n"
    "s_waitcnt lgkmcnt(0)\n"
    "v_mfma_f32_32x32x16_bf16 v[204:219], v[228:231], v[232:235], v[204:219]\n"
    "v_cvt_pk_bf16_f32 v122, v112, v113\n"
    "v_cvt_pk_bf16_f32 v123, v114, v115\n"
    "s_waitcnt lgkmcnt(0)\n"
    "v_mov_b32 v252, 0x10000\n"
    "ds_add_u32 v254, v252 offset:61244\n"
    "v_exp_f32 v84, v84\n"
    "v_exp_f32 v85, v85\n"
    "v_mfma_f32_32x32x16_bf16 v[176:191], v[152:155], v[120:123], v[176:191]\n"
    "v_exp_f32 v86, v86\n"
    "v_cvt_pk_bf16_f32 v116, v84, v85\n"
    "v_exp_f32 v87, v87\n"
    "v_exp_f32 v88, v88\n"
    "v_mfma_f32_32x32x16_bf16 v[100:115], v[14:17], v[198:201], 0\n"
    "v_cvt_pk_bf16_f32 v117, v86, v87\n"
    "v_exp_f32 v89, v89\n"
    "v_exp_f32 v90, v90\n"
    "v_cvt_pk_bf16_f32 v118, v88, v89\n"
    "v_exp_f32 v91, v91\n"
    "v_mfma_f32_32x32x8_bf16 v[100:115], v[18:19], v[202:203], v[100:115]\n"
    "v_exp_f32 v92, v92\n"
    "v_cvt_pk_bf16_f32 v119, v90, v91\n"
    "v_exp_f32 v93, v93\n"
    "v_exp_f32 v94, v94\n"
    "v_mfma_f32_32x32x16_bf16 v[176:191], v[6:9], v[116:119], v[176:191]\n"
    "v_cvt_pk_bf16_f32 v120, v92, v93\n"
    "v_exp_f32 v95, v95\n"
    "v_exp_f32 v96, v96\n"
    "v_cvt_pk_bf16_f32 v121, v94, v95\n"
    "v_exp_f32 v97, v97\n"
    "v_exp_f32 v98, v98\n"
    "v_exp_f32 v99, v99\n"
    "v_cvt_pk_bf16_f32 v122, v96, v97\n"
    "v_cvt_pk_bf16_f32 v123, v98, v99\n"
    "s_setprio 2\n"
    "ds_read_b32 v124, v245 offset:61244\n"
    "s_waitcnt vmcnt(0)\n"
    "s_add_u32 s38, s93, 512\n"
    "v_mov_b32 v252, s34\n"
    "v_mov_b32 v253, s35\n"
    "buffer_store_dwordx2 v[252:253], v254, s[12:15], s38 offen sc1\n"
    "ds_read_b32 v249, v245 offset:61436\n"
    "v_exp_f32 v100, v100\n"
    "v_exp_f32 v101, v101\n"
    "v_mfma_f32_32x32x16_bf16 v[176:191], v[10:13], v[120:123], v[176:191]\n"
    "v_exp_f32 v102, v102\n"
    "v_cvt_pk_bf16_f32 v116, v100, v101\n"
    "v_exp_f32 v103, v103\n"
    "v_exp_f32 v104, v104\n"
    "v_mfma_f32_32x32x16_bf16 v[84:99], v[28:31], v[198:201], 0\n"
    "v_cvt_pk_bf16_f32 v117, v102, v103\n"
    "v_exp_f32 v105, v105\n"
    "v_exp_f32 v106, v106\n"
    "v_cvt_pk_bf16_f32 v118, v104, v105\n"
    "v_exp_f32 v107, v107\n"
    "v_mfma_f32_32x32x8_bf16 v[84:99], v[32:33], v[202:203], v[84:99]\n"
    "v_exp_f32 v108, v108\n"
    "v_cvt_pk_bf16_f32 v119, v106, v107\n"
    "v_exp_f32 v109, v109\n"
    "v_exp_f32 v110, v110\n"
    "v_mfma_f32_32x32x16_bf16 v[176:191], v[20:23], v[116:119], v[176:191]\n"
    "v_cvt_pk_bf16_f32 v120, v108, v109\n"
    "v_exp_f32 v111, v111\n"
    "v_exp_f32 v112, v112\n"
    "v_cvt_pk_bf16_f32 v121, v110, v111\n"
    "v_exp_f32 v113, v113\n"
    "v_exp_f32 v114, v114\n"
    "v_exp_f32 v115, v115\n"
    "v_cvt_pk_bf16_f32 v122, v112, v113\n"
    "v_cvt_pk_bf16_f32 v123, v114, v115\n"
    "s_waitcnt lgkmcnt(1)\n"
    ".Lkc_96_%=:\n"
    "v_readfirstlane_b32 s47, v124\n"
    "s_cmp_ge_u32 s47, 786432\n"
    "s_cbranch_scc1 .Lkd_97_%=\n"
    "ds_read_b32 v124, v245 offset:61244\n"
    "s_sleep 1\n"
    "s_waitcnt lgkmcnt(0)\n"
    "s_sub_u32 s80, s80, 1\n"
    "s_cbranch_scc0 .Lkc_96_%=\n"
    "s_mov_b32 s80, 0\n"
    ".Lkd_97_%=:\n"
    "s_waitcnt lgkmcnt(0)\n"
    ".Lkc_98_%=:\n"
    "v_readfirstlane_b32 s47, v249\n"
    "s_cmp_ge_u32 s47, 1572864\n"
    "s_cbranch_scc1 .Lkd_99_%=\n"
    "ds_read_b32 v249, v245 offset:61436\n"
    "s_sleep 1\n"
    "s_waitcnt lgkmcnt(0)\n"
    "s_sub_u32 s80, s80, 1\n"
    "s_cbranch_scc0 .Lkc_98_%=\n"
    "s_mov_b32 s80, 0\n"
    ".Lkd_99_%=:\n"
    "v_exp_f32 v84, v84\n"
    "v_exp_f32 v85, v85\n"
    "v_mfma_f32_32x32x16_bf16 v[176:191], v[24:27], v[120:123], v[176:191]\n"
    "s_and_b32 s39, s24, 3\n"
    "s_lshr_b32 s40, s24, 2\n"
    "s_mul_i32 s94, s39, 6400\n"
    "s_mul_i32 s39, s39, 800\n"
    "s_mul_i32 s41, s40, 409600\n"
    "s_mul_i32 s40, s40, 61440\n"
    "s_add_u32 s94, s94, s41\n"
    "s_add_u32 m0, s39, s40\n"
    "s_mov_b32 exec_hi, 0x3ffff\n"
    "buffer_load_dwordx4 v248, s[20:23], s94 offen lds\n"
    "s_mov_b64 exec, -1\n"
    "v_exp_f32 v86, v86\n"
    "v_cvt_pk_bf16_f32 v116, v84, v85\n"
    "v_exp_f32 v87, v87\n"
    "v_exp_f32 v88, v88\n"
    "v_mfma_f32_32x32x16_bf16 v[100:115], v[42:45], v[198:201], 0\n"
    "v_cvt_pk_bf16_f32 v117, v86, v87\n"
    "v_exp_f32 v89, v89\n"
    "v_exp_f32 v90, v90\n"
    "v_cvt_pk_bf16_f32 v118, v88, v89\n"
    "v_exp_f32 v91, v91\n"
    "v_mfma_f32_32x32x8_bf16 v[100:115], v[46:47], v[202:203], v[100:115]\n"
    "v_exp_f32 v92, v92\n"
    "v_cvt_pk_bf16_f32 v119, v90, v91\n"
    "v_exp_f32 v93, v93\n"
    "ds_write_b128 v244, v[160:163] offset:0\n"
    "v_exp_f32 v94, v94\n"
    "v_mfma_f32_32x32x16_bf16 v[176:191], v[34:37], v[116:119], v[176:191]\n"
    "v_cvt_pk_bf16_f32 v120, v92, v93\n"
    "v_exp_f32 v95, v95\n"
    "v_exp_f32 v96, v96\n"
    "v_cvt_pk_bf16_f32 v121, v94, v95\n"
    "v_exp_f32 v97, v97\n"
    "v_exp_f32 v98, v98\n"
    "v_exp_f32 v99, v99\n"
    "v_cvt_pk_bf16_f32 v122, v96, v97\n"
    "v_cvt_pk_bf16_f32 v123, v98, v99\n"
    "s_and_b32 s43, s24, 3\n"
    "s_mul_i32 s43, s43, 12800\n"
    "s_lshr_b32 s44, s24, 2\n"
    "s_mul_i32 s44, s44, 61440\n"
    "s_add_u32 s43, s43, s44\n"
    "s_add_u32 s43, s43, 9600\n"
    "s_cmp_eq_u32 s24, 3\n"
    "s_cselect_b32 s43, 161792, s43\n"
    "v_cvt_pk_bf16_f32 v220, v204, v205\n"
    "v_cvt_pk_bf16_f32 v221, v206, v207\n"
    "v_cvt_pk_bf16_f32 v222, v208, v209\n"
    "v_cvt_pk_bf16_f32 v223, v210, v211\n"
    "v_cvt_pk_bf16_f32 v224, v212, v213\n"
    "v_cvt_pk_bf16_f32 v225, v214, v215\n"
    "v_cvt_pk_bf16_f32 v226, v216, v217\n"
    "v_cvt_pk_bf16_f32 v227, v218, v219\n"
    "v_lshrrev_b32 v250, 3, v247\n"
    "v_and_b32 v250, 0x70, v250\n"
    "v_xor_b32 v250, v250, v247\n"
    "v_add_u32 v250, s43, v250\n"
    "v_xor_b32 v251, 0, v240\n"
    "v_add_u32 v251, s43, v251\n"
    "ds_write_b64 v251, v[220:221]\n"
    "v_xor_b32 v251, 32, v240\n"
    "v_add_u32 v251, s43, v251\n"
    "ds_write_b64 v251, v[222:223]\n"
    "v_xor_b32 v251, 64, v240\n"
    "v_add_u32 v251, s43, v251\n"
    "ds_write_b64 v251, v[224:225]\n"
    "v_xor_b32 v251, 96, v240\n"
    "v_add_u32 v251, s43, v251\n"
    "ds_write_b64 v251, v[226:227]\n"
    "ds_read_b128 v[228:231], v250 offset:0\n"
    "ds_read_b128 v[232:235], v250 offset:1024\n"
    "v_exp_f32 v100, v100\n"
    "v_exp_f32 v101, v101\n"
    "v_mfma_f32_32x32x16_bf16 v[176:191], v[38:41], v[120:123], v[176:191]\n"
    "ds_read_b128 v[192:195], v242 offset:144\n"
    "ds_read_b64 v[196:197], v243 offset:176\n"
    "s_add_u32 m0, m0, 12800\n"
    "s_add_u32 s94, s94, 102400\n"
    "s_mov_b32 exec_hi, 0x3ffff\n"
    "buffer_load_dwordx4 v248, s[20:23], s94 offen lds\n"
    "s_mov_b64 exec, -1\n"
    "v_exp_f32 v102, v102\n"
    "v_cvt_pk_bf16_f32 v116, v100, v101\n"
    "v_exp_f32 v103, v103\n"
    "v_exp_f32 v104, v104\n"
    "v_mfma_f32_32x32x16_bf16 v[84:99], v[56:59], v[198:201], 0\n"
    "v_cvt_pk_bf16_f32 v117, v102, v103\n"
    "v_exp_f32 v105, v105\n"
    "v_exp_f32 v106, v106\n"
    "v_cvt_pk_bf16_f32 v118, v104, v105\n"
    "v_exp_f32 v107, v107\n"
    "v_mfma_f32_32x32x8_bf16 v[84:99], v[60:61], v[202:203], v[84:99]\n"
    "v_exp_f32 v108, v108\n"
    "v_cvt_pk_bf16_f32 v119, v106, v107\n"
    "v_exp_f32 v109, v109\n"
    "ds_write_b128 v244, v[164:167] offset:1024\n"
    "v_exp_f32 v110, v110\n"
    "v_mfma_f32_32x32x16_bf16 v[176:191], v[48:51], v[116:119], v[176:191]\n"
    "v_cvt_pk_bf16_f32 v120, v108, v109\n"
    "v_exp_f32 v111, v111\n"
    "v_exp_f32 v112, v112\n"
    "v_cvt_pk_bf16_f32 v121, v110, v111\n"
    "v_exp_f32 v113, v113\n"
    "v_exp_f32 v114, v114\n"
    "v_exp_f32 v115, v115\n"
    "v_cvt_pk_bf16_f32 v122, v112, v113\n"
    "v_cvt_pk_bf16_f32 v123, v114, v115\n"
    "s_waitcnt lgkmcnt(3)\n"
    "buffer_store_dwordx4 v[228:231], v247, s[12:15], s91 offen offset:0 sc1\n"
    "buffer_store_dwordx4 v[232:235], v247, s[12:15], s91 offen offset:1024 sc1\n"
    "v_exp_f32 v84, v84\n"
    "v_exp_f32 v85, v85\n"
    "v_mfma_f32_32x32x16_bf16 v[176:191], v[52:55], v[120:123], v[176:191]\n"
    "v_exp_f32 v86, v86\n"
    "v_cvt_pk_bf16_f32 v116, v84, v85\n"
    "v_exp_f32 v87, v87\n"
    "v_exp_f32 v88, v88\n"
    "v_mfma_f32_32x32x16_bf16 v[100:115], v[70:73], v[198:201], 0\n"
    "v_cvt_pk_bf16_f32 v117, v86, v87\n"
    "v_exp_f32 v89, v89\n"
    "v_exp_f32 v90, v90\n"
    "v_cvt_pk_bf16_f32 v118, v88, v89\n"
    "v_exp_f32 v91, v91\n"
    "v_mfma_f32_32x32x8_bf16 v[100:115], v[74:75], v[202:203], v[100:115]\n"
    "v_exp_f32 v92, v92\n"
    "v_cvt_pk_bf16_f32 v119, v90, v91\n"
    "v_exp_f32 v93, v93\n"
    "ds_write_b128 v244, v[168:171] offset:2048\n"
    "v_exp_f32 v94, v94\n"
    "v_mfma_f32_32x32x16_bf16 v[176:191], v[62:65], v[116:119], v[176:191]\n"
    "v_cvt_pk_bf16_f32 v120, v92, v93\n"
    "v_exp_f32 v95, v95\n"
    "v_exp_f32 v96, v96\n"
    "v_cvt_pk_bf16_f32 v121, v94, v95\n"
    "v_exp_f32 v97, v97\n"
    "v_exp_f32 v98, v98\n"
    "v_exp_f32 v99, v99\n"
    "v_cvt_pk_bf16_f32 v122, v96, v97\n"
    "v_cvt_pk_bf16_f32 v123, v98, v99\n"
    "v_exp_f32 v100, v100\n"
    "v_exp_f32 v101, v101\n"
    "v_mfma_f32_32x32x16_bf16 v[176:191], v[66:69], v[120:123], v[176:191]\n"
    "s_add_u32 m0, m0, 12800\n"
    "s_add_u32 s94, s94, 102400\n"
    "s_mov_b32 exec_hi, 0x3ffff\n"
    "buffer_load_dwordx4 v248, s[20:23], s94 offen lds\n"
    "s_mov_b64 exec, -1\n"
    "v_exp_f32 v102, v102\n"
    "v_cvt_pk_bf16_f32 v116, v100, v101\n"
    "v_exp_f32 v103, v103\n"
    "v_exp_f32 v104, v104\n"
    "s_waitcnt lgkmcnt(2)\n"
    "v_mfma_f32_32x32x16_bf16 v[84:99], v[128:131], v[192:195], 0\n"
    "v_cvt_pk_bf16_f32 v117, v102, v103\n"
    "v_exp_f32 v105, v105\n"
    "v_exp_f32 v106, v106\n"
    "v_cvt_pk_bf16_f32 v118, v104, v105\n"
    "v_exp_f32 v107, v107\n"
    "v_mfma_f32_32x32x8_bf16 v[84:99], v[132:133], v[196:197], v[84:99]\n"
    "v_exp_f32 v108, v108\n"
    "v_cvt_pk_bf16_f32 v119, v106, v107\n"
    "v_exp_f32 v109, v109\n"
    "v_exp_f32 v110, v110\n"
    "v_mfma_f32_32x32x16_bf16 v[176:191], v[76:79], v[116:119], v[176:191]\n"
    "v_cvt_pk_bf16_f32 v120, v108, v109\n"
    "v_exp_f32 v111, v111\n"
    "v_exp_f32 v112, v112\n"
    "v_cvt_pk_bf16_f32 v121, v110, v111\n"
    "v_exp_f32 v113, v113\n"
    "v_exp_f32 v114, v114\n"
    "v_exp_f32 v115, v115\n"
    "v_cvt_pk_bf16_f32 v122, v112, v113\n"
    "v_cvt_pk_bf16_f32 v123, v114, v115\n"
    "s_setprio 1\n"
    "v_exp_f32 v84, v84\n"
    "v_exp_f32 v85, v85\n"
    "v_mfma_f32_32x32x16_bf16 v[176:191], v[80:83], v[120:123], v[176:191]\n"
    "s_add_u32 m0, m0, 12800\n"
    "s_add_u32 s94, s94, 102400\n"
    "s_mov_b32 exec_hi, 0x3ffff\n"
    "buffer_load_dwordx4 v248, s[20:23], s94 offen lds\n"
    "s_mov_b64 exec, -1\n"
    "v_exp_f32 v86, v86\n"
    "v_cvt_pk_bf16_f32 v116, v84, v85\n"
    "v_exp_f32 v87, v87\n"
    "v_exp_f32 v88, v88\n"
    "v_mfma_f32_32x32x16_bf16 v[100:115], v[142:145], v[192:195], 0\n"
    "v_cvt_pk_bf16_f32 v117, v86, v87\n"
    "v_exp_f32 v89, v89\n"
    "v_exp_f32 v90, v90\n"
    "v_cvt_pk_bf16_f32 v118, v88, v89\n"
    "v_exp_f32 v91, v91\n"
    "v_mfma_f32_32x32x8_bf16 v[100:115], v[146:147], v[196:197], v[100:115]\n"
    "v_exp_f32 v92, v92\n"
    "v_cvt_pk_bf16_f32 v119, v90, v91\n"
    "v_exp_f32 v93, v93\n"
    "ds_write_b128 v244, v[176:179] offset:24576\n"
    "v_exp_f32 v94, v94\n"
    "v_mfma_f32_32x32x16_bf16 v[160:175], v[134:137], v[116:119], 0\n"
    "v_cvt_pk_bf16_f32 v120, v92, v93\n"
    "v_exp_f32 v95, v95\n"
    "v_exp_f32 v96, v96\n"
    "v_cvt_pk_bf16_f32 v121, v94, v95\n"
    "v_exp_f32 v97, v97\n"
    "v_exp_f32 v98, v98\n"
    "v_exp_f32 v99, v99\n"
    "v_cvt_pk_bf16_f32 v122, v96, v97\n"
    "v_cvt_pk_bf16_f32 v123, v98, v99\n"
    "s_waitcnt vmcnt(2)\n"
    "v_mov_b32 v252, s34\n"
    "v_mov_b32 v253, s35\n"
    "buffer_store_dwordx2 v[252:253], v254, s[12:15], s3 offen sc1\n"
    "v_exp_f32 v100, v100\n"
    "v_exp_f32 v101, v101\n"
    "v_mfma_f32_32x32x16_bf16 v[160:175], v[138:141], v[120:123], v[160:175]\n"
    "s_sub_u32 m0, m0, 35200\n"
    "s_sub_u32 s94, s94, 281600\n"
    "s_mov_b32 exec_hi, 0x3ffff\n"
    "buffer_load_dwordx4 v248, s[20:23], s94 offen lds\n"
    "s_mov_b64 exec, -1\n"
    "v_exp_f32 v102, v102\n"
    "v_cvt_pk_bf16_f32 v116, v100, v101\n"
    "v_exp_f32 v103, v103\n"
    "v_exp_f32 v104, v104\n"
    "v_mfma_f32_32x32x16_bf16 v[84:99], v[0:3], v[192:195], 0\n"
    "v_cvt_pk_bf16_f32 v117, v102, v103\n"
    "v_exp_f32 v105, v105\n"
    "v_exp_f32 v106, v106\n"
    "v_cvt_pk_bf16_f32 v118, v104, v105\n"
    "v_exp_f32 v107, v107\n"
    "v_mfma_f32_32x32x8_bf16 v[84:99], v[4:5], v[196:197], v[84:99]\n"
    "v_exp_f32 v108, v108\n"
    "v_cvt_pk_bf16_f32 v119, v106, v107\n"
    "v_exp_f32 v109, v109\n"
    "ds_write_b128 v244, v[180:183] offset:25600\n"
    "v_exp_f32 v110, v110\n"
    "v_mfma_f32_32x32x16_bf16 v[160:175], v[148:151], v[116:119], v[160:175]\n"
    "v_cvt_pk_bf16_f32 v120, v108, v109\n"
    "v_exp_f32 v111, v111\n"
    "v_exp_f32 v112, v112\n"
    "v_cvt_pk_bf16_f32 v121, v110, v111\n"
    "v_exp_f32 v113, v113\n"
    "v_exp_f32 v114, v114\n"
    "v_exp_f32 v115, v115\n"
    "v_cvt_pk_bf16_f32 v122, v112, v113\n"
    "v_cvt_pk_bf16_f32 v123, v114, v115\n"
    "v_exp_f32 v84, v84\n"
    "v_exp_f32 v85, v85\n"
    "v_mfma_f32_32x32x16_bf16 v[160:175], v[152:155], v[120:123], v[160:175]\n"
    "s_add_u32 m0, m0, 12800\n"
    "s_add_u32 s94, s94, 102400\n"
    "s_mov_b32 exec_hi, 0x3ffff\n"
    "buffer_load_dwordx4 v248, s[20:23], s94 offen lds\n"
    "s_mov_b64 exec, -1\n"
    "v_exp_f32 v86, v86\n"
    "v_cvt_pk_bf16_f32 v116, v84, v85\n"
    "v_exp_f32 v87, v87\n"
    "v_exp_f32 v88, v88\n"
    "v_mfma_f32_32x32x16_bf16 v[100:115], v[14:17], v[192:195], 0\n"
    "v_cvt_pk_bf16_f32 v117, v86, v87\n"
    "v_exp_f32 v89, v89\n"
    "v_exp_f32 v90, v90\n"
    "v_cvt_pk_bf16_f32 v118, v88, v89\n"
    "v_exp_f32 v91, v91\n"
    "v_mfma_f32_32x32x8_bf16 v[100:115], v[18:19], v[196:197], v[100:115]\n"
    "v_exp_f32 v92, v92\n"
    "v_cvt_pk_bf16_f32 v119, v90, v91\n"
    "v_exp_f32 v93, v93\n"
    "ds_write_b128 v244, v[184:187] offset:26624\n"
    "v_exp_f32 v94, v94\n"
    "v_mfma_f32_32x32x16_bf16 v[160:175], v[6:9], v[116:119], v[160:175]\n"
    "v_cvt_pk_bf16_f32 v120, v92, v93\n"
    "v_exp_f32 v95, v95\n"
    "v_exp_f32 v96, v96\n"
    "v_cvt_pk_bf16_f32 v121, v94, v95\n"
    "v_exp_f32 v97, v97\n"
    "v_exp_f32 v98, v98\n"
    "v_exp_f32 v99, v99\n"
    "v_cvt_pk_bf16_f32 v122, v96, v97\n"
    "v_cvt_pk_bf16_f32 v123, v98, v99\n"
    "s_setprio 0\n"
    "s_waitcnt lgkmcnt(0)\n"
    "v_mov_b32 v252, 0x10000\n"
    "ds_add_u32 v254, v252 offset:61052\n"
    "v_exp_f32 v100, v100\n"
    "v_exp_f32 v101, v101\n"
    "v_mfma_f32_32x32x16_bf16 v[160:175], v[10:13], v[120:123], v[160:175]\n"
    "s_add_u32 m0, m0, 12800\n"
    "s_add_u32 s94, s94, 102400\n"
    "s_mov_b32 exec_hi, 0x3ffff\n"
    "buffer_load_dwordx4 v248, s[20:23], s94 offen lds\n"
    "s_mov_b64 exec, -1\n"
    "v_exp_f32 v102, v102\n"
    "v_cvt_pk_bf16_f32 v116, v100, v101\n"
    "v_exp_f32 v103, v103\n"
    "v_exp_f32 v104, v104\n"
    "v_mfma_f32_32x32x16_bf16 v[84:99], v[28:31], v[192:195], 0\n"
    "v_cvt_pk_bf16_f32 v117, v102, v103\n"
    "v_exp_f32 v105, v105\n"
    "v_exp_f32 v106, v106\n"
    "v_cvt_pk_bf16_f32 v118, v104, v105\n"
    "v_exp_f32 v107, v107\n"
    "v_mfma_f32_32x32x8_bf16 v[84:99], v[32:33], v[196:197], v[84:99]\n"
    "v_exp_f32 v108, v108\n"
    "v_cvt_pk_bf16_f32 v119, v106, v107\n"
    "v_exp_f32 v109, v109\n"
    "v_exp_f32 v110, v110\n"
    "v_mfma_f32_32x32x16_bf16 v[160:175], v[20:23], v[116:119], v[160:175]\n"
    "v_cvt_pk_bf16_f32 v120, v108, v109\n"
    "v_exp_f32 v111, v111\n"
    "v_exp_f32 v112, v112\n"
    "v_cvt_pk_bf16_f32 v121, v110, v111\n"
    "v_exp_f32 v113, v113\n"
    "v_exp_f32 v114, v114\n"
    "v_exp_f32 v115, v115\n"
    "v_cvt_pk_bf16_f32 v122, v112, v113\n"
    "v_cvt_pk_bf16_f32 v123, v114, v115\n"
    "ds_read_b32 v249, v245 offset:61052\n"
    "v_exp_f32 v84, v84\n"
    "v_exp_f32 v85, v85\n"
    "v_mfma_f32_32x32x16_bf16 v[160:175], v[24:27], v[120:123], v[160:175]\n"
    "s_add_u32 m0, m0, 12800\n"
    "s_add_u32 s94, s94, 102400\n"
    "s_mov_b32 exec_hi, 0x3ffff\n"
    "buffer_load_dwordx4 v248, s[20:23], s94 offen lds\n"
    "s_mov_b64 exec, -1\n"
    "v_exp_f32 v86, v86\n"
    "v_cvt_pk_bf16_f32 v116, v84, v85\n"
    "v_exp_f32 v87, v87\n"
    "v_exp_f32 v88, v88\n"
    "v_mfma_f32_32x32x16_bf16 v[100:115], v[42:45], v[192:195], 0\n"
    "v_cvt_pk_bf16_f32 v117, v86, v87\n"
    "v_exp_f32 v89, v89\n"
    "v_exp_f32 v90, v90\n"
    "v_cvt_pk_bf16_f32 v118, v88, v89\n"
    "v_exp_f32 v91, v91\n"
    "v_mfma_f32_32x32x8_bf16 v[100:115], v[46:47], v[196:197], v[100:115]\n"
    "v_exp_f32 v92, v92\n"
    "v_cvt_pk_bf16_f32 v119, v90, v91\n"
    "v_exp_f32 v93, v93\n"
    "v_exp_f32 v94, v94\n"
    "v_mfma_f32_32x32x16_bf16 v[160:175], v[34:37], v[116:119], v[160:175]\n"
    "v_cvt_pk_bf16_f32 v120, v92, v93\n"
    "v_exp_f32 v95, v95\n"
    "v_exp_f32 v96, v96\n"
    "v_cvt_pk_bf16_f32 v121, v94, v95\n"
    "v_exp_f32 v97, v97\n"
    "v_exp_f32 v98, v98\n"
    "v_exp_f32 v99, v99\n"
    "v_cvt_pk_bf16_f32 v122, v96, v97\n"
    "v_cvt_pk_bf16_f32 v123, v98, v99\n"
    "s_waitcnt lgkmcnt(0)\n"
    ".Lkc_100_%=:\n"
    "v_readfirstlane_b32 s47, v249\n"
    "s_cmp_ge_u32 s47, 1572864\n"
    "s_cbranch_scc1 .Lkd_101_%=\n"
    "ds_read_b32 v249, v245 offset:61052\n"
    "s_sleep 1\n"
    "s_waitcnt lgkmcnt(0)\n"
    "s_sub_u32 s80, s80, 1\n"
    "s_cbranch_scc0 .Lkc_100_%=\n"
    "s_mov_b32 s80, 0\n"
    ".Lkd_101_%=:\n"
    "s_setprio 3\n"
    "ds_read_b128 v[176:179], v246 offset:0\n"
    "ds_read_b128 v[180:183], v246 offset:3072\n"
    "ds_read_b128 v[184:187], v246 offset:6144\n"
    "ds_read_b128 v[188:191], v246 offset:9216\n"
    "ds_read_b128 v[124:127], v246 offset:12288\n"
    "ds_read_b128 v[156:159], v246 offset:15360\n"
    "ds_read_b128 v[236:239], v246 offset:18432\n"
    "v_exp_f32 v100, v100\n"
    "v_exp_f32 v101, v101\n"
    "v_mfma_f32_32x32x16_bf16 v[160:175], v[38:41], v[120:123], v[160:175]\n"
    "ds_read_b128 v[198:201], v242 offset:6288\n"
    "ds_read_b64 v[202:203], v243 offset:6320\n"
    "v_exp_f32 v102, v102\n"
    "v_cvt_pk_bf16_f32 v116, v100, v101\n"
    "v_exp_f32 v103, v103\n"
    "v_exp_f32 v104, v104\n"
    "v_mfma_f32_32x32x16_bf16 v[84:99], v[56:59], v[192:195], 0\n"
    "v_cvt_pk_bf16_f32 v117, v102, v103\n"
    "v_exp_f32 v105, v105\n"
    "v_exp_f32 v106, v106\n"
    "v_cvt_pk_bf16_f32 v118, v104, v105\n"
    "v_exp_f32 v107, v107\n"
    "v_mfma_f32_32x32x8_bf16 v[84:99], v[60:61], v[196:197], v[84:99]\n"
    "v_exp_f32 v108, v108\n"
    "v_cvt_pk_bf16_f32 v119, v106, v107\n"
    "v_exp_f32 v109, v109\n"
    "v_exp_f32 v110, v110\n"
    "v_mfma_f32_32x32x16_bf16 v[160:175], v[48:51], v[116:119], v[160:175]\n"
    "v_cvt_pk_bf16_f32 v120, v108, v109\n"
    "v_exp_f32 v111, v111\n"
    "v_exp_f32 v112, v112\n"
    "v_cvt_pk_bf16_f32 v121, v110, v111\n"
    "v_exp_f32 v113, v113\n"
    "v_exp_f32 v114, v114\n"
    "v_exp_f32 v115, v115\n"
    "v_cvt_pk_bf16_f32 v122, v112, v113\n"
    "v_cvt_pk_bf16_f32 v123, v114, v115\n"
    "s_waitcnt lgkmcnt(7)\n"
    "v_pk_add_f32 v[176:177], v[176:177], v[180:181]\n"
    "v_pk_add_f32 v[178:179], v[178:179], v[182:183]\n"
    "ds_read_b128 v[180:183], v246 offset:21504\n"
    "s_waitcnt lgkmcnt(4)\n"
    "v_pk_add_f32 v[184:185], v[184:185], v[188:189]\n"
    "v_pk_add_f32 v[186:187], v[186:187], v[190:191]\n"
    "v_pk_add_f32 v[124:125], v[124:125], v[156:157]\n"
    "v_pk_add_f32 v[126:127], v[126:127], v[158:159]\n"
    "v_pk_add_f32 v[176:177], v[176:177], v[184:185]\n"
    "v_pk_add_f32 v[178:179], v[178:179], v[186:187]\n"
    "v_lshrrev_b32 v208, 4, v247\n"
    "v_and_b32 v209, 1, v208\n"
    "v_bfe_u32 v210, v208, 1, 2\n"
    "v_lshl_add_u32 v209, v210, 3, v209\n"
    "v_lshlrev_b32 v209, 3, v209\n"
    "s_and_b32 s88, s24, 3\n"
    "s_lshr_b32 s89, s24, 2\n"
    "s_lshl_b32 s43, s25, 11\n"
    "s_lshl_b32 s44, s89, 8\n"
    "s_add_u32 s43, s43, s44\n"
    "s_lshl_b32 s44, s88, 4\n"
    "s_add_u32 s43, s43, s44\n"
    "s_add_u32 s43, s43, 10027008\n"
    "v_add_u32 v209, s43, v209\n"
    "v_bfe_u32 v210, v208, 3, 1\n"
    "v_lshl_add_u32 v210, v210, 9, v209\n"
    "s_lshl_b32 s81, s25, 4\n"
    "s_add_u32 s81, s81, s88\n"
    "s_mul_i32 s81, s81, 6144\n"
    "s_mul_i32 s44, s89, 3072\n"
    "s_add_u32 s81, s81, s44\n"
    "s_add_u32 s81, s81, 6881280\n"
    "buffer_load_dwordx2 v[206:207], v210, s[12:15], 0 offen sc1\n"
    "v_exp_f32 v84, v84\n"
    "v_exp_f32 v85, v85\n"
    "v_mfma_f32_32x32x16_bf16 v[160:175], v[52:55], v[120:123], v[160:175]\n"
    "s_sub_u32 m0, m0, 35200\n"
    "s_sub_u32 s94, s94, 281600\n"
    "s_mov_b32 exec_hi, 0x3ffff\n"
    "buffer_load_dwordx4 v248, s[20:23], s94 offen lds\n"
    "s_mov_b64 exec, -1\n"
    "v_exp_f32 v86, v86\n"
    "v_cvt_pk_bf16_f32 v116, v84, v85\n"
    "v_exp_f32 v87, v87\n"
    "v_exp_f32 v88, v88\n"
    "v_mfma_f32_32x32x16_bf16 v[100:115], v[70:73], v[192:195], 0\n"
    "v_cvt_pk_bf16_f32 v117, v86, v87\n"
    "v_exp_f32 v89, v89\n"
    "v_exp_f32 v90, v90\n"
    "v_cvt_pk_bf16_f32 v118, v88, v89\n"
    "v_exp_f32 v91, v91\n"
    "v_mfma_f32_32x32x8_bf16 v[100:115], v[74:75], v[196:197], v[100:115]\n"
    "v_exp_f32 v92, v92\n"
    "v_cvt_pk_bf16_f32 v119, v90, v91\n"
    "v_exp_f32 v93, v93\n"
    "v_exp_f32 v94, v94\n"
    "v_mfma_f32_32x32x16_bf16 v[160:175], v[62:65], v[116:119], v[160:175]\n"
    "v_cvt_pk_bf16_f32 v120, v92, v93\n"
    "v_exp_f32 v95, v95\n"
    "v_exp_f32 v96, v96\n"
    "v_cvt_pk_bf16_f32 v121, v94, v95\n"
    "v_exp_f32 v97, v97\n"
    "v_exp_f32 v98, v98\n"
    "v_exp_f32 v99, v99\n"
    "v_cvt_pk_bf16_f32 v122, v96, v97\n"
    "v_cvt_pk_bf16_f32 v123, v98, v99\n"
    "s_waitcnt lgkmcnt(0)\n"
    "v_pk_add_f32 v[236:237], v[236:237], v[180:181]\n"
    "v_pk_add_f32 v[238:239], v[238:239], v[182:183]\n"
    "v_pk_add_f32 v[176:177], v[176:177], v[124:125]\n"
    "v_pk_add_f32 v[178:179], v[178:179], v[126:127]\n"
    "v_pk_add_f32 v[176:177], v[176:177], v[236:237]\n"
    "v_pk_add_f32 v[178:179], v[178:179], v[238:239]\n"
    "v_bfe_u32 v180, v247, 2, 5\n"
    "v_add_u32 v180, 160, v180\n"
    "ds_bpermute_b32 v180, v180, v176\n"
    "v_mov_b32 v252, 0x10000\n"
    "ds_add_u32 v254, v252 offset:61436\n"
    "v_exp_f32 v100, v100\n"
    "v_exp_f32 v101, v101\n"
    "v_mfma_f32_32x32x16_bf16 v[160:175], v[66:69], v[120:123], v[160:175]\n"
    "s_add_u32 m0, m0, 12800\n"
    "s_add_u32 s94, s94, 102400\n"
    "s_mov_b32 exec_hi, 0x3ffff\n"
    "buffer_load_dwordx4 v248, s[20:23], s94 offen lds\n"
    "s_mov_b64 exec, -1\n"
    "v_exp_f32 v102, v102\n"
    "v_cvt_pk_bf16_f32 v116, v100, v101\n"
    "v_exp_f32 v103, v103\n"
    "v_exp_f32 v104, v104\n"
    "v_mfma_f32_32x32x16_bf16 v[84:99], v[128:131], v[198:201], 0\n"
    "v_cvt_pk_bf16_f32 v117, v102, v103\n"
    "v_exp_f32 v105, v105\n"
    "v_exp_f32 v106, v106\n"
    "v_cvt_pk_bf16_f32 v118, v104, v105\n"
    "v_exp_f32 v107, v107\n"
    "v_mfma_f32_32x32x8_bf16 v[84:99], v[132:133], v[202:203], v[84:99]\n"
    "v_exp_f32 v108, v108\n"
    "v_cvt_pk_bf16_f32 v119, v106, v107\n"
    "v_exp_f32 v109, v109\n"
    "v_exp_f32 v110, v110\n"
    "v_mfma_f32_32x32x16_bf16 v[160:175], v[76:79], v[116:119], v[160:175]\n"
    "v_cvt_pk_bf16_f32 v120, v108, v109\n"
    "v_exp_f32 v111, v111\n"
    "v_exp_f32 v112, v112\n"
    "v_cvt_pk_bf16_f32 v121, v110, v111\n"
    "v_exp_f32 v113, v113\n"
    "v_exp_f32 v114, v114\n"
    "v_exp_f32 v115, v115\n"
    "v_cvt_pk_bf16_f32 v122, v112, v113\n"
    "v_cvt_pk_bf16_f32 v123, v114, v115\n"
    "s_waitcnt lgkmcnt(1)\n"
    "v_rcp_f32 v180, v180\n"
    "s_add_u32 s38, s92, 49152\n"
    "v_cndmask_b32 v180, v180, 0, s[96:97]\n"
    "v_pk_mul_f32 v[176:177], v[176:177], v[180:181] op_sel_hi:[1,0]\n"
    "v_pk_mul_f32 v[178:179], v[178:179], v[180:181] op_sel_hi:[1,0]\n"
    "v_cvt_pk_bf16_f32 v124, v176, v177\n"
    "v_cvt_pk_bf16_f32 v125, v178, v179\n"
    "buffer_store_dwordx2 v[124:125], v241, s[12:15], s38 offen sc1\n"
    "v_exp_f32 v84, v84\n"
    "v_exp_f32 v85, v85\n"
    "v_mfma_f32_32x32x16_bf16 v[160:175], v[80:83], v[120:123], v[160:175]\n"
    "s_add_u32 m0, m0, 12800\n"
    "s_add_u32 s94, s94, 102400\n"
    "s_mov_b32 exec_hi, 0x3ffff\n"
    "buffer_load_dwordx4 v248, s[20:23], s94 offen lds\n"
    "s_mov_b64 exec, -1\n"
    "v_exp_f32 v86, v86\n"
    "v_cvt_pk_bf16_f32 v116, v84, v85\n"
    "v_exp_f32 v87, v87\n"
    "v_exp_f32 v88, v88\n"
    "v_mfma_f32_32x32x16_bf16 v[100:115], v[142:145], v[198:201], 0\n"
    "v_cvt_pk_bf16_f32 v117, v86, v87\n"
    "v_exp_f32 v89, v89\n"
    "v_exp_f32 v90, v90\n"
    "v_cvt_pk_bf16_f32 v118, v88, v89\n"
    "v_exp_f32 v91, v91\n"
    "v_mfma_f32_32x32x8_bf16 v[100:115], v[146:147], v[202:203], v[100:115]\n"
    "v_exp_f32 v92, v92\n"
    "v_cvt_pk_bf16_f32 v119, v90, v91\n"
    "v_exp_f32 v93, v93\n"
    "v_exp_f32 v94, v94\n"
    "v_mfma_f32_32x32x16_bf16 v[176:191], v[134:137], v[116:119], 0\n"
    "v_cvt_pk_bf16_f32 v120, v92, v93\n"
    "v_exp_f32 v95, v95\n"
    "v_exp_f32 v96, v96\n"
    "v_cvt_pk_bf16_f32 v121, v94, v95\n"
    "v_exp_f32 v97, v97\n"
    "v_exp_f32 v98, v98\n"
    "v_exp_f32 v99, v99\n"
    "v_cvt_pk_bf16_f32 v122, v96, v97\n"
    "v_cvt_pk_bf16_f32 v123, v98, v99\n"
    "s_setprio 2\n"
    "s_waitcnt vmcnt(4)\n"
    "v_and_b32 v208, 0xffff0000, v206\n"
    "v_cmp_eq_u32 s[86:87], s35, v207\n"
    "v_cmp_eq_u32 vcc, s34, v208\n"
    "s_and_b64 vcc, vcc, s[86:87]\n"
    "s_cmp_eq_u64 vcc, -1\n"
    "s_cbranch_scc1 .LkL_102_%=\n"
    ".Lkp_103_%=:\n"
    "buffer_load_dwordx2 v[206:207], v210, s[12:15], 0 offen sc1\n"
    "s_waitcnt vmcnt(0)\n"
    "v_and_b32 v208, 0xffff0000, v206\n"
    "v_cmp_eq_u32 s[86:87], s35, v207\n"
    "v_cmp_eq_u32 vcc, s34, v208\n"
    "s_and_b64 vcc, vcc, s[86:87]\n"
    "s_cmp_eq_u64 vcc, -1\n"
    "s_cbranch_scc1 .Lkd_104_%=\n"
    "s_sleep 5\n"
    "s_sub_u32 s80, s80, 1\n"
    "s_cbranch_scc0 .Lkp_103_%=\n"
    "s_mov_b32 s80, 0\n"
    ".Lkd_104_%=:\n"
    ".LkL_102_%=:\n"
    "v_exp_f32 v100, v100\n"
    "v_exp_f32 v101, v101\n"
    "v_mfma_f32_32x32x16_bf16 v[176:191], v[138:141], v[120:123], v[176:191]\n"
    "s_add_u32 m0, m0, 12800\n"
    "s_add_u32 s94, s94, 102400\n"
    "s_mov_b32 exec_hi, 0x3ffff\n"
    "buffer_load_dwordx4 v248, s[20:23], s94 offen lds\n"
    "s_mov_b64 exec, -1\n"
    "v_exp_f32 v102, v102\n"
    "v_cvt_pk_bf16_f32 v116, v100, v101\n"
    "v_exp_f32 v103, v103\n"
    "v_exp_f32 v104, v104\n"
    "v_mfma_f32_32x32x16_bf16 v[84:99], v[0:3], v[198:201], 0\n"
    "v_cvt_pk_bf16_f32 v117, v102, v103\n"
    "v_exp_f32 v105, v105\n"
    "v_exp_f32 v106, v106\n"
    "v_cvt_pk_bf16_f32 v118, v104, v105\n"
    "v_exp_f32 v107, v107\n"
    "v_mfma_f32_32x32x8_bf16 v[84:99], v[4:5], v[202:203], v[84:99]\n"
    "v_exp_f32 v108, v108\n"
    "v_cvt_pk_bf16_f32 v119, v106, v107\n"
    "v_exp_f32 v109, v109\n"
    "v_exp_f32 v110, v110\n"
    "v_mfma_f32_32x32x16_bf16 v[176:191], v[148:151], v[116:119], v[176:191]\n"
    "v_cvt_pk_bf16_f32 v120, v108, v109\n"
    "v_exp_f32 v111, v111\n"
    "v_exp_f32 v112, v112\n"
    "v_cvt_pk_bf16_f32 v121, v110, v111\n"
    "v_exp_f32 v113, v113\n"
    "v_exp_f32 v114, v114\n"
    "v_exp_f32 v115, v115\n"
    "v_cvt_pk_bf16_f32 v122, v112, v113\n"
    "v_cvt_pk_bf16_f32 v123, v114, v115\n"
    "v_exp_f32 v84, v84\n"
    "v_exp_f32 v85, v85\n"
    "v_mfma_f32_32x32x16_bf16 v[176:191], v[152:155], v[120:123], v[176:191]\n"
    "s_sub_u32 m0, m0, 35200\n"
    "s_sub_u32 s94, s94, 281600\n"
    "s_mov_b32 exec_hi, 0x3ffff\n"
    "buffer_load_dwordx4 v248, s[20:23], s94 offen lds\n"
    "s_mov_b64 exec, -1\n"
    "v_exp_f32 v86, v86\n"
    "v_cvt_pk_bf16_f32 v116, v84, v85\n"
    "v_exp_f32 v87, v87\n"
    "v_exp_f32 v88, v88\n"
    "v_mfma_f32_32x32x16_bf16 v[100:115], v[14:17], v[198:201], 0\n"
    "v_cvt_pk_bf16_f32 v117, v86, v87\n"
    "v_exp_f32 v89, v89\n"
    "v_exp_f32 v90, v90\n"
    "v_cvt_pk_bf16_f32 v118, v88, v89\n"
    "v_exp_f32 v91, v91\n"
    "v_mfma_f32_32x32x8_bf16 v[100:115], v[18:19], v[202:203], v[100:115]\n"
    "v_exp_f32 v92, v92\n"
    "v_cvt_pk_bf16_f32 v119, v90, v91\n"
    "v_exp_f32 v93, v93\n"
    "v_exp_f32 v94, v94\n"
    "v_mfma_f32_32x32x16_bf16 v[176:191], v[6:9], v[116:119], v[176:191]\n"
    "v_cvt_pk_bf16_f32 v120, v92, v93\n"
    "v_exp_f32 v95, v95\n"
    "v_exp_f32 v96, v96\n"
    "v_cvt_pk_bf16_f32 v121, v94, v95\n"
    "v_exp_f32 v97, v97\n"
    "v_exp_f32 v98, v98\n"
    "v_exp_f32 v99, v99\n"
    "v_cvt_pk_bf16_f32 v122, v96, v97\n"
    "v_cvt_pk_bf16_f32 v123, v98, v99\n"
    "s_add_u32 s38, s81, 0\n"
    "buffer_load_dwordx4 v[212:215], v247, s[12:15], s38 offen offset:0 sc1\n"
    "buffer_load_dwordx4 v[216:219], v247, s[12:15], s38 offen offset:1024 sc1\n"
    "buffer_load_dwordx4 v[220:223], v247, s[12:15], s38 offen offset:2048 sc1\n"
    "v_lshrrev_b32 v144, 4, v247\n"
    "s_cmp_lt_u32 s24, 2\n"
    "s_cselect_b32 s41, 64, 0\n"
    "s_cmp_lt_u32 s25, 16\n"
    "s_cselect_b32 s42, 0, 1\n"
    "s_mul_i32 s40, s26, 80\n"
    "s_lshl_b32 s43, s24, 3\n"
    "s_add_u32 s40, s40, s43\n"
    "v_bfe_u32 v145, v144, 4, 1\n"
    "v_mul_u32_u24 v145, s41, v145\n"
    "v_bfe_u32 v146, v144, 1, 3\n"
    "v_add3_u32 v145, v145, v146, s40\n"
    "v_and_b32 v146, s42, v144\n"
    "v_lshl_add_u32 v145, v145, 2, v146\n"
    "s_lshl_b32 s42, s42, 3\n"
    "s_add_u32 s42, s42, 278528\n"
    "v_lshl_add_u32 v149, v145, 3, s42\n"
    "buffer_load_dwordx2 v[150:151], v149, s[12:15], 0 offen sc1\n"
    "s_mov_b32 s98, s43\n"
    "s_waitcnt vmcnt(7)\n"
    "s_add_u32 s38, s93, 1024\n"
    "v_mov_b32 v252, s34\n"
    "v_mov_b32 v253, s35\n"
    "buffer_store_dwordx2 v[252:253], v254, s[12:15], s38 offen sc1\n"
    "ds_read_b32 v249, v245 offset:61436\n"
    "v_exp_f32 v100, v100\n"
    "v_exp_f32 v101, v101\n"
    "v_mfma_f32_32x32x16_bf16 v[176:191], v[10:13], v[120:123], v[176:191]\n"
    "s_add_u32 m0, m0, 12800\n"
    "s_add_u32 s94, s94, 102400\n"
    "s_mov_b32 exec_hi, 0x3ffff\n"
    "buffer_load_dwordx4 v248, s[20:23], s94 offen lds\n"
    "s_mov_b64 exec, -1\n"
    "v_exp_f32 v102, v102\n"
    "v_cvt_pk_bf16_f32 v116, v100, v101\n"
    "v_exp_f32 v103, v103\n"
    "v_exp_f32 v104, v104\n"
    "v_mfma_f32_32x32x16_bf16 v[84:99], v[28:31], v[198:201], 0\n"
    "v_cvt_pk_bf16_f32 v117, v102, v103\n"
    "v_exp_f32 v105, v105\n"
    "v_exp_f32 v106, v106\n"
    "v_cvt_pk_bf16_f32 v118, v104, v105\n"
    "v_exp_f32 v107, v107\n"
    "v_mfma_f32_32x32x8_bf16 v[84:99], v[32:33], v[202:203], v[84:99]\n"
    "v_exp_f32 v108, v108\n"
    "v_cvt_pk_bf16_f32 v119, v106, v107\n"
    "v_exp_f32 v109, v109\n"
    "v_exp_f32 v110, v110\n"
    "v_mfma_f32_32x32x16_bf16 v[176:191], v[20:23], v[116:119], v[176:191]\n"
    "v_cvt_pk_bf16_f32 v120, v108, v109\n"
    "v_exp_f32 v111, v111\n"
    "v_exp_f32 v112, v112\n"
    "v_cvt_pk_bf16_f32 v121, v110, v111\n"
    "v_exp_f32 v113, v113\n"
    "v_exp_f32 v114, v114\n"
    "v_exp_f32 v115, v115\n"
    "v_cvt_pk_bf16_f32 v122, v112, v113\n"
    "v_cvt_pk_bf16_f32 v123, v114, v115\n"
    "s_setprio 1\n"
    "s_waitcnt lgkmcnt(0)\n"
    ".Lkc_105_%=:\n"
    "v_readfirstlane_b32 s47, v249\n"
    "s_cmp_ge_u32 s47, 2097152\n"
    "s_cbranch_scc1 .Lkd_106_%=\n"
    "ds_read_b32 v249, v245 offset:61436\n"
    "s_sleep 1\n"
    "s_waitcnt lgkmcnt(0)\n"
    "s_sub_u32 s80, s80, 1\n"
    "s_cbranch_scc0 .Lkc_105_%=\n"
    "s_mov_b32 s80, 0\n"
    ".Lkd_106_%=:\n"
    "v_exp_f32 v84, v84\n"
    "v_exp_f32 v85, v85\n"
    "v_mfma_f32_32x32x16_bf16 v[176:191], v[24:27], v[120:123], v[176:191]\n"
    "s_add_u32 m0, m0, 12800\n"
    "s_add_u32 s94, s94, 102400\n"
    "s_mov_b32 exec_hi, 0x3ffff\n"
    "buffer_load_dwordx4 v248, s[20:23], s94 offen lds\n"
    "s_mov_b64 exec, -1\n"
    "v_exp_f32 v86, v86\n"
    "v_cvt_pk_bf16_f32 v116, v84, v85\n"
    "v_exp_f32 v87, v87\n"
    "v_exp_f32 v88, v88\n"
    "v_mfma_f32_32x32x16_bf16 v[100:115], v[42:45], v[198:201], 0\n"
    "v_cvt_pk_bf16_f32 v117, v86, v87\n"
    "v_exp_f32 v89, v89\n"
    "v_exp_f32 v90, v90\n"
    "v_cvt_pk_bf16_f32 v118, v88, v89\n"
    "v_exp_f32 v91, v91\n"
    "v_mfma_f32_32x32x8_bf16 v[100:115], v[46:47], v[202:203], v[100:115]\n"
    "v_exp_f32 v92, v92\n"
    "v_cvt_pk_bf16_f32 v119, v90, v91\n"
    "v_exp_f32 v93, v93\n"
    "ds_write_b128 v244, v[160:163] offset:0\n"
    "v_exp_f32 v94, v94\n"
    "v_mfma_f32_32x32x16_bf16 v[176:191], v[34:37], v[116:119], v[176:191]\n"
    "v_cvt_pk_bf16_f32 v120, v92, v93\n"
    "v_exp_f32 v95, v95\n"
    "v_exp_f32 v96, v96\n"
    "v_cvt_pk_bf16_f32 v121, v94, v95\n"
    "v_exp_f32 v97, v97\n"
    "v_exp_f32 v98, v98\n"
    "v_exp_f32 v99, v99\n"
    "v_cvt_pk_bf16_f32 v122, v96, v97\n"
    "v_cvt_pk_bf16_f32 v123, v98, v99\n"
    "s_mov_b32 s44, s98\n"
    "v_lshrrev_b32 v145, 3, v144\n"
    "v_add_u32 v145, s44, v145\n"
    "v_and_b32 v146, 7, v144\n"
    "v_lshlrev_b32 v146, 4, v146\n"
    "v_lshl_add_u32 v147, v145, 12, v146\n"
    "v_lshl_add_u32 v148, v145, 11, v146\n"
    "s_mul_i32 s41, s26, 327680\n"
    "s_lshl_b32 s42, s29, 1\n"
    "s_add_u32 s41, s41, s42\n"
    "s_add_u32 s41, s41, 2949120\n"
    "s_mul_i32 s43, s26, 163840\n"
    "s_add_u32 s43, s43, s42\n"
    "s_add_u32 s43, s43, 5568512\n"
    "s_cmp_lt_u32 s25, 16\n"
    "s_cselect_b32 s43, 0x7f000000, s43\n"
    "s_waitcnt vmcnt(3)\n"
    "v_and_b32 v152, 0xffff0000, v150\n"
    "v_cmp_eq_u32 s[86:87], s35, v151\n"
    "v_cmp_eq_u32 vcc, s34, v152\n"
    "s_and_b64 vcc, vcc, s[86:87]\n"
    "s_cmp_eq_u64 vcc, -1\n"
    "s_cbranch_scc1 .LkL_107_%=\n"
    ".Lkp_108_%=:\n"
    "buffer_load_dwordx2 v[150:151], v149, s[12:15], 0 offen sc1\n"
    "s_waitcnt vmcnt(0)\n"
    "v_and_b32 v152, 0xffff0000, v150\n"
    "v_cmp_eq_u32 s[86:87], s35, v151\n"
    "v_cmp_eq_u32 vcc, s34, v152\n"
    "s_and_b64 vcc, vcc, s[86:87]\n"
    "s_cmp_eq_u64 vcc, -1\n"
    "s_cbranch_scc1 .Lkd_109_%=\n"
    "s_sleep 5\n"
    "s_sub_u32 s80, s80, 1\n"
    "s_cbranch_scc0 .Lkp_108_%=\n"
    "s_mov_b32 s80, 0\n"
    ".Lkd_109_%=:\n"
    ".LkL_107_%=:\n"
    "buffer_load_dwordx4 v[128:131], v147, s[12:15], s41 offen sc1\n"
    "buffer_load_dwordx4 v[136:139], v148, s[12:15], s43 offen sc1\n"
    "s_cmp_ge_u32 s24, 2\n"
    "s_cbranch_scc1 .LkL_110_%=\n"
    "s_add_u32 s41, s41, 262144\n"
    "s_add_u32 s43, s43, 131072\n"
    "buffer_load_dwordx4 v[132:135], v147, s[12:15], s41 offen sc1\n"
    "buffer_load_dwordx4 v[140:143], v148, s[12:15], s43 offen sc1\n"
    ".LkL_110_%=:\n"
    "v_exp_f32 v100, v100\n"
    "v_exp_f32 v101, v101\n"
    "v_mfma_f32_32x32x16_bf16 v[176:191], v[38:41], v[120:123], v[176:191]\n"
    "s_add_u32 m0, m0, 12800\n"
    "s_add_u32 s94, s94, 102400\n"
    "s_mov_b32 exec_hi, 0x3ffff\n"
    "buffer_load_dwordx4 v248, s[20:23], s94 offen lds\n"
    "s_mov_b64 exec, -1\n"
    "v_exp_f32 v102, v102\n"
    "v_cvt_pk_bf16_f32 v116, v100, v101\n"
    "v_exp_f32 v103, v103\n"
    "v_exp_f32 v104, v104\n"
    "v_mfma_f32_32x32x16_bf16 v[84:99], v[56:59], v[198:201], 0\n"
    "v_cvt_pk_bf16_f32 v117, v102, v103\n"
    "v_exp_f32 v105, v105\n"
    "v_exp_f32 v106, v106\n"
    "v_cvt_pk_bf16_f32 v118, v104, v105\n"
    "v_exp_f32 v107, v107\n"
    "v_mfma_f32_32x32x8_bf16 v[84:99], v[60:61], v[202:203], v[84:99]\n"
    "v_exp_f32 v108, v108\n"
    "v_cvt_pk_bf16_f32 v119, v106, v107\n"
    "v_exp_f32 v109, v109\n"
    "ds_write_b128 v244, v[164:167] offset:1024\n"
    "v_exp_f32 v110, v110\n"
    "v_mfma_f32_32x32x16_bf16 v[176:191], v[48:51], v[116:119], v[176:191]\n"
    "v_cvt_pk_bf16_f32 v120, v108, v109\n"
    "v_exp_f32 v111, v111\n"
    "v_exp_f32 v112, v112\n"
    "v_cvt_pk_bf16_f32 v121, v110, v111\n"
    "v_exp_f32 v113, v113\n"
    "v_exp_f32 v114, v114\n"
    "v_exp_f32 v115, v115\n"
    "v_cvt_pk_bf16_f32 v122, v112, v113\n"
    "v_cvt_pk_bf16_f32 v123, v114, v115\n"
    "s_setprio 0\n"
    "s_lshl_b32 s40, s88, 4\n"
    "s_add_u32 s40, s40, s29\n"
    "s_lshl_b32 s40, s40, 3\n"
    "s_add_u32 s40, s40, 311296\n"
    "v_and_b32 v153, 15, v144\n"
    "v_lshl_add_u32 v153, v153, 3, s40\n"
    "buffer_load_dwordx2 v[154:155], v153, s[12:15], 0 offen sc1\n"
    "v_exp_f32 v84, v84\n"
    "v_exp_f32 v85, v85\n"
    "v_mfma_f32_32x32x16_bf16 v[176:191], v[52:55], v[120:123], v[176:191]\n"
    "v_exp_f32 v86, v86\n"
    "v_cvt_pk_bf16_f32 v116, v84, v85\n"
    "v_exp_f32 v87, v87\n"
    "v_exp_f32 v88, v88\n"
    "v_mfma_f32_32x32x16_bf16 v[100:115], v[70:73], v[198:201], 0\n"
    "v_cvt_pk_bf16_f32 v117, v86, v87\n"
    "v_exp_f32 v89, v89\n"
    "v_exp_f32 v90, v90\n"
    "v_cvt_pk_bf16_f32 v118, v88, v89\n"
    "v_exp_f32 v91, v91\n"
    "v_mfma_f32_32x32x8_bf16 v[100:115], v[74:75], v[202:203], v[100:115]\n"
    "v_exp_f32 v92, v92\n"
    "v_cvt_pk_bf16_f32 v119, v90, v91\n"
    "v_exp_f32 v93, v93\n"
    "ds_write_b128 v244, v[168:171] offset:2048\n"
    "v_exp_f32 v94, v94\n"
    "v_mfma_f32_32x32x16_bf16 v[176:191], v[62:65], v[116:119], v[176:191]\n"
    "v_cvt_pk_bf16_f32 v120, v92, v93\n"
    "v_exp_f32 v95, v95\n"
    "v_exp_f32 v96, v96\n"
    "v_cvt_pk_bf16_f32 v121, v94, v95\n"
    "v_exp_f32 v97, v97\n"
    "v_exp_f32 v98, v98\n"
    "v_exp_f32 v99, v99\n"
    "v_cvt_pk_bf16_f32 v122, v96, v97\n"
    "v_cvt_pk_bf16_f32 v123, v98, v99\n"
    "s_add_u32 s38, s81, 24576\n"
    "buffer_load_dwordx4 v[224:227], v247, s[12:15], s38 offen offset:0 sc1\n"
    "buffer_load_dwordx4 v[228:231], v247, s[12:15], s38 offen offset:1024 sc1\n"
    "buffer_load_dwordx4 v[232:235], v247, s[12:15], s38 offen offset:2048 sc1\n"
    "v_exp_f32 v100, v100\n"
    "v_exp_f32 v101, v101\n"
    "v_mfma_f32_32x32x16_bf16 v[176:191], v[66:69], v[120:123], v[176:191]\n"
    "v_exp_f32 v102, v102\n"
    "v_cvt_pk_bf16_f32 v116, v100, v101\n"
    "v_exp_f32 v103, v103\n"
    "v_exp_f32 v104, v104\n"
    "v_cvt_pk_bf16_f32 v117, v102, v103\n"
    "v_exp_f32 v105, v105\n"
    "v_exp_f32 v106, v106\n"
    "v_cvt_pk_bf16_f32 v118, v104, v105\n"
    "v_exp_f32 v107, v107\n"
    "v_exp_f32 v108, v108\n"
    "v_cvt_pk_bf16_f32 v119, v106, v107\n"
    "v_exp_f32 v109, v109\n"
    "v_exp_f32 v110, v110\n"
    "v_mfma_f32_32x32x16_bf16 v[176:191], v[76:79], v[116:119], v[176:191]\n"
    "v_cvt_pk_bf16_f32 v120, v108, v109\n"
    "v_exp_f32 v111, v111\n"
    "v_exp_f32 v112, v112\n"
    "v_cvt_pk_bf16_f32 v121, v110, v111\n"
    "v_exp_f32 v113, v113\n"
    "v_exp_f32 v114, v114\n"
    "v_exp_f32 v115, v115\n"
    "v_cvt_pk_bf16_f32 v122, v112, v113\n"
    "v_cvt_pk_bf16_f32 v123, v114, v115\n"
    "s_nop 1\n"
    "v_mfma_f32_32x32x16_bf16 v[176:191], v[80:83], v[120:123], v[176:191]\n"
    "v_lshrrev_b32 v0, 4, v247\n"
    "v_and_b32 v1, 15, v0\n"
    "v_lshrrev_b32 v2, 4, v0\n"
    "v_and_b32 v3, 31, v0\n"
    "s_setprio 0\n"
    "s_nop 6\n"
    "ds_write_b128 v244, v[176:179] offset:24576\n"
    "ds_write_b128 v244, v[180:183] offset:25600\n"
    "ds_write_b128 v244, v[184:187] offset:26624\n"
    "v_and_b32 v6, 0x3f0, v241\n"
    "v_and_b32 v7, 8, v241\n"
    "v_lshl_add_u32 v6, v7, 7, v6\n"
    "v_and_b32 v7, 0xfffffc00, v241\n"
    "v_add_u32 v6, v7, v6\n"
    "v_and_b32 v7, 0x1c00, v241\n"
    "v_add_u32 v6, v7, v6\n"
    "s_lshl_b32 s41, s25, 2\n"
    "s_lshr_b32 s42, s24, 1\n"
    "s_add_u32 s41, s41, s42\n"
    "s_mul_i32 s41, s41, 12288\n"
    "s_add_u32 s41, s41, 11075584\n"
    "s_lshl_b32 s98, s25, 2\n"
    "s_add_u32 s98, s98, s88\n"
    "s_mul_i32 s98, s98, 12288\n"
    "s_mul_i32 s42, s89, 6144\n"
    "s_add_u32 s98, s98, s42\n"
    "s_add_u32 s98, s98, 11075584\n"
    "s_add_u32 s99, s98, 4096\n"
    "s_waitcnt lgkmcnt(0)\n"
    "s_barrier\n"
    "ds_read_b128 v[160:163], v246 offset:0\n"
    "ds_read_b128 v[164:167], v246 offset:3072\n"
    "ds_read_b128 v[168:171], v246 offset:6144\n"
    "ds_read_b128 v[172:175], v246 offset:9216\n"
    "ds_read_b128 v[124:127], v246 offset:12288\n"
    "ds_read_b128 v[156:159], v246 offset:15360\n"
    "ds_read_b128 v[236:239], v246 offset:18432\n"
    "s_waitcnt lgkmcnt(5)\n"
    "v_pk_add_f32 v[160:161], v[160:161], v[164:165]\n"
    "v_pk_add_f32 v[162:163], v[162:163], v[166:167]\n"
    "ds_read_b128 v[164:167], v246 offset:21504\n"
    "s_waitcnt lgkmcnt(2)\n"
    "v_pk_add_f32 v[168:169], v[168:169], v[172:173]\n"
    "v_pk_add_f32 v[170:171], v[170:171], v[174:175]\n"
    "v_pk_add_f32 v[124:125], v[124:125], v[156:157]\n"
    "v_pk_add_f32 v[126:127], v[126:127], v[158:159]\n"
    "v_pk_add_f32 v[160:161], v[160:161], v[168:169]\n"
    "v_pk_add_f32 v[162:163], v[162:163], v[170:171]\n"
    "s_waitcnt lgkmcnt(0)\n"
    "v_pk_add_f32 v[236:237], v[236:237], v[164:165]\n"
    "v_pk_add_f32 v[238:239], v[238:239], v[166:167]\n"
    "v_pk_add_f32 v[160:161], v[160:161], v[124:125]\n"
    "v_pk_add_f32 v[162:163], v[162:163], v[126:127]\n"
    "v_pk_add_f32 v[160:161], v[160:161], v[236:237]\n"
    "v_pk_add_f32 v[162:163], v[162:163], v[238:239]\n"
    "v_bfe_u32 v164, v247, 2, 5\n"
    "v_add_u32 v164, 160, v164\n"
    "ds_bpermute_b32 v164, v164, v160\n"
    "s_waitcnt lgkmcnt(0)\n"
    "v_rcp_f32 v164, v164\n"
    "s_add_u32 s38, s92, 73728\n"
    "v_cndmask_b32 v164, v164, 0, s[96:97]\n"
    "v_pk_mul_f32 v[160:161], v[160:161], v[164:165] op_sel_hi:[1,0]\n"
    "v_pk_mul_f32 v[162:163], v[162:163], v[164:165] op_sel_hi:[1,0]\n"
    "v_cvt_pk_bf16_f32 v124, v160, v161\n"
    "v_cvt_pk_bf16_f32 v125, v162, v163\n"
    "v_mov_b32 v126, v125\n"
    "v_mov_b32 v125, s36\n"
    "v_mov_b32 v127, s36\n"
    "buffer_store_dwordx2 v[124:125], v6, s[12:15], s41 offen sc1\n"
    "buffer_store_dwordx2 v[126:127], v6, s[12:15], s41 offen offset:8 sc1\n"
    "s_mov_b32 s39, 1024\n"
    "buffer_load_dwordx2 v[206:207], v209, s[12:15], s39 offen sc1\n"
    "v_lshrrev_b32 v12, 2, v1\n"
    "v_mul_u32_u24 v12, 160, v12\n"
    "v_and_b32 v13, 3, v1\n"
    "v_lshl_add_u32 v12, v13, 3, v12\n"
    "s_mul_i32 s40, s89, 61440\n"
    "v_add_u32 v12, s40, v12\n"
    "v_lshlrev_b32 v13, 3, v2\n"
    "s_movk_i32 s40, 640\n"
    "s_mov_b32 s39, 352584448\n"
    "v_bfe_u32 v14, s39, v13, 8\n"
    "v_mad_u32_u24 v80, v14, s40, v12\n"
    "s_mov_b32 s39, 335806977\n"
    "v_bfe_u32 v14, s39, v13, 8\n"
    "v_mad_u32_u24 v81, v14, s40, v12\n"
    "s_mov_b32 s39, 724047894\n"
    "v_bfe_u32 v14, s39, v13, 8\n"
    "v_mad_u32_u24 v82, v14, s40, v12\n"
    "s_mov_b32 s39, 707336215\n"
    "v_bfe_u32 v14, s39, v13, 8\n"
    "v_mad_u32_u24 v83, v14, s40, v12\n"
    "s_mov_b32 s39, 1077820716\n"
    "v_bfe_u32 v14, s39, v13, 8\n"
    "v_mad_u32_u24 v84, v14, s40, v12\n"
    "s_mov_b32 s39, 1077885996\n"
    "v_bfe_u32 v14, s39, v13, 8\n"
    "v_mad_u32_u24 v85, v14, s40, v12\n"
    "ds_read_b64_tr_b16 v[160:161], v80 offset:0\n"
    "ds_read_b64_tr_b16 v[162:163], v81 offset:0\n"
    "ds_read_b64_tr_b16 v[164:165], v80 offset:32\n"
    "ds_read_b64_tr_b16 v[166:167], v81 offset:32\n"
    "ds_read_b64_tr_b16 v[168:169], v80 offset:64\n"
    "ds_read_b64_tr_b16 v[170:171], v81 offset:64\n"
    "ds_read_b64_tr_b16 v[172:173], v80 offset:96\n"
    "ds_read_b64_tr_b16 v[174:175], v81 offset:96\n"
    "ds_read_b64_tr_b16 v[176:177], v80 offset:128\n"
    "ds_read_b64_tr_b16 v[178:179], v81 offset:128\n"
    "ds_read_b64_tr_b16 v[180:181], v82 offset:0\n"
    "ds_read_b64_tr_b16 v[182:183], v83 offset:0\n"
    "ds_read_b64_tr_b16 v[184:185], v82 offset:32\n"
    "ds_read_b64_tr_b16 v[186:187], v83 offset:32\n"
    "s_waitcnt lgkmcnt(12)\n"
    "v_mfma_f32_16x16x32_bf16 v[88:91], v[160:163], v[212:215], 0\n"
    "ds_read_b64_tr_b16 v[188:189], v82 offset:64\n"
    "ds_read_b64_tr_b16 v[190:191], v83 offset:64\n"
    "s_waitcnt lgkmcnt(12)\n"
    "v_mfma_f32_16x16x32_bf16 v[92:95], v[164:167], v[212:215], 0\n"
    "ds_read_b64_tr_b16 v[192:193], v82 offset:96\n"
    "ds_read_b64_tr_b16 v[194:195], v83 offset:96\n"
    "s_waitcnt lgkmcnt(12)\n"
    "v_mfma_f32_16x16x32_bf16 v[96:99], v[168:171], v[212:215], 0\n"
    "ds_read_b64_tr_b16 v[160:161], v82 offset:128\n"
    "ds_read_b64_tr_b16 v[162:163], v83 offset:128\n"
    "s_waitcnt lgkmcnt(12)\n"
    "v_mfma_f32_16x16x32_bf16 v[100:103], v[172:175], v[212:215], 0\n"
    "ds_read_b64_tr_b16 v[164:165], v84 offset:0\n"
    "ds_read_b64_tr_b16 v[166:167], v85 offset:0\n"
    "s_waitcnt lgkmcnt(12)\n"
    "v_mfma_f32_16x16x32_bf16 v[104:107], v[176:179], v[212:215], 0\n"
    "ds_read_b64_tr_b16 v[168:169], v84 offset:32\n"
    "ds_read_b64_tr_b16 v[170:171], v85 offset:32\n"
    "s_waitcnt lgkmcnt(12)\n"
    "v_mfma_f32_16x16x32_bf16 v[88:91], v[180:183], v[216:219], v[88:91]\n"
    "ds_read_b64_tr_b16 v[172:173], v84 offset:64\n"
    "ds_read_b64_tr_b16 v[174:175], v85 offset:64\n"
    "s_waitcnt lgkmcnt(12)\n"
    "v_mfma_f32_16x16x32_bf16 v[92:95], v[184:187], v[216:219], v[92:95]\n"
    "ds_read_b64_tr_b16 v[176:177], v84 offset:96\n"
    "ds_read_b64_tr_b16 v[178:179], v85 offset:96\n"
    "s_waitcnt lgkmcnt(12)\n"
    "v_mfma_f32_16x16x32_bf16 v[96:99], v[188:191], v[216:219], v[96:99]\n"
    "ds_read_b64_tr_b16 v[180:181], v84 offset:128\n"
    "ds_read_b64_tr_b16 v[182:183], v85 offset:128\n"
    "s_waitcnt lgkmcnt(12)\n"
    "v_mfma_f32_16x16x32_bf16 v[100:103], v[192:195], v[216:219], v[100:103]\n"
    "s_waitcnt lgkmcnt(10)\n"
    "v_mfma_f32_16x16x32_bf16 v[104:107], v[160:163], v[216:219], v[104:107]\n"
    "s_waitcnt lgkmcnt(8)\n"
    "v_mfma_f32_16x16x32_bf16 v[88:91], v[164:167], v[220:223], v[88:91]\n"
    "s_waitcnt lgkmcnt(6)\n"
    "v_mfma_f32_16x16x32_bf16 v[92:95], v[168:171], v[220:223], v[92:95]\n"
    "s_waitcnt lgkmcnt(4)\n"
    "v_mfma_f32_16x16x32_bf16 v[96:99], v[172:175], v[220:223], v[96:99]\n"
    "s_waitcnt lgkmcnt(2)\n"
    "v_mfma_f32_16x16x32_bf16 v[100:103], v[176:179], v[220:223], v[100:103]\n"
    "s_waitcnt lgkmcnt(0)\n"
    "v_mfma_f32_16x16x32_bf16 v[104:107], v[180:183], v[220:223], v[104:107]\n"
    "s_waitcnt vmcnt(6)\n"
    "s_lshl_b32 s40, s24, 10\n"
    "v_add_u32 v10, s40, v247\n"
    "v_lshlrev_b32 v6, 16, v128\n"
    "v_and_b32 v7, 0xffff0000, v128\n"
    "v_lshlrev_b32 v8, 16, v136\n"
    "v_and_b32 v9, 0xffff0000, v136\n"
    "v_pk_add_f32 v[6:7], v[6:7], v[8:9]\n"
    "v_cvt_pk_bf16_f32 v128, v6, v7\n"
    "v_lshlrev_b32 v6, 16, v129\n"
    "v_and_b32 v7, 0xffff0000, v129\n"
    "v_lshlrev_b32 v8, 16, v137\n"
    "v_and_b32 v9, 0xffff0000, v137\n"
    "v_pk_add_f32 v[6:7], v[6:7], v[8:9]\n"
    "v_cvt_pk_bf16_f32 v129, v6, v7\n"
    "v_lshlrev_b32 v6, 16, v130\n"
    "v_and_b32 v7, 0xffff0000, v130\n"
    "v_lshlrev_b32 v8, 16, v138\n"
    "v_and_b32 v9, 0xffff0000, v138\n"
    "v_pk_add_f32 v[6:7], v[6:7], v[8:9]\n"
    "v_cvt_pk_bf16_f32 v130, v6, v7\n"
    "v_lshlrev_b32 v6, 16, v131\n"
    "v_and_b32 v7, 0xffff0000, v131\n"
    "v_lshlrev_b32 v8, 16, v139\n"
    "v_and_b32 v9, 0xffff0000, v139\n"
    "v_pk_add_f32 v[6:7], v[6:7], v[8:9]\n"
    "v_cvt_pk_bf16_f32 v131, v6, v7\n"
    "ds_write_b128 v10, v[128:131] offset:51200\n"
    "s_cmp_ge_u32 s24, 2\n"
    "s_cbranch_scc1 .LkL_111_%=\n"
    "v_lshlrev_b32 v6, 16, v132\n"
    "v_and_b32 v7, 0xffff0000, v132\n"
    "v_lshlrev_b32 v8, 16, v140\n"
    "v_and_b32 v9, 0xffff0000, v140\n"
    "v_pk_add_f32 v[6:7], v[6:7], v[8:9]\n"
    "v_cvt_pk_bf16_f32 v132, v6, v7\n"
    "v_lshlrev_b32 v6, 16, v133\n"
    "v_and_b32 v7, 0xffff0000, v133\n"
    "v_lshlrev_b32 v8, 16, v141\n"
    "v_and_b32 v9, 0xffff0000, v141\n"
    "v_pk_add_f32 v[6:7], v[6:7], v[8:9]\n"
    "v_cvt_pk_bf16_f32 v133, v6, v7\n"
    "v_lshlrev_b32 v6, 16, v134\n"
    "v_and_b32 v7, 0xffff0000, v134\n"
    "v_lshlrev_b32 v8, 16, v142\n"
    "v_and_b32 v9, 0xffff0000, v142\n"
    "v_pk_add_f32 v[6:7], v[6:7], v[8:9]\n"
    "v_cvt_pk_bf16_f32 v134, v6, v7\n"
    "v_lshlrev_b32 v6, 16, v135\n"
    "v_and_b32 v7, 0xffff0000, v135\n"
    "v_lshlrev_b32 v8, 16, v143\n"
    "v_and_b32 v9, 0xffff0000, v143\n"
    "v_pk_add_f32 v[6:7], v[6:7], v[8:9]\n"
    "v_cvt_pk_bf16_f32 v135, v6, v7\n"
    "ds_write_b128 v10, v[132:135] offset:59392\n"
    ".LkL_111_%=:\n"
    "s_waitcnt vmcnt(0)\n"
    "v_and_b32 v208, 0xffff0000, v206\n"
    "v_cmp_eq_u32 s[86:87], s35, v207\n"
    "v_cmp_eq_u32 vcc, s34, v208\n"
    "s_and_b64 vcc, vcc, s[86:87]\n"
    "s_cmp_eq_u64 vcc, -1\n"
    "s_cbranch_scc1 .LkL_112_%=\n"
    "s_mov_b32 s39, 1024\n"
    ".Lkp_113_%=:\n"
    "buffer_load_dwordx2 v[206:207], v209, s[12:15], s39 offen sc1\n"
    "s_waitcnt vmcnt(0)\n"
    "v_and_b32 v208, 0xffff0000, v206\n"
    "v_cmp_eq_u32 s[86:87], s35, v207\n"
    "v_cmp_eq_u32 vcc, s34, v208\n"
    "s_and_b64 vcc, vcc, s[86:87]\n"
    "s_cmp_eq_u64 vcc, -1\n"
    "s_cbranch_scc1 .Lkd_114_%=\n"
    "s_sleep 5\n"
    "s_sub_u32 s80, s80, 1\n"
    "s_cbranch_scc0 .Lkp_113_%=\n"
    "s_mov_b32 s80, 0\n"
    ".Lkd_114_%=:\n"
    ".LkL_112_%=:\n"
    "s_add_u32 s39, s81, 49152\n"
    "buffer_load_dwordx4 v[16:19], v247, s[12:15], s39 offen offset:0 sc1\n"
    "buffer_load_dwordx4 v[20:23], v247, s[12:15], s39 offen offset:1024 sc1\n"
    "buffer_load_dwordx4 v[24:27], v247, s[12:15], s39 offen offset:2048 sc1\n"
    "ds_read_b64_tr_b16 v[160:161], v80 offset:3200\n"
    "ds_read_b64_tr_b16 v[162:163], v81 offset:3200\n"
    "ds_read_b64_tr_b16 v[164:165], v80 offset:3232\n"
    "ds_read_b64_tr_b16 v[166:167], v81 offset:3232\n"
    "ds_read_b64_tr_b16 v[168:169], v80 offset:3264\n"
    "ds_read_b64_tr_b16 v[170:171], v81 offset:3264\n"
    "ds_read_b64_tr_b16 v[172:173], v80 offset:3296\n"
    "ds_read_b64_tr_b16 v[174:175], v81 offset:3296\n"
    "ds_read_b64_tr_b16 v[176:177], v80 offset:3328\n"
    "ds_read_b64_tr_b16 v[178:179], v81 offset:3328\n"
    "ds_read_b64_tr_b16 v[180:181], v82 offset:3200\n"
    "ds_read_b64_tr_b16 v[182:183], v83 offset:3200\n"
    "ds_read_b64_tr_b16 v[184:185], v82 offset:3232\n"
    "ds_read_b64_tr_b16 v[186:187], v83 offset:3232\n"
    "s_waitcnt lgkmcnt(12)\n"
    "v_mfma_f32_16x16x32_bf16 v[88:91], v[160:163], v[224:227], v[88:91]\n"
    "ds_read_b64_tr_b16 v[188:189], v82 offset:3264\n"
    "ds_read_b64_tr_b16 v[190:191], v83 offset:3264\n"
    "s_waitcnt lgkmcnt(12)\n"
    "v_mfma_f32_16x16x32_bf16 v[92:95], v[164:167], v[224:227], v[92:95]\n"
    "ds_read_b64_tr_b16 v[192:193], v82 offset:3296\n"
    "ds_read_b64_tr_b16 v[194:195], v83 offset:3296\n"
    "s_waitcnt lgkmcnt(12)\n"
    "v_mfma_f32_16x16x32_bf16 v[96:99], v[168:171], v[224:227], v[96:99]\n"
    "ds_read_b64_tr_b16 v[160:161], v82 offset:3328\n"
    "ds_read_b64_tr_b16 v[162:163], v83 offset:3328\n"
    "s_waitcnt lgkmcnt(12)\n"
    "v_mfma_f32_16x16x32_bf16 v[100:103], v[172:175], v[224:227], v[100:103]\n"
    "ds_read_b64_tr_b16 v[164:165], v84 offset:3200\n"
    "ds_read_b64_tr_b16 v[166:167], v85 offset:3200\n"
    "s_waitcnt lgkmcnt(12)\n"
    "v_mfma_f32_16x16x32_bf16 v[104:107], v[176:179], v[224:227], v[104:107]\n"
    "ds_read_b64_tr_b16 v[168:169], v84 offset:3232\n"
    "ds_read_b64_tr_b16 v[170:171], v85 offset:3232\n"
    "s_waitcnt lgkmcnt(12)\n"
    "v_mfma_f32_16x16x32_bf16 v[88:91], v[180:183], v[228:231], v[88:91]\n"
    "ds_read_b64_tr_b16 v[172:173], v84 offset:3264\n"
    "ds_read_b64_tr_b16 v[174:175], v85 offset:3264\n"
    "s_waitcnt lgkmcnt(12)\n"
    "v_mfma_f32_16x16x32_bf16 v[92:95], v[184:187], v[228:231], v[92:95]\n"
    "ds_read_b64_tr_b16 v[176:177], v84 offset:3296\n"
    "ds_read_b64_tr_b16 v[178:179], v85 offset:3296\n"
    "s_waitcnt lgkmcnt(12)\n"
    "v_mfma_f32_16x16x32_bf16 v[96:99], v[188:191], v[228:231], v[96:99]\n"
    "ds_read_b64_tr_b16 v[180:181], v84 offset:3328\n"
    "ds_read_b64_tr_b16 v[182:183], v85 offset:3328\n"
    "s_waitcnt lgkmcnt(12)\n"
    "v_mfma_f32_16x16x32_bf16 v[100:103], v[192:195], v[228:231], v[100:103]\n"
    "s_waitcnt lgkmcnt(10)\n"
    "v_mfma_f32_16x16x32_bf16 v[104:107], v[160:163], v[228:231], v[104:107]\n"
    "s_waitcnt lgkmcnt(8)\n"
    "v_mfma_f32_16x16x32_bf16 v[88:91], v[164:167], v[232:235], v[88:91]\n"
    "s_waitcnt lgkmcnt(6)\n"
    "v_mfma_f32_16x16x32_bf16 v[92:95], v[168:171], v[232:235], v[92:95]\n"
    "s_waitcnt lgkmcnt(4)\n"
    "v_mfma_f32_16x16x32_bf16 v[96:99], v[172:175], v[232:235], v[96:99]\n"
    "s_waitcnt lgkmcnt(2)\n"
    "v_mfma_f32_16x16x32_bf16 v[100:103], v[176:179], v[232:235], v[100:103]\n"
    "s_waitcnt lgkmcnt(0)\n"
    "v_mfma_f32_16x16x32_bf16 v[104:107], v[180:183], v[232:235], v[104:107]\n"
    "buffer_load_dwordx4 v[28:31], v247, s[12:15], s98 offen offset:0 sc1\n"
    "buffer_load_dwordx4 v[32:35], v247, s[12:15], s98 offen offset:1024 sc1\n"
    "buffer_load_dwordx4 v[36:39], v247, s[12:15], s98 offen offset:2048 sc1\n"
    "buffer_load_dwordx4 v[40:43], v247, s[12:15], s98 offen offset:3072 sc1\n"
    "buffer_load_dwordx4 v[44:47], v247, s[12:15], s99 offen offset:0 sc1\n"
    "buffer_load_dwordx4 v[48:51], v247, s[12:15], s99 offen offset:1024 sc1\n"
    "ds_read_b64_tr_b16 v[160:161], v80 offset:6400\n"
    "ds_read_b64_tr_b16 v[162:163], v81 offset:6400\n"
    "ds_read_b64_tr_b16 v[164:165], v80 offset:6432\n"
    "ds_read_b64_tr_b16 v[166:167], v81 offset:6432\n"
    "ds_read_b64_tr_b16 v[168:169], v80 offset:6464\n"
    "ds_read_b64_tr_b16 v[170:171], v81 offset:6464\n"
    "ds_read_b64_tr_b16 v[172:173], v80 offset:6496\n"
    "ds_read_b64_tr_b16 v[174:175], v81 offset:6496\n"
    "ds_read_b64_tr_b16 v[176:177], v80 offset:6528\n"
    "ds_read_b64_tr_b16 v[178:179], v81 offset:6528\n"
    "ds_read_b64_tr_b16 v[180:181], v82 offset:6400\n"
    "ds_read_b64_tr_b16 v[182:183], v83 offset:6400\n"
    "ds_read_b64_tr_b16 v[184:185], v82 offset:6432\n"
    "ds_read_b64_tr_b16 v[186:187], v83 offset:6432\n"
    "s_waitcnt vmcnt(6)\n"
    "s_waitcnt lgkmcnt(12)\n"
    "v_mfma_f32_16x16x32_bf16 v[88:91], v[160:163], v[16:19], v[88:91]\n"
    "ds_read_b64_tr_b16 v[188:189], v82 offset:6464\n"
    "ds_read_b64_tr_b16 v[190:191], v83 offset:6464\n"
    "s_waitcnt lgkmcnt(12)\n"
    "v_mfma_f32_16x16x32_bf16 v[92:95], v[164:167], v[16:19], v[92:95]\n"
    "ds_read_b64_tr_b16 v[192:193], v82 offset:6496\n"
    "ds_read_b64_tr_b16 v[194:195], v83 offset:6496\n"
    "s_waitcnt lgkmcnt(12)\n"
    "v_mfma_f32_16x16x32_bf16 v[96:99], v[168:171], v[16:19], v[96:99]\n"
    "ds_read_b64_tr_b16 v[160:161], v82 offset:6528\n"
    "ds_read_b64_tr_b16 v[162:163], v83 offset:6528\n"
    "s_waitcnt lgkmcnt(12)\n"
    "v_mfma_f32_16x16x32_bf16 v[100:103], v[172:175], v[16:19], v[100:103]\n"
    "ds_read_b64_tr_b16 v[164:165], v84 offset:6400\n"
    "ds_read_b64_tr_b16 v[166:167], v85 offset:6400\n"
    "s_waitcnt lgkmcnt(12)\n"
    "v_mfma_f32_16x16x32_bf16 v[104:107], v[176:179], v[16:19], v[104:107]\n"
    "ds_read_b64_tr_b16 v[168:169], v84 offset:6432\n"
    "ds_read_b64_tr_b16 v[170:171], v85 offset:6432\n"
    "s_waitcnt lgkmcnt(12)\n"
    "v_mfma_f32_16x16x32_bf16 v[88:91], v[180:183], v[20:23], v[88:91]\n"
    "ds_read_b64_tr_b16 v[172:173], v84 offset:6464\n"
    "ds_read_b64_tr_b16 v[174:175], v85 offset:6464\n"
    "s_waitcnt lgkmcnt(12)\n"
    "v_mfma_f32_16x16x32_bf16 v[92:95], v[184:187], v[20:23], v[92:95]\n"
    "ds_read_b64_tr_b16 v[176:177], v84 offset:6496\n"
    "ds_read_b64_tr_b16 v[178:179], v85 offset:6496\n"
    "s_waitcnt lgkmcnt(12)\n"
    "v_mfma_f32_16x16x32_bf16 v[96:99], v[188:191], v[20:23], v[96:99]\n"
    "ds_read_b64_tr_b16 v[180:181], v84 offset:6528\n"
    "ds_read_b64_tr_b16 v[182:183], v85 offset:6528\n"
    "s_waitcnt lgkmcnt(12)\n"
    "v_mfma_f32_16x16x32_bf16 v[100:103], v[192:195], v[20:23], v[100:103]\n"
    "s_waitcnt lgkmcnt(10)\n"
    "v_mfma_f32_16x16x32_bf16 v[104:107], v[160:163], v[20:23], v[104:107]\n"
    "s_waitcnt lgkmcnt(8)\n"
    "v_mfma_f32_16x16x32_bf16 v[88:91], v[164:167], v[24:27], v[88:91]\n"
    "s_waitcnt lgkmcnt(6)\n"
    "v_mfma_f32_16x16x32_bf16 v[92:95], v[168:171], v[24:27], v[92:95]\n"
    "s_waitcnt lgkmcnt(4)\n"
    "v_mfma_f32_16x16x32_bf16 v[96:99], v[172:175], v[24:27], v[96:99]\n"
    "s_waitcnt lgkmcnt(2)\n"
    "v_mfma_f32_16x16x32_bf16 v[100:103], v[176:179], v[24:27], v[100:103]\n"
    "s_waitcnt lgkmcnt(0)\n"
    "v_mfma_f32_16x16x32_bf16 v[104:107], v[180:183], v[24:27], v[104:107]\n"
    ".LkL_115_%=:\n"
    "v_cmp_eq_u32 vcc, s36, v155\n"
    "s_cmp_eq_u64 vcc, -1\n"
    "s_cbranch_scc1 .LkL_116_%=\n"
    "buffer_load_dwordx2 v[154:155], v153, s[12:15], 0 offen sc1\n"
    "s_waitcnt vmcnt(0)\n"
    "s_sub_u32 s80, s80, 1\n"
    "s_cbranch_scc0 .LkL_115_%=\n"
    "s_mov_b32 s80, 0\n"
    ".LkL_116_%=:\n"
    "v_mul_f32 v154, 0xbfb8aa3b, v154\n"
    "v_exp_f32 v154, v154\n"
    "s_nop 0\n"
    "v_add_f32 v154, 1.0, v154\n"
    "v_rcp_f32 v154, v154\n"
    "s_nop 0\n"
    "v_sub_f32 v155, 1.0, v154\n"
    "s_mov_b32 s16, s76\n"
    "s_and_b32 s17, s77, 0xffff\n"
    "s_mov_b32 s18, 2621440\n"
    "s_mov_b32 s19, 0x20000\n"
    "s_mul_i32 s40, s88, 5120\n"
    "s_add_u32 s40, s40, 112640\n"
    "v_add_u32 v12, s40, v247\n"
    "v_lshrrev_b32 v13, 2, v1\n"
    "v_lshl_add_u32 v13, v2, 2, v13\n"
    "v_lshlrev_b32 v13, 7, v13\n"
    "v_and_b32 v14, 3, v1\n"
    "v_lshl_add_u32 v13, v14, 3, v13\n"
    "s_lshl_b32 s40, s88, 5\n"
    "v_add_u32 v13, s40, v13\n"
    "v_lshrrev_b32 v14, 2, v0\n"
    "v_mul_u32_u24 v15, 1280, v14\n"
    "v_and_b32 v5, 3, v0\n"
    "v_lshl_add_u32 v15, v5, 3, v15\n"
    "v_lshl_add_u32 v14, v5, 4, v14\n"
    "v_lshlrev_b32 v14, 2, v14\n"
    "s_lshl_b32 s40, s88, 4\n"
    "s_add_u32 s40, s40, s29\n"
    "s_mul_i32 s40, s40, 1280\n"
    "s_mul_i32 s41, s26, 160\n"
    "s_add_u32 s40, s40, s41\n"
    "s_waitcnt lgkmcnt(0)\n"
    "s_barrier\n"
    ".LkL_117_%=:\n"
    "s_waitcnt vmcnt(0)\n"
    "v_min3_u32 v52, v29, v31, v33\n"
    "v_max3_u32 v53, v29, v31, v33\n"
    "v_min3_u32 v52, v52, v35, v37\n"
    "v_max3_u32 v53, v53, v35, v37\n"
    "v_min3_u32 v52, v52, v39, v41\n"
    "v_max3_u32 v53, v53, v39, v41\n"
    "v_min3_u32 v52, v52, v43, v45\n"
    "v_max3_u32 v53, v53, v43, v45\n"
    "v_min3_u32 v52, v52, v47, v49\n"
    "v_max3_u32 v53, v53, v47, v49\n"
    "v_min_u32 v52, v52, v51\n"
    "v_max_u32 v53, v53, v51\n"
    "v_cmp_eq_u32 vcc, s36, v52\n"
    "v_cmp_eq_u32 s[86:87], s36, v53\n"
    "s_and_b64 vcc, vcc, s[86:87]\n"
    "s_cmp_eq_u64 vcc, -1\n"
    "s_cbranch_scc1 .LkL_118_%=\n"
    "s_sleep 2\n"
    "buffer_load_dwordx4 v[28:31], v247, s[12:15], s98 offen offset:0 sc1\n"
    "buffer_load_dwordx4 v[32:35], v247, s[12:15], s98 offen offset:1024 sc1\n"
    "buffer_load_dwordx4 v[36:39], v247, s[12:15], s98 offen offset:2048 sc1\n"
    "buffer_load_dwordx4 v[40:43], v247, s[12:15], s98 offen offset:3072 sc1\n"
    "buffer_load_dwordx4 v[44:47], v247, s[12:15], s99 offen offset:0 sc1\n"
    "buffer_load_dwordx4 v[48:51], v247, s[12:15], s99 offen offset:1024 sc1\n"
    "s_sub_u32 s80, s80, 1\n"
    "s_cbranch_scc0 .LkL_117_%=\n"
    "s_mov_b32 s80, 0\n"
    ".LkL_118_%=:\n"
    "v_mov_b32 v29, v30\n"
    "v_mov_b32 v30, v32\n"
    "v_mov_b32 v31, v34\n"
    "v_mov_b32 v37, v38\n"
    "v_mov_b32 v38, v40\n"
    "v_mov_b32 v39, v42\n"
    "v_mov_b32 v45, v46\n"
    "v_mov_b32 v46, v48\n"
    "v_mov_b32 v47, v50\n"
    "ds_read_b64_tr_b16 v[160:161], v80 offset:9600\n"
    "ds_read_b64_tr_b16 v[162:163], v81 offset:9600\n"
    "ds_read_b64_tr_b16 v[164:165], v80 offset:9632\n"
    "ds_read_b64_tr_b16 v[166:167], v81 offset:9632\n"
    "ds_read_b64_tr_b16 v[168:169], v80 offset:9664\n"
    "ds_read_b64_tr_b16 v[170:171], v81 offset:9664\n"
    "ds_read_b64_tr_b16 v[172:173], v80 offset:9696\n"
    "ds_read_b64_tr_b16 v[174:175], v81 offset:9696\n"
    "ds_read_b64_tr_b16 v[176:177], v80 offset:9728\n"
    "ds_read_b64_tr_b16 v[178:179], v81 offset:9728\n"
    "ds_read_b64_tr_b16 v[180:181], v82 offset:9600\n"
    "ds_read_b64_tr_b16 v[182:183], v83 offset:9600\n"
    "ds_read_b64_tr_b16 v[184:185], v82 offset:9632\n"
    "ds_read_b64_tr_b16 v[186:187], v83 offset:9632\n"
    "s_waitcnt lgkmcnt(12)\n"
    "v_mfma_f32_16x16x32_bf16 v[88:91], v[160:163], v[28:31], v[88:91]\n"
    "ds_read_b64_tr_b16 v[188:189], v82 offset:9664\n"
    "ds_read_b64_tr_b16 v[190:191], v83 offset:9664\n"
    "s_waitcnt lgkmcnt(12)\n"
    "v_mfma_f32_16x16x32_bf16 v[92:95], v[164:167], v[28:31], v[92:95]\n"
    "ds_read_b64_tr_b16 v[192:193], v82 offset:9696\n"
    "ds_read_b64_tr_b16 v[194:195], v83 offset:9696\n"
    "s_waitcnt lgkmcnt(12)\n"
    "v_mfma_f32_16x16x32_bf16 v[96:99], v[168:171], v[28:31], v[96:99]\n"
    "ds_read_b64_tr_b16 v[160:161], v82 offset:9728\n"
    "ds_read_b64_tr_b16 v[162:163], v83 offset:9728\n"
    "s_waitcnt lgkmcnt(12)\n"
    "v_mfma_f32_16x16x32_bf16 v[100:103], v[172:175], v[28:31], v[100:103]\n"
    "ds_read_b64_tr_b16 v[164:165], v84 offset:9600\n"
    "ds_read_b64_tr_b16 v[166:167], v85 offset:9600\n"
    "s_waitcnt lgkmcnt(12)\n"
    "v_mfma_f32_16x16x32_bf16 v[104:107], v[176:179], v[28:31], v[104:107]\n"
    "ds_read_b64_tr_b16 v[168:169], v84 offset:9632\n"
    "ds_read_b64_tr_b16 v[170:171], v85 offset:9632\n"
    "s_waitcnt lgkmcnt(12)\n"
    "v_mfma_f32_16x16x32_bf16 v[88:91], v[180:183], v[36:39], v[88:91]\n"
    "ds_read_b64_tr_b16 v[172:173], v84 offset:9664\n"
    "ds_read_b64_tr_b16 v[174:175], v85 offset:9664\n"
    "s_waitcnt lgkmcnt(12)\n"
    "v_mfma_f32_16x16x32_bf16 v[92:95], v[184:187], v[36:39], v[92:95]\n"
    "ds_read_b64_tr_b16 v[176:177], v84 offset:9696\n"
    "ds_read_b64_tr_b16 v[178:179], v85 offset:9696\n"
    "s_waitcnt lgkmcnt(12)\n"
    "v_mfma_f32_16x16x32_bf16 v[96:99], v[188:191], v[36:39], v[96:99]\n"
    "ds_read_b64_tr_b16 v[180:181], v84 offset:9728\n"
    "ds_read_b64_tr_b16 v[182:183], v85 offset:9728\n"
    "s_waitcnt lgkmcnt(12)\n"
    "v_mfma_f32_16x16x32_bf16 v[100:103], v[192:195], v[36:39], v[100:103]\n"
    "s_waitcnt lgkmcnt(10)\n"
    "v_mfma_f32_16x16x32_bf16 v[104:107], v[160:163], v[36:39], v[104:107]\n"
    "s_waitcnt lgkmcnt(8)\n"
    "v_mfma_f32_16x16x32_bf16 v[88:91], v[164:167], v[44:47], v[88:91]\n"
    "s_waitcnt lgkmcnt(6)\n"
    "v_mfma_f32_16x16x32_bf16 v[92:95], v[168:171], v[44:47], v[92:95]\n"
    "s_waitcnt lgkmcnt(4)\n"
    "v_mfma_f32_16x16x32_bf16 v[96:99], v[172:175], v[44:47], v[96:99]\n"
    "s_waitcnt lgkmcnt(2)\n"
    "v_mfma_f32_16x16x32_bf16 v[100:103], v[176:179], v[44:47], v[100:103]\n"
    "s_waitcnt lgkmcnt(0)\n"
    "v_mfma_f32_16x16x32_bf16 v[104:107], v[180:183], v[44:47], v[104:107]\n"
    "s_nop 4\n"
    "s_cmp_eq_u32 s89, 1\n"
    "s_cbranch_scc1 .LkL_119_%=\n"
    "ds_write_b128 v12, v[100:103] offset:3072\n"
    "ds_write_b128 v12, v[104:107] offset:4096\n"
    "s_waitcnt lgkmcnt(0)\n"
    "s_barrier\n"
    "ds_read_b128 v[20:23], v12 offset:0\n"
    "ds_read_b64_tr_b16 v[24:25], v13 offset:51200\n"
    "ds_read_b128 v[28:31], v12 offset:1024\n"
    "ds_read_b64_tr_b16 v[32:33], v13 offset:53248\n"
    "ds_read_b128 v[36:39], v12 offset:2048\n"
    "ds_read_b64_tr_b16 v[40:41], v13 offset:55296\n"
    "s_waitcnt lgkmcnt(0)\n"
    "v_pk_add_f32 v[20:21], v[20:21], v[88:89]\n"
    "v_pk_add_f32 v[22:23], v[22:23], v[90:91]\n"
    "v_lshlrev_b32 v50, 16, v24\n"
    "v_and_b32 v51, 0xffff0000, v24\n"
    "v_pk_mul_f32 v[50:51], v[50:51], v[154:155] op_sel_hi:[1,0]\n"
    "v_pk_fma_f32 v[50:51], v[20:21], v[154:155], v[50:51] op_sel:[0,1,0] op_sel_hi:[1,1,1]\n"
    "v_cvt_pk_bf16_f32 v24, v50, v51\n"
    "v_lshlrev_b32 v50, 16, v25\n"
    "v_and_b32 v51, 0xffff0000, v25\n"
    "v_pk_mul_f32 v[50:51], v[50:51], v[154:155] op_sel_hi:[1,0]\n"
    "v_pk_fma_f32 v[50:51], v[22:23], v[154:155], v[50:51] op_sel:[0,1,0] op_sel_hi:[1,1,1]\n"
    "v_cvt_pk_bf16_f32 v25, v50, v51\n"
    "ds_bpermute_b32 v20, v14, v24\n"
    "ds_bpermute_b32 v21, v14, v25\n"
    "v_pk_add_f32 v[28:29], v[28:29], v[92:93]\n"
    "v_pk_add_f32 v[30:31], v[30:31], v[94:95]\n"
    "v_lshlrev_b32 v50, 16, v32\n"
    "v_and_b32 v51, 0xffff0000, v32\n"
    "v_pk_mul_f32 v[50:51], v[50:51], v[154:155] op_sel_hi:[1,0]\n"
    "v_pk_fma_f32 v[50:51], v[28:29], v[154:155], v[50:51] op_sel:[0,1,0] op_sel_hi:[1,1,1]\n"
    "v_cvt_pk_bf16_f32 v32, v50, v51\n"
    "v_lshlrev_b32 v50, 16, v33\n"
    "v_and_b32 v51, 0xffff0000, v33\n"
    "v_pk_mul_f32 v[50:51], v[50:51], v[154:155] op_sel_hi:[1,0]\n"
    "v_pk_fma_f32 v[50:51], v[30:31], v[154:155], v[50:51] op_sel:[0,1,0] op_sel_hi:[1,1,1]\n"
    "v_cvt_pk_bf16_f32 v33, v50, v51\n"
    "ds_bpermute_b32 v28, v14, v32\n"
    "ds_bpermute_b32 v29, v14, v33\n"
    "v_pk_add_f32 v[36:37], v[36:37], v[96:97]\n"
    "v_pk_add_f32 v[38:39], v[38:39], v[98:99]\n"
    "v_lshlrev_b32 v50, 16, v40\n"
    "v_and_b32 v51, 0xffff0000, v40\n"
    "v_pk_mul_f32 v[50:51], v[50:51], v[154:155] op_sel_hi:[1,0]\n"
    "v_pk_fma_f32 v[50:51], v[36:37], v[154:155], v[50:51] op_sel:[0,1,0] op_sel_hi:[1,1,1]\n"
    "v_cvt_pk_bf16_f32 v40, v50, v51\n"
    "v_lshlrev_b32 v50, 16, v41\n"
    "v_and_b32 v51, 0xffff0000, v41\n"
    "v_pk_mul_f32 v[50:51], v[50:51], v[154:155] op_sel_hi:[1,0]\n"
    "v_pk_fma_f32 v[50:51], v[38:39], v[154:155], v[50:51] op_sel:[0,1,0] op_sel_hi:[1,1,1]\n"
    "v_cvt_pk_bf16_f32 v41, v50, v51\n"
    "ds_bpermute_b32 v36, v14, v40\n"
    "ds_bpermute_b32 v37, v14, v41\n"
    "s_waitcnt lgkmcnt(4)\n"
    "buffer_store_dwordx2 v[20:21], v15, s[16:19], s40 offen offset:0\n"
    "s_waitcnt lgkmcnt(2)\n"
    "buffer_store_dwordx2 v[28:29], v15, s[16:19], s40 offen offset:32\n"
    "s_waitcnt lgkmcnt(0)\n"
    "buffer_store_dwordx2 v[36:37], v15, s[16:19], s40 offen offset:64\n"
    "s_branch .LkL_120_%=\n"
    ".LkL_119_%=:\n"
    "ds_write_b128 v12, v[88:91] offset:0\n"
    "ds_write_b128 v12, v[92:95] offset:1024\n"
    "ds_write_b128 v12, v[96:99] offset:2048\n"
    "s_waitcnt lgkmcnt(0)\n"
    "s_barrier\n"
    "ds_read_b128 v[20:23], v12 offset:3072\n"
    "ds_read_b64_tr_b16 v[24:25], v13 offset:57344\n"
    "ds_read_b128 v[28:31], v12 offset:4096\n"
    "ds_read_b64_tr_b16 v[32:33], v13 offset:59392\n"
    "s_waitcnt lgkmcnt(0)\n"
    "v_pk_add_f32 v[20:21], v[20:21], v[100:101]\n"
    "v_pk_add_f32 v[22:23], v[22:23], v[102:103]\n"
    "v_lshlrev_b32 v50, 16, v24\n"
    "v_and_b32 v51, 0xffff0000, v24\n"
    "v_pk_mul_f32 v[50:51], v[50:51], v[154:155] op_sel_hi:[1,0]\n"
    "v_pk_fma_f32 v[50:51], v[20:21], v[154:155], v[50:51] op_sel:[0,1,0] op_sel_hi:[1,1,1]\n"
    "v_cvt_pk_bf16_f32 v24, v50, v51\n"
    "v_lshlrev_b32 v50, 16, v25\n"
    "v_and_b32 v51, 0xffff0000, v25\n"
    "v_pk_mul_f32 v[50:51], v[50:51], v[154:155] op_sel_hi:[1,0]\n"
    "v_pk_fma_f32 v[50:51], v[22:23], v[154:155], v[50:51] op_sel:[0,1,0] op_sel_hi:[1,1,1]\n"
    "v_cvt_pk_bf16_f32 v25, v50, v51\n"
    "ds_bpermute_b32 v20, v14, v24\n"
    "ds_bpermute_b32 v21, v14, v25\n"
    "v_pk_add_f32 v[28:29], v[28:29], v[104:105]\n"
    "v_pk_add_f32 v[30:31], v[30:31], v[106:107]\n"
    "v_lshlrev_b32 v50, 16, v32\n"
    "v_and_b32 v51, 0xffff0000, v32\n"
    "v_pk_mul_f32 v[50:51], v[50:51], v[154:155] op_sel_hi:[1,0]\n"
    "v_pk_fma_f32 v[50:51], v[28:29], v[154:155], v[50:51] op_sel:[0,1,0] op_sel_hi:[1,1,1]\n"
    "v_cvt_pk_bf16_f32 v32, v50, v51\n"
    "v_lshlrev_b32 v50, 16, v33\n"
    "v_and_b32 v51, 0xffff0000, v33\n"
    "v_pk_mul_f32 v[50:51], v[50:51], v[154:155] op_sel_hi:[1,0]\n"
    "v_pk_fma_f32 v[50:51], v[30:31], v[154:155], v[50:51] op_sel:[0,1,0] op_sel_hi:[1,1,1]\n"
    "v_cvt_pk_bf16_f32 v33, v50, v51\n"
    "ds_bpermute_b32 v28, v14, v32\n"
    "ds_bpermute_b32 v29, v14, v33\n"
    "s_waitcnt lgkmcnt(2)\n"
    "buffer_store_dwordx2 v[20:21], v15, s[16:19], s40 offen offset:96\n"
    "s_waitcnt lgkmcnt(0)\n"
    "buffer_store_dwordx2 v[28:29], v15, s[16:19], s40 offen offset:128\n"
    ".LkL_120_%=:\n"
    "s_or_b32 s38, s2, s24\n"
    "s_cmp_lg_u32 s38, 0\n"
    "s_cbranch_scc1 .LkL_121_%=\n"
    "v_mov_b32 v50, s36\n"
    "v_mov_b32 v51, s37\n"
    "buffer_store_dwordx2 v[50:51], v254, s[12:15], 0 offen sc1\n"
    ".LkL_121_%=:\n"
    "s_branch .Lkend_2_%=\n"
    ".Lkfall_82_%=:\n"
    "s_nop 15\n"
    "s_waitcnt vmcnt(0)\n"
    "v_mbcnt_lo_u32_b32 v172, -1, 0\n"
    "v_mbcnt_hi_u32_b32 v172, -1, v172\n"
    "v_and_b32 v173, 31, v172\n"
    "v_lshrrev_b32 v174, 5, v172\n"
    "s_mov_b32 s39, 0\n"
    "v_mov_b32 v160, v242\n"
    "v_mov_b32 v161, v243\n"
    "s_lshl_b32 s40, s24, 10\n"
    "s_add_u32 s40, s40, 112640\n"
    "v_lshlrev_b32 v162, 2, v173\n"
    "v_add_u32 v162, s40, v162\n"
    ".LkL_122_%=:\n"
    "ds_read_b128 v[192:195], v160\n"
    "ds_read_b64 v[196:197], v161 offset:32\n"
    "v_mov_b32 v163, 0xff800000\n"
    "s_waitcnt lgkmcnt(0)\n"
    "v_mfma_f32_32x32x16_bf16 v[84:99], v[128:131], v[192:195], 0\n"
    "v_mfma_f32_32x32x8_bf16 v[84:99], v[132:133], v[196:197], v[84:99]\n"
    "s_nop 15\n"
    "v_max3_f32 v163, v163, v84, v85\n"
    "v_max3_f32 v163, v163, v86, v87\n"
    "v_max3_f32 v163, v163, v88, v89\n"
    "v_max3_f32 v163, v163, v90, v91\n"
    "v_max3_f32 v163, v163, v92, v93\n"
    "v_max3_f32 v163, v163, v94, v95\n"
    "v_max3_f32 v163, v163, v96, v97\n"
    "v_max3_f32 v163, v163, v98, v99\n"
    "v_mfma_f32_32x32x16_bf16 v[84:99], v[142:145], v[192:195], 0\n"
    "v_mfma_f32_32x32x8_bf16 v[84:99], v[146:147], v[196:197], v[84:99]\n"
    "s_nop 15\n"
    "v_max3_f32 v163, v163, v84, v85\n"
    "v_max3_f32 v163, v163, v86, v87\n"
    "v_max3_f32 v163, v163, v88, v89\n"
    "v_max3_f32 v163, v163, v90, v91\n"
    "v_max3_f32 v163, v163, v92, v93\n"
    "v_max3_f32 v163, v163, v94, v95\n"
    "v_max3_f32 v163, v163, v96, v97\n"
    "v_max3_f32 v163, v163, v98, v99\n"
    "v_mfma_f32_32x32x16_bf16 v[84:99], v[0:3], v[192:195], 0\n"
    "v_mfma_f32_32x32x8_bf16 v[84:99], v[4:5], v[196:197], v[84:99]\n"
    "s_nop 15\n"
    "v_max3_f32 v163, v163, v84, v85\n"
    "v_max3_f32 v163, v163, v86, v87\n"
    "v_max3_f32 v163, v163, v88, v89\n"
    "v_max3_f32 v163, v163, v90, v91\n"
    "v_max3_f32 v163, v163, v92, v93\n"
    "v_max3_f32 v163, v163, v94, v95\n"
    "v_max3_f32 v163, v163, v96, v97\n"
    "v_max3_f32 v163, v163, v98, v99\n"
    "v_mfma_f32_32x32x16_bf16 v[84:99], v[14:17], v[192:195], 0\n"
    "v_mfma_f32_32x32x8_bf16 v[84:99], v[18:19], v[196:197], v[84:99]\n"
    "s_nop 15\n"
    "v_max3_f32 v163, v163, v84, v85\n"
    "v_max3_f32 v163, v163, v86, v87\n"
    "v_max3_f32 v163, v163, v88, v89\n"
    "v_max3_f32 v163, v163, v90, v91\n"
    "v_max3_f32 v163, v163, v92, v93\n"
    "v_max3_f32 v163, v163, v94, v95\n"
    "v_max3_f32 v163, v163, v96, v97\n"
    "v_max3_f32 v163, v163, v98, v99\n"
    "v_mfma_f32_32x32x16_bf16 v[84:99], v[28:31], v[192:195], 0\n"
    "v_mfma_f32_32x32x8_bf16 v[84:99], v[32:33], v[196:197], v[84:99]\n"
    "s_nop 15\n"
    "v_max3_f32 v163, v163, v84, v85\n"
    "v_max3_f32 v163, v163, v86, v87\n"
    "v_max3_f32 v163, v163, v88, v89\n"
    "v_max3_f32 v163, v163, v90, v91\n"
    "v_max3_f32 v163, v163, v92, v93\n"
    "v_max3_f32 v163, v163, v94, v95\n"
    "v_max3_f32 v163, v163, v96, v97\n"
    "v_max3_f32 v163, v163, v98, v99\n"
    "v_mfma_f32_32x32x16_bf16 v[84:99], v[42:45], v[192:195], 0\n"
    "v_mfma_f32_32x32x8_bf16 v[84:99], v[46:47], v[196:197], v[84:99]\n"
    "s_nop 15\n"
    "v_max3_f32 v163, v163, v84, v85\n"
    "v_max3_f32 v163, v163, v86, v87\n"
    "v_max3_f32 v163, v163, v88, v89\n"
    "v_max3_f32 v163, v163, v90, v91\n"
    "v_max3_f32 v163, v163, v92, v93\n"
    "v_max3_f32 v163, v163, v94, v95\n"
    "v_max3_f32 v163, v163, v96, v97\n"
    "v_max3_f32 v163, v163, v98, v99\n"
    "v_mfma_f32_32x32x16_bf16 v[84:99], v[56:59], v[192:195], 0\n"
    "v_mfma_f32_32x32x8_bf16 v[84:99], v[60:61], v[196:197], v[84:99]\n"
    "s_nop 15\n"
    "v_max3_f32 v163, v163, v84, v85\n"
    "v_max3_f32 v163, v163, v86, v87\n"
    "v_max3_f32 v163, v163, v88, v89\n"
    "v_max3_f32 v163, v163, v90, v91\n"
    "v_max3_f32 v163, v163, v92, v93\n"
    "v_max3_f32 v163, v163, v94, v95\n"
    "v_max3_f32 v163, v163, v96, v97\n"
    "v_max3_f32 v163, v163, v98, v99\n"
    "v_mfma_f32_32x32x16_bf16 v[84:99], v[70:73], v[192:195], 0\n"
    "v_mfma_f32_32x32x8_bf16 v[84:99], v[74:75], v[196:197], v[84:99]\n"
    "s_nop 15\n"
    "v_max3_f32 v163, v163, v84, v85\n"
    "v_max3_f32 v163, v163, v86, v87\n"
    "v_max3_f32 v163, v163, v88, v89\n"
    "v_max3_f32 v163, v163, v90, v91\n"
    "v_max3_f32 v163, v163, v92, v93\n"
    "v_max3_f32 v163, v163, v94, v95\n"
    "v_max3_f32 v163, v163, v96, v97\n"
    "v_max3_f32 v163, v163, v98, v99\n"
    "v_mov_b32 v164, v163\n"
    "s_nop 1\n"
    "v_permlane32_swap_b32 v163, v164\n"
    "v_max_f32 v163, v163, v164\n"
    "ds_write_b32 v162, v163 offset:1024\n"
    "s_movk_i32 s41, 6144\n"
    "s_bitcmp1_b32 s39, 0\n"
    "s_cselect_b32 s41, -6096, s41\n"
    "v_add_u32 v160, s41, v160\n"
    "v_add_u32 v161, s41, v161\n"
    "v_add_u32 v162, 128, v162\n"
    "s_add_u32 s39, s39, 1\n"
    "s_cmp_lt_u32 s39, 8\n"
    "s_cbranch_scc1 .LkL_122_%=\n"
    "s_waitcnt lgkmcnt(0)\n"
    "s_barrier\n"
    "s_cmp_ge_u32 s24, 4\n"
    "s_cbranch_scc1 .LkL_123_%=\n"
    "s_lshl_b32 s40, s24, 8\n"
    "s_add_u32 s40, s40, 112640\n"
    "v_lshl_add_u32 v160, v172, 2, s40\n"
    "ds_read_b32 v84, v160 offset:1024\n"
    "ds_read_b32 v85, v160 offset:2048\n"
    "ds_read_b32 v86, v160 offset:3072\n"
    "ds_read_b32 v87, v160 offset:4096\n"
    "ds_read_b32 v88, v160 offset:5120\n"
    "ds_read_b32 v89, v160 offset:6144\n"
    "ds_read_b32 v90, v160 offset:7168\n"
    "ds_read_b32 v91, v160 offset:8192\n"
    "s_waitcnt lgkmcnt(0)\n"
    "v_max3_f32 v84, v84, v85, v86\n"
    "v_max3_f32 v87, v87, v88, v89\n"
    "v_max3_f32 v84, v84, v90, v91\n"
    "v_max_f32 v84, v84, v87\n"
    "v_sub_f32 v84, 0, v84\n"
    "v_cvt_pk_bf16_f32 v85, v84, v84\n"
    "v_and_b32 v86, 0xffff0000, v85\n"
    "v_sub_f32 v84, v84, v86\n"
    "v_cvt_pk_bf16_f32 v87, v84, v84\n"
    "v_and_b32 v86, 0xffff0000, v87\n"
    "v_sub_f32 v84, v84, v86\n"
    "v_cvt_pk_bf16_f32 v89, v84, 0\n"
    "v_alignbit_b32 v88, v87, v85, 16\n"
    "s_mul_i32 s40, s24, 48\n"
    "s_add_u32 s40, s40, 49192\n"
    "v_mul_u32_u24 v160, 192, v172\n"
    "v_add_u32 v160, s40, v160\n"
    "ds_write_b32 v160, v88\n"
    "ds_write_b16 v160, v89 offset:4\n"
    ".LkL_123_%=:\n"
    "s_waitcnt lgkmcnt(0)\n"
    "s_barrier\n"
    "s_setprio 3\n"
    "s_branch .Lkstart_81_%=\n"
    ".Lkend_2_%=:\n"
    :
    : "{s[0:1]}"(__builtin_amdgcn_kernarg_segment_ptr()), "{s2}"(blockIdx.x), "{v0}"(threadIdx.x)
    : "v1", "v2", "v3", "v4", "v5", "v6", "v7", "v8", "v9", "v10", "v11", "v12", "v13", "v14", "v15", "v16", "v17", "v18", "v19", "v20", "v21", "v22", "v23", "v24", "v25", "v26", "v27", "v28", "v29", "v30", "v31", "v32", "v33", "v34", "v35", "v36", "v37", "v38", "v39", "v40", "v41", "v42", "v43", "v44", "v45", "v46", "v47", "v48", "v49", "v50", "v51", "v52", "v53", "v54", "v55", "v56", "v57", "v58", "v59", "v60", "v61", "v62", "v63", "v64", "v65", "v66", "v67", "v68", "v69", "v70", "v71", "v72", "v73", "v74", "v75", "v76", "v77", "v78", "v79", "v80", "v81", "v82", "v83", "v84", "v85", "v86", "v87", "v88", "v89", "v90", "v91", "v92", "v93", "v94", "v95", "v96", "v97", "v98", "v99", "v100", "v101", "v102", "v103", "v104", "v105", "v106", "v107", "v108", "v109", "v110", "v111", "v112", "v113", "v114", "v115", "v116", "v117", "v118", "v119", "v120", "v121", "v122", "v123", "v124", "v125", "v126", "v127", "v128", "v129", "v130", "v131", "v132", "v133", "v134", "v135", "v136", "v137", "v138", "v139", "v140", "v141", "v142", "v143", "v144", "v145", "v146", "v147", "v148", "v149", "v150", "v151", "v152", "v153", "v154", "v155", "v156", "v157", "v158", "v159", "v160", "v161", "v162", "v163", "v164", "v165", "v166", "v167", "v168", "v169", "v170", "v171", "v172", "v173", "v174", "v175", "v176", "v177", "v178", "v179", "v180", "v181", "v182", "v183", "v184", "v185", "v186", "v187", "v188", "v189", "v190", "v191", "v192", "v193", "v194", "v195", "v196", "v197", "v198", "v199", "v200", "v201", "v202", "v203", "v204", "v205", "v206", "v207", "v208", "v209", "v210", "v211", "v212", "v213", "v214", "v215", "v216", "v217", "v218", "v219", "v220", "v221", "v222", "v223", "v224", "v225", "v226", "v227", "v228", "v229", "v230", "v231", "v232", "v233", "v234", "v235", "v236", "v237", "v238", "v239", "v240", "v241", "v242", "v243", "v244", "v245", "v246", "v247", "v248", "v249", "v250", "v251", "v252", "v253", "v254", "v255", "s3", "s4", "s5", "s6", "s7", "s8", "s9", "s10", "s11", "s12", "s13", "s14", "s15", "s16", "s17", "s18", "s19", "s20", "s21", "s22", "s23", "s24", "s25", "s26", "s27", "s28", "s29", "s30", "s31", "s33", "s34", "s35", "s36", "s37", "s38", "s39", "s40", "s41", "s42", "s43", "s44", "s45", "s46", "s47", "s48", "s49", "s50", "s51", "s52", "s53", "s54", "s55", "s56", "s57", "s58", "s59", "s60", "s61", "s62", "s63", "s64", "s65", "s66", "s67", "s68", "s69", "s70", "s71", "s72", "s73", "s74", "s75", "s76", "s77", "s78", "s79", "s80", "s81", "s82", "s83", "s84", "s85", "s86", "s87", "s88", "s89", "s90", "s91", "s92", "s93", "s94", "s95", "s96", "s97", "s98", "s99", "vcc", "scc", "memory");
}

extern "C" void kernel_launch(void* const* d_in, const int* in_sizes, int n_in, void* d_out, int out_size, void* d_ws,
                              size_t ws_size, hipStream_t stream) {
  Params p;
  p.x = d_in[0]; p.are = d_in[1]; p.aim = d_in[2]; p.bre = d_in[3]; p.bim = d_in[4]; p.cre = d_in[5]; p.cim = d_in[6];
  p.logdt = d_in[7]; p.dskip = d_in[8]; p.wq = d_in[10]; p.wk = d_in[11]; p.wv = d_in[12]; p.wo = d_in[13]; p.gw = d_in[14];
  p.out = d_out; p.ws = d_ws;
  (void)hipFuncSetAttribute((const void*)fused_kernel, hipFuncAttributeMaxDynamicSharedMemorySize, 163840);
  fused_kernel<<<256, 512, 163840, stream>>>(p);
}
```
